```python
import torch

def _softmax(x, axis=-1):
    return torch.softmax(x, dim=axis)
def _gelu(x):
    return torch.nn.functional.gelu(x, approximate="tanh")
def _silu(x):
    return torch.nn.functional.silu(x)
def _sigmoid(x):
    return torch.sigmoid(x)
def _layernorm(x, axis=-1, eps=1e-5):
    m = x.mean(dim=axis, keepdim=True)
    v = x.var(dim=axis, keepdim=True, unbiased=False)
    return (x - m) / torch.sqrt(v + eps)
def _scatter_add(tgt, idx, src, axis=0):
    return tgt.clone().index_add_(axis, idx, src)
def _segment_sum(src, seg, n):
    out = torch.zeros((n,) + src.shape[1:], dtype=src.dtype)
    return out.index_add_(0, seg, src)
def _pad(x, pw):
    flat = tuple(v for pair in reversed(pw) for v in pair)
    return torch.nn.functional.pad(x, flat)

kernel_name = 'standalone__gqa_block_cached__near_ridge'

def setup_inputs():

    _w = (lambda a: (lambda t: t.to(torch.bfloat16) if t.is_floating_point() else t)(torch.as_tensor(a)))
    B, Sq, D = 32, 112, 640
    Hq, Hkv, Sk, Dh = 32, 32, 512, 128
    assert Hq % Hkv == 0, (Hq, Hkv)
    return {
        "x": _w((lambda *s: torch.randn(*s, dtype=torch.bfloat16) * 0.8)(B, Sq, D)),
        "k_cache": _w((lambda *s: torch.randn(*s, dtype=torch.bfloat16) * 0.8)(B, Hkv, Sk, Dh)),
        "v_cache": _w((lambda *s: torch.randn(*s, dtype=torch.bfloat16) * 0.05)(B, Hkv, Sk, Dh)),
        "wq": _w((lambda *s: torch.randn(*s, dtype=torch.bfloat16) * 0.8)(D, Hq * Dh) * (D ** -0.5)),
        "wo": _w((lambda *s: torch.randn(*s, dtype=torch.bfloat16) * 0.05)(Hq * Dh, D) * ((Hq * Dh) ** -0.5)),
    }

def reference(x, k_cache, v_cache, wq, wo):
    B, Sq, D = x.shape
    Hq, Dh = 32, 128
    Hkv = k_cache.shape[1]
    rep = Hq // Hkv
    q = (lambda x, *d: x.permute(*d))((x @ wq).reshape(B, Sq, Hq, Dh), 0, 2, 1, 3)
    k = (lambda x, n, axis: torch.repeat_interleave(x, n, dim=axis))(k_cache, rep, axis=1)
    v = (lambda x, n, axis: torch.repeat_interleave(x, n, dim=axis))(v_cache, rep, axis=1)
    scores = torch.einsum("bhqd,bhkd->bhqk", q, k) * (Dh ** -0.5)
    p = _softmax(scores, axis=-1)
    o = torch.einsum("bhqk,bhkd->bhqd", p, v)
    return (lambda x, *d: x.permute(*d))(o, 0, 2, 1, 3).reshape(B, Sq, Hq * Dh) @ wo
```

```cpp
#include <hip/hip_runtime.h>
#include <hip/hip_bf16.h>

typedef __bf16 bf16x8 __attribute__((ext_vector_type(8)));
typedef __bf16 bf16x4 __attribute__((ext_vector_type(4)));
typedef __bf16 bf16x2 __attribute__((ext_vector_type(2)));
typedef float f32x16 __attribute__((ext_vector_type(16)));
typedef float f32x4 __attribute__((ext_vector_type(4)));
typedef float f32x2 __attribute__((ext_vector_type(2)));
typedef unsigned u32x4 __attribute__((ext_vector_type(4)));
typedef unsigned u32x2 __attribute__((ext_vector_type(2)));
typedef __amdgpu_buffer_rsrc_t srd_t;
#define LAS __attribute__((address_space(3)))
#define DI __device__ __forceinline__
#define MFMA32(a, b, c) __builtin_amdgcn_mfma_f32_32x32x16_bf16((a), (b), (c), 0, 0, 0)
#define MFMA16(a, b, c) __builtin_amdgcn_mfma_f32_16x16x32_bf16((a), (b), (c), 0, 0, 0)

#define KV_POL 1

constexpr int NB = 32, SQ = 112, DM = 640, NH = 32, SK = 512, DH = 128;
constexpr unsigned KV_BYTES = 134217728u, X_BYTES = 3584u * 1280u, W_BYTES = 640u * 8192u;
constexpr unsigned O_ITEM = 28672u, O_BYTES = 1024u * O_ITEM;
constexpr int RING = 8;
constexpr unsigned FLAG_OFF = RING * 16384u, LDS1 = FLAG_OFF + 128u;
constexpr int TILES_PER_ITEM = 36, NTILES = 4 * TILES_PER_ITEM;
constexpr unsigned SPIN_CAP = 1u << 22;
constexpr float QSCALE = 0.12751743082f;
constexpr float THR = 8.0f;

DI unsigned rfl(unsigned x) { return __builtin_amdgcn_readfirstlane(x); }
DI unsigned cvtpk(float lo, float hi) { f32x2 v = {lo, hi}; bf16x2 b = __builtin_convertvector(v, bf16x2); return __builtin_bit_cast(unsigned, b); }
template <int N> DI void waitv() { asm volatile("s_waitcnt vmcnt(%c0)" ::"i"(N) : "memory"); }
template <int POL> DI void dma4(unsigned m0v, const unsigned (&v)[4], srd_t srd, unsigned soff) {
#define D4(P) asm volatile("s_mov_b32 m0, %0\n\ts_nop 3\n\tbuffer_load_dwordx4 %1, %5, %6 offen " P " lds\n\tbuffer_load_dwordx4 %2, %5, %6 offen offset:1024 " P " lds\n\t" \
                           "buffer_load_dwordx4 %3, %5, %6 offen offset:2048 " P " lds\n\tbuffer_load_dwordx4 %4, %5, %6 offen offset:3072 " P " lds"                        \
                           ::"s"(m0v), "v"(v[0]), "v"(v[1]), "v"(v[2]), "v"(v[3]), "s"(srd), "s"(soff) : "m0", "memory")
  if constexpr (POL == 0) D4(""); else D4("nt");
#undef D4
}
DI void dma1(unsigned m0v, unsigned v, srd_t srd, unsigned soff) {
  asm volatile("s_mov_b32 m0, %0\n\ts_nop 3\n\tbuffer_load_dwordx4 %1, %2, %3 offen lds" ::"s"(m0v), "v"(v), "s"(srd), "s"(soff) : "m0", "memory");
}
DI bf16x8 lds_b128(unsigned a) { return *(const LAS bf16x8*)(size_t)a; }
DI bf16x8 lds_tr2(unsigned a0, unsigned a1) {
  bf16x4 lo = __builtin_amdgcn_ds_read_tr16_b64_v4bf16((LAS bf16x4*)(size_t)a0);
  bf16x4 hi = __builtin_amdgcn_ds_read_tr16_b64_v4bf16((LAS bf16x4*)(size_t)a1);
  return __builtin_shufflevector(lo, hi, 0, 1, 2, 3, 4, 5, 6, 7);
}
DI unsigned min4(unsigned a) {
  u32x4 v;
  asm volatile("ds_read_b128 %0, %1\n\ts_waitcnt lgkmcnt(0)" : "=v"(v) : "v"(a) : "memory");
  unsigned m = min(min(v[0], v[1]), min(v[2], v[3]));
  return rfl(m);
}
DI void lds_put(unsigned a, unsigned val) { asm volatile("ds_write_b32 %0, %1" ::"v"(a), "v"(val) : "memory"); }
DI bf16x8 pack8(const f32x16& x, int o, float s) {
  u32x4 w = {cvtpk(x[o] * s, x[o + 1] * s), cvtpk(x[o + 2] * s, x[o + 3] * s), cvtpk(x[o + 4] * s, x[o + 5] * s), cvtpk(x[o + 6] * s, x[o + 7] * s)};
  return __builtin_bit_cast(bf16x8, w);
}
DI bf16x8 pack8n(const f32x16& x, int o) {
  u32x4 w = {cvtpk(x[o], x[o + 1]), cvtpk(x[o + 2], x[o + 3]), cvtpk(x[o + 4], x[o + 5]), cvtpk(x[o + 6], x[o + 7])};
  return __builtin_bit_cast(bf16x8, w);
}
DI float xhalf_max(float v) { auto r = __builtin_amdgcn_permlane32_swap(__float_as_uint(v), __float_as_uint(v), false, false); return fmaxf(__uint_as_float(r[0]), __uint_as_float(r[1])); }
DI float xhalf_sum(float v) { auto r = __builtin_amdgcn_permlane32_swap(__float_as_uint(v), __float_as_uint(v), false, false); return __uint_as_float(r[0]) + __uint_as_float(r[1]); }

DI void map_block(unsigned g, unsigned& b, unsigned& h0, unsigned& rot) {
  const unsigned xcd = g & 7u, sl = g >> 3, j = sl & 3u, bi = sl >> 2;
  b = (xcd >> 1) * 8u + bi; h0 = ((xcd & 1u) * 4u + j) * 4u;
  rot = ((bi * 10u) / 8u + (j * 10u) / 4u) % 10u;
}

__global__ __launch_bounds__(512, 2) void attn_k(const void* __restrict__ Xp, const void* __restrict__ Kp, const void* __restrict__ Vp,
                                                 const void* __restrict__ Wq, void* __restrict__ Ows) {
  extern __shared__ __attribute__((aligned(16))) char lds[];
  const unsigned ldsb = (unsigned)(size_t)lds;
  const unsigned l = threadIdx.x & 63u, wid = rfl(threadIdx.x >> 6), w4 = wid & 3u;
  unsigned b, h0, rot;
  map_block(blockIdx.x, b, h0, rot);
  const unsigned a_full = ldsb + FLAG_OFF, a_free = ldsb + FLAG_OFF + 64u;
  if (threadIdx.x < 32) lds_put(a_full + threadIdx.x * 4u, 0u);
  asm volatile("s_waitcnt lgkmcnt(0)" ::: "memory");
  __builtin_amdgcn_s_barrier();

  if (wid >= 4) {
    srd_t sK = __builtin_amdgcn_make_buffer_rsrc((void*)Kp, (short)0, (int)KV_BYTES, 0x00020000);
    srd_t sV = __builtin_amdgcn_make_buffer_rsrc((void*)Vp, (short)0, (int)KV_BYTES, 0x00020000);
    srd_t sX = __builtin_amdgcn_make_buffer_rsrc((void*)Xp, (short)0, (int)X_BYTES, 0x00020000);
    srd_t sW = __builtin_amdgcn_make_buffer_rsrc((void*)Wq, (short)0, (int)W_BYTES, 0x00020000);
    unsigned vk[4], vv[4], vw[4], vx[4];
#pragma unroll
    for (int i = 0; i < 4; ++i) {
      vk[i] = (l >> 4) * 256u + (((l & 15u) ^ ((4u * i + (l >> 4)) & 15u)) * 16u) + w4 * 4096u;
      vv[i] = (l >> 4) * 256u + (((l & 15u) ^ ((l >> 4) << 2)) * 16u) + w4 * 4096u;
      vw[i] = (l >> 4) * 8192u + (((l & 15u) ^ ((l >> 4) << 2)) * 16u) + i * (32768u - 1024u) + w4 * 131072u;
      vx[i] = (l >> 3) * 1280u + (((l & 7u) ^ ((4u * i + (l >> 4)) & 7u)) * 16u) + i * (10240u - 1024u) + w4 * 40960u;
    }
    const unsigned myfull = a_full + w4 * 4u, m0b = ldsb + w4 * 4096u;
    unsigned t = 0, freec = 0, pub = 0;
    auto pre = [&]() {
      if (t >= (unsigned)RING && freec + RING <= t) {
        freec = min4(a_free);
        if (freec + RING <= t) {
          waitv<0>(); lds_put(myfull, t); pub = t;
          unsigned sp = 0;
          do { __builtin_amdgcn_s_sleep(1); freec = min4(a_free); if (++sp > SPIN_CAP) freec = 0x40000000u; } while (freec + RING <= t);
        }
      }
    };
    auto post = [&]() { waitv<8>(); if (t > pub + 1u) { pub = t - 1u; lds_put(myfull, pub); } ++t; };
    for (unsigned r = 0; r < 4; ++r) {
      const unsigned h = h0 + r;
      unsigned c = rot;
      for (unsigned i = 0; i < 10; ++i) {
        pre(); dma4<0>(m0b + (t & (RING - 1)) * 16384u, vw, sW, c * 524288u + h * 256u);
        waitv<8>(); ++t;
        pre(); dma4<0>(m0b + (t & (RING - 1)) * 16384u, vx, sX, b * 143360u + c * 128u);
        post();
        c = c == 9u ? 0u : c + 1u;
      }
      const unsigned kvo = (b * 32u + h) * 131072u;
      for (unsigned kt = 0; kt < 8; ++kt) {
        pre(); dma4<KV_POL>(m0b + (t & (RING - 1)) * 16384u, vk, sK, kvo + kt * 16384u);
        post();
        pre(); dma4<KV_POL>(m0b + (t & (RING - 1)) * 16384u, vv, sV, kvo + kt * 16384u);
        post();
      }
    }
    waitv<4>(); lds_put(myfull, t - 1u);
    waitv<0>(); lds_put(myfull, t);
    return;
  }

  const unsigned hh = l >> 5, l31 = l & 31u, q4 = (l >> 2) & 3u, blk = (l >> 4) & 1u, p4 = l & 3u;
  const unsigned sp4 = ((p4 & 1u) << 1) | (p4 >> 1);
  unsigned lw[4], lv[4];
#pragma unroll
  for (int c = 0; c < 4; ++c) {
    lw[c] = ldsb + 256u * (8u * hh + q4) + 64u * ((unsigned)c ^ q4) + 32u * blk + 8u * sp4;
    lv[c] = ldsb + 256u * (4u * hh + q4) + 64u * ((unsigned)c ^ q4) + 32u * blk + 8u * p4;
  }
  const unsigned xrow = w4 * 32u + l31;
  const unsigned lx = ldsb + 128u * xrow + 16u * (hh ^ ((xrow >> 1) & 7u));
  const unsigned lk = ldsb + 256u * l31 + 16u * (hh ^ (l31 & 15u));
  const unsigned myfree = a_free + w4 * 4u;
  srd_t sO = __builtin_amdgcn_make_buffer_rsrc(Ows, (short)0, (int)O_BYTES, 0x00020000);
  const unsigned qb16 = 2u * w4 + blk;
  const unsigned vo = qb16 < 7u ? qb16 * 4096u + hh * 256u + (l & 15u) * 16u : 0x80000000u;
  unsigned t = 0, fullc = 0;
  auto wait_full = [&](unsigned tt) {
    if (fullc <= tt) {
      unsigned sp = 0;
      while (true) { fullc = min4(a_full); if (fullc > tt) break; if (++sp > SPIN_CAP) { fullc = 0x40000000u; break; } }
    }
    asm volatile("" ::: "memory");
  };

  for (unsigned r = 0; r < 4; ++r) {
    f32x16 acc[4];
#pragma unroll
    for (int c = 0; c < 4; ++c) acc[c] = f32x16{};
    for (unsigned i = 0; i < 10; ++i) {
      wait_full(t + 1u);
      const unsigned sw = (t & (RING - 1)) * 16384u, sx = ((t + 1u) & (RING - 1)) * 16384u;
#pragma unroll
      for (int s = 0; s < 4; ++s) {
        const bf16x8 xb = lds_b128((lx + sx) ^ (unsigned)(s << 5));
#pragma unroll
        for (int c = 0; c < 4; ++c) {
          const unsigned a = lw[c] + sw + 4096u * s;
          acc[c] = MFMA32(lds_tr2(a, a + 1024u), xb, acc[c]);
        }
      }
      t += 2u;
      lds_put(myfree, t);
    }
    bf16x8 qf[8];
#pragma unroll
    for (int c = 0; c < 4; ++c) { qf[2 * c] = pack8(acc[c], 0, QSCALE); qf[2 * c + 1] = pack8(acc[c], 8, QSCALE); }
#pragma unroll
    for (int c = 0; c < 4; ++c) acc[c] = f32x16{};
    float m = 0.f, lsum = 0.f;
    for (unsigned kt = 0; kt < 8; ++kt) {
      wait_full(t);
      const unsigned sk = lk + (t & (RING - 1)) * 16384u;
      f32x16 s0, s1;
#pragma unroll
      for (int i = 0; i < 16; ++i) { s0[i] = -m; s1[i] = -m; }
#pragma unroll
      for (int s = 0; s < 8; ++s) {
        const unsigned a = sk ^ (unsigned)(s << 5);
        s0 = MFMA32(lds_b128(a), qf[s], s0);
        s1 = MFMA32(lds_b128(a + 8192u), qf[s], s1);
      }
      lds_put(myfree, t + 1u);
      float mx = fmaxf(s0[0], s1[0]);
#pragma unroll
      for (int i = 1; i < 16; ++i) mx = fmaxf(mx, fmaxf(s0[i], s1[i]));
      mx = xhalf_max(mx);
      if (kt == 0 || __any(mx > THR)) {
        const float d = kt == 0 ? mx : fmaxf(mx, 0.f);
        const float al = __builtin_amdgcn_exp2f(-d);
        m += d;
#pragma unroll
        for (int i = 0; i < 16; ++i) { s0[i] -= d; s1[i] -= d; }
        if (kt != 0) {
          lsum *= al;
#pragma unroll
          for (int c = 0; c < 4; ++c)
#pragma unroll
            for (int i = 0; i < 16; ++i) acc[c][i] *= al;
        }
      }
      float ps = 0.f;
#pragma unroll
      for (int i = 0; i < 16; ++i) { s0[i] = __builtin_amdgcn_exp2f(s0[i]); s1[i] = __builtin_amdgcn_exp2f(s1[i]); ps += s0[i] + s1[i]; }
      lsum += ps;
      bf16x8 pf[4] = {pack8n(s0, 0), pack8n(s0, 8), pack8n(s1, 0), pack8n(s1, 8)};
      wait_full(t + 1u);
      const unsigned sv = ((t + 1u) & (RING - 1)) * 16384u;
#pragma unroll
      for (int ks = 0; ks < 4; ++ks)
#pragma unroll
        for (int c = 0; c < 4; ++c) {
          const unsigned a = lv[c] + sv + 4096u * ks;
          acc[c] = MFMA32(lds_tr2(a, a + 2048u), pf[ks], acc[c]);
        }
      t += 2u;
      lds_put(myfree, t);
    }
    const float inv = __builtin_amdgcn_rcpf(xhalf_sum(lsum));
    const unsigned so = vo + (b * 32u + h0 + r) * O_ITEM;
#pragma unroll
    for (int c = 0; c < 4; ++c) {
      __builtin_amdgcn_raw_buffer_store_b128(__builtin_bit_cast(u32x4, pack8(acc[c], 0, inv)), sO, so + c * 1024u, 0, 0);
      __builtin_amdgcn_raw_buffer_store_b128(__builtin_bit_cast(u32x4, pack8(acc[c], 8, inv)), sO, so + c * 1024u + 512u, 0, 0);
    }
  }
}

constexpr unsigned K2_BUF = 12288u, K2_WAVE = 3u * K2_BUF, LDS2 = 4u * K2_WAVE;
__global__ __launch_bounds__(256, 1) void oproj_k(const void* __restrict__ Ows, const void* __restrict__ Wo, __hip_bfloat16* __restrict__ out) {
  extern __shared__ __attribute__((aligned(16))) char lds[];
  const unsigned ldsb = (unsigned)(size_t)lds;
  const unsigned l = threadIdx.x & 63u, w = rfl(threadIdx.x >> 6);
  const unsigned g = blockIdx.x, xcd = g & 7u, sl = g >> 3, bi = sl >> 3, b = xcd * 4u + bi, j = sl & 7u;
  srd_t sO = __builtin_amdgcn_make_buffer_rsrc((void*)Ows, (short)0, (int)O_BYTES, 0x00020000);
  srd_t sW = __builtin_amdgcn_make_buffer_rsrc((void*)Wo, (short)0, (int)W_BYTES, 0x00020000);
  unsigned vwo[5], vo[4];
#pragma unroll
  for (int i = 0; i < 5; ++i) { const unsigned c = 64u * i + l; vwo[i] = (c / 10u) * 1280u + (c % 10u) * 16u + j * 160u - (i < 4 ? 1024u * i : 0u); }
#pragma unroll
  for (int i = 0; i < 4; ++i) vo[i] = l * 16u + i * (4096u - 1024u);
  const unsigned vw4[4] = {vwo[0], vwo[1], vwo[2], vwo[3]};
  const unsigned base = ldsb + w * K2_WAVE;
  const unsigned rot = (j + 2u * bi) & 7u;
  auto issue = [&](unsigned u, unsigned buf) {
    const unsigned h = w * 8u + (((u >> 2) + rot) & 7u), ks = u & 3u;
    const unsigned m = base + buf * K2_BUF;
    const unsigned so = (b * 32u + h) * O_ITEM + ks * 1024u;
    const unsigned sw = (h * 128u + ks * 32u) * 1280u;
    dma4<0>(m, vw4, sW, sw); dma1(m + 4096u, vwo[4], sW, sw);
    dma4<0>(m + 5120u, vo, sO, so);
#pragma unroll
    for (int i = 0; i < 3; ++i) dma1(m + 5120u + 4096u + 1024u * i, vo[0], sO, so + (4u + i) * 4096u);
  };
  f32x4 acc[5][7];
#pragma unroll
  for (int n = 0; n < 5; ++n)
#pragma unroll
    for (int q = 0; q < 7; ++q) acc[n][q] = f32x4{};
  const unsigned gq = l >> 4, q4 = (l >> 2) & 3u, p4 = l & 3u;
  const unsigned la = 160u * (16u * (gq >> 1) + 4u * (gq & 1u) + q4) + 8u * p4;
  issue(0, 0); issue(1, 1);
  unsigned buf = 0;
  for (unsigned u = 0; u < 32; ++u) {
    const unsigned nb = buf == 0 ? 2u : buf - 1u;
    if (u + 2u < 32u) { issue(u + 2u, nb); waitv<24>(); } else if (u + 1u < 32u) waitv<12>(); else waitv<0>();
    const unsigned m = base + buf * K2_BUF;
    bf16x8 af[5];
#pragma unroll
    for (int n = 0; n < 5; ++n) af[n] = lds_tr2(m + la + 32u * n, m + la + 32u * n + 1280u);
#pragma unroll
    for (int q = 0; q < 7; ++q) {
      const bf16x8 bf = lds_b128(m + 5120u + 1024u * q + 16u * l);
#pragma unroll
      for (int n = 0; n < 5; ++n) acc[n][q] = MFMA16(af[n], bf, acc[n][q]);
    }
    asm volatile("s_waitcnt lgkmcnt(0)" ::: "memory");
    buf = buf == 2u ? 0u : buf + 1u;
  }
  __syncthreads();
  LAS f32x4* red = (LAS f32x4*)(size_t)ldsb;
#pragma unroll
  for (int n = 0; n < 5; ++n)
#pragma unroll
    for (int q = 0; q < 7; ++q) red[(w * 35u + n * 7 + q) * 64u + l] = acc[n][q];
  __syncthreads();
  for (unsigned tI = w; tI < 35u; tI += 4u) {
    const f32x4 v = (red[tI * 64u + l] + red[(35u + tI) * 64u + l]) + (red[(70u + tI) * 64u + l] + red[(105u + tI) * 64u + l]);
    const unsigned n = tI / 7u, q = tI % 7u;
    u32x2 o = {cvtpk(v[0], v[1]), cvtpk(v[2], v[3])};
    *(u32x2*)((char*)out + (size_t)(b * 112u + q * 16u + (l & 15u)) * 1280u + j * 160u + n * 32u + gq * 8u) = o;
  }
}

extern "C" void kernel_launch(void* const* d_in, const int* in_sizes, int n_in, void* d_out, int out_size, void* d_ws, size_t ws_size,
                              hipStream_t stream) {
  (void)in_sizes; (void)n_in; (void)out_size; (void)ws_size;
  (void)hipFuncSetAttribute((const void*)attn_k, hipFuncAttributeMaxDynamicSharedMemorySize, (int)LDS1);
  (void)hipFuncSetAttribute((const void*)oproj_k, hipFuncAttributeMaxDynamicSharedMemorySize, (int)LDS2);
  attn_k<<<256, 512, LDS1, stream>>>(d_in[0], d_in[1], d_in[2], d_in[3], d_ws);
  oproj_k<<<256, 256, LDS2, stream>>>(d_ws, d_in[4], (__hip_bfloat16*)d_out);
}
```

```cpp
#include <hip/hip_runtime.h>
#include <hip/hip_bf16.h>

typedef __bf16 bf16x8 __attribute__((ext_vector_type(8)));
typedef __bf16 bf16x4 __attribute__((ext_vector_type(4)));
typedef __bf16 bf16x2 __attribute__((ext_vector_type(2)));
typedef float f32x16 __attribute__((ext_vector_type(16)));
typedef float f32x4 __attribute__((ext_vector_type(4)));
typedef float f32x2 __attribute__((ext_vector_type(2)));
typedef unsigned u32x4 __attribute__((ext_vector_type(4)));
typedef unsigned u32x2 __attribute__((ext_vector_type(2)));
typedef __amdgpu_buffer_rsrc_t srd_t;
#define LAS __attribute__((address_space(3)))
#define DI __device__ __forceinline__
#define MFMA32(a, b, c) __builtin_amdgcn_mfma_f32_32x32x16_bf16((a), (b), (c), 0, 0, 0)
#define MFMA16(a, b, c) __builtin_amdgcn_mfma_f32_16x16x32_bf16((a), (b), (c), 0, 0, 0)

#define KV_POL 1

constexpr int NB = 32, SQ = 112, DM = 640, NH = 32, SK = 512, DH = 128;
constexpr unsigned KV_BYTES = 134217728u, X_BYTES = 3584u * 1280u, W_BYTES = 640u * 8192u;
constexpr unsigned O_ITEM = 28672u, O_BYTES = 1024u * O_ITEM;
constexpr int RING = 8;
constexpr unsigned FLAG_OFF = RING * 16384u, LDS1 = FLAG_OFF + 128u;
constexpr int TILES_PER_ITEM = 36, NTILES = 4 * TILES_PER_ITEM;
constexpr unsigned SPIN_CAP = 1u << 22;
constexpr float QSCALE = 0.12751743082f;
constexpr float THR = 8.0f;

DI unsigned rfl(unsigned x) { return __builtin_amdgcn_readfirstlane(x); }
DI unsigned cvtpk(float lo, float hi) { f32x2 v = {lo, hi}; bf16x2 b = __builtin_convertvector(v, bf16x2); return __builtin_bit_cast(unsigned, b); }
template <int N> DI void waitv() { asm volatile("s_waitcnt vmcnt(%c0)" ::"i"(N) : "memory"); }
template <int POL> DI void dma4(unsigned m0v, const unsigned (&v)[4], srd_t srd, unsigned soff) {
#define D4(P) asm volatile("s_mov_b32 m0, %0\n\ts_nop 3\n\tbuffer_load_dwordx4 %1, %5, %6 offen " P " lds\n\tbuffer_load_dwordx4 %2, %5, %6 offen offset:1024 " P " lds\n\t" \
                           "buffer_load_dwordx4 %3, %5, %6 offen offset:2048 " P " lds\n\tbuffer_load_dwordx4 %4, %5, %6 offen offset:3072 " P " lds"                        \
                           ::"s"(m0v), "v"(v[0]), "v"(v[1]), "v"(v[2]), "v"(v[3]), "s"(srd), "s"(soff) : "m0", "memory")
  if constexpr (POL == 0) D4(""); else D4("nt");
#undef D4
}
DI void dma1(unsigned m0v, unsigned v, srd_t srd, unsigned soff) {
  asm volatile("s_mov_b32 m0, %0\n\ts_nop 3\n\tbuffer_load_dwordx4 %1, %2, %3 offen lds" ::"s"(m0v), "v"(v), "s"(srd), "s"(soff) : "m0", "memory");
}
DI bf16x8 lds_b128(unsigned a) { return *(const LAS bf16x8*)(size_t)a; }
DI bf16x8 lds_tr2(unsigned a0, unsigned a1) {
  bf16x4 lo = __builtin_amdgcn_ds_read_tr16_b64_v4bf16((LAS bf16x4*)(size_t)a0);
  bf16x4 hi = __builtin_amdgcn_ds_read_tr16_b64_v4bf16((LAS bf16x4*)(size_t)a1);
  return __builtin_shufflevector(lo, hi, 0, 1, 2, 3, 4, 5, 6, 7);
}
DI unsigned min4(unsigned a) {
  u32x4 v;
  asm volatile("ds_read_b128 %0, %1\n\ts_waitcnt lgkmcnt(0)" : "=v"(v) : "v"(a) : "memory");
  unsigned m = min(min(v[0], v[1]), min(v[2], v[3]));
  return rfl(m);
}
DI void lds_put(unsigned a, unsigned val) { asm volatile("ds_write_b32 %0, %1" ::"v"(a), "v"(val) : "memory"); }
DI bf16x8 pack8(const f32x16& x, int o, float s) {
  u32x4 w = {cvtpk(x[o] * s, x[o + 1] * s), cvtpk(x[o + 2] * s, x[o + 3] * s), cvtpk(x[o + 4] * s, x[o + 5] * s), cvtpk(x[o + 6] * s, x[o + 7] * s)};
  return __builtin_bit_cast(bf16x8, w);
}
DI bf16x8 pack8n(const f32x16& x, int o) {
  u32x4 w = {cvtpk(x[o], x[o + 1]), cvtpk(x[o + 2], x[o + 3]), cvtpk(x[o + 4], x[o + 5]), cvtpk(x[o + 6], x[o + 7])};
  return __builtin_bit_cast(bf16x8, w);
}
DI float xhalf_max(float v) { auto r = __builtin_amdgcn_permlane32_swap(__float_as_uint(v), __float_as_uint(v), false, false); return fmaxf(__uint_as_float(r[0]), __uint_as_float(r[1])); }
DI float xhalf_sum(float v) { auto r = __builtin_amdgcn_permlane32_swap(__float_as_uint(v), __float_as_uint(v), false, false); return __uint_as_float(r[0]) + __uint_as_float(r[1]); }

DI void map_block(unsigned g, unsigned& b, unsigned& h0, unsigned& rot) {
  const unsigned xcd = g & 7u, sl = g >> 3, j = sl & 3u, bi = sl >> 2;
  b = (xcd >> 1) * 8u + bi; h0 = ((xcd & 1u) * 4u + j) * 4u;
  rot = ((bi * 10u) / 8u + (j * 10u) / 4u) % 10u;
}

#define GAP() __builtin_amdgcn_sched_barrier(0)
#define OPAQUE(x) asm volatile("" : "+v"(x))
constexpr int PD = 6, NF = 8;

struct CW {
  f32x16 acc[4], s[2][2], negm;
  bf16x8 qf[8], fr[NF];
  u32x4 pf[4];
  float m, l0, l1, mx, al;
  unsigned bk, bv, a_full, myfree, t;
  bool pend;
};
DI unsigned slot_of(unsigned t) { return (t & (RING - 1)) * 16384u; }
DI unsigned flag_ld(unsigned a) { return *(volatile LAS unsigned*)(size_t)a; }
DI void flag_chk(unsigned v, unsigned a, unsigned tt) {
  if (!__all(v > tt)) {
    unsigned sp = 0;
    do { v = flag_ld(a); } while (!__all(v > tt) && ++sp < SPIN_CAP);
  }
  asm volatile("" ::: "memory");
}
DI bf16x8 kfrag(unsigned a0, unsigned a1, int g) { return lds_b128(((g & 1) ? a1 : a0) + 8192u * (g >> 3) + 512u * ((g & 7) >> 1)); }
DI bf16x8 vfrag(unsigned a, int g) { return lds_tr2(a + 4096u * (g >> 2) + 512u * (g & 3), a + 4096u * (g >> 2) + 512u * (g & 3) + 2048u); }
DI bf16x8 wfrag(unsigned a, int g) { return lds_tr2(a + 4096u * (g >> 2) + 512u * (g & 3), a + 4096u * (g >> 2) + 512u * (g & 3) + 256u); }
template <int X> DI void pair_op(CW& c, int j) {
  const int kb = j >> 3, e = 2 * (j & 7);
  const float p0 = __builtin_amdgcn_exp2f(c.s[X][kb][e]), p1 = __builtin_amdgcn_exp2f(c.s[X][kb][e + 1]);
  c.l0 += p0; c.l1 += p1;
  c.pf[j >> 2][j & 3] = cvtpk(p0, p1);
}
template <int X> DI void max8(CW& c, int i) {
  const int kb = i >> 1, e = 8 * (i & 1);
  float a = i == 0 ? fmaxf(c.s[X][kb][e], c.s[X][kb][e + 1]) : fmaxf(fmaxf(c.mx, c.s[X][kb][e]), c.s[X][kb][e + 1]);
#pragma unroll
  for (int k = 2; k < 8; k += 2) a = fmaxf(fmaxf(a, c.s[X][kb][e + k]), c.s[X][kb][e + k + 1]);
  c.mx = a;
}
template <int X> DI void new_ref(CW& c, float d) {
  c.m += d;
#pragma unroll
  for (int i = 0; i < 16; ++i) { c.s[X][0][i] -= d; c.s[X][1][i] -= d; c.negm[i] = -c.m; }
}
template <int X> DI void phaseA(CW& c) {
  constexpr int Y = X ^ 1;
  const unsigned tk = c.t;
  unsigned a0 = c.bk + slot_of(tk), a1, av = c.bv + slot_of(tk + 1u), fl = 0;
  OPAQUE(a0); a1 = a0 ^ 32u; OPAQUE(a1); OPAQUE(av);
#pragma unroll
  for (int g = 0; g < 16; ++g) {
    c.s[Y][g >> 3] = MFMA32(c.fr[g % NF], c.qf[g & 7], (g & 7) == 0 ? c.negm : c.s[Y][g >> 3]);
    if (g == 2) fl = flag_ld(c.a_full);
    if (g == 16 - PD) { lds_put(c.myfree, tk + 1u); flag_chk(fl, c.a_full, tk + 1u); }
    if (g + PD < 16) c.fr[(g + PD) % NF] = kfrag(a0, a1, g + PD); else c.fr[(g + PD) % NF] = vfrag(av, g + PD - 16);
    const int lo = 5 + (11 * g) / 16, hi = 5 + (11 * (g + 1)) / 16;
    if (hi > lo) pair_op<X>(c, lo);
    GAP();
  }
  c.t = tk + 1u;
}
template <int Y, bool NEXTV> DI void phaseB(CW& c) {
  const unsigned tv = c.t;
  unsigned av = c.bv + slot_of(tv), a0 = (NEXTV ? c.bv : c.bk) + slot_of(tv + 1u), a1, fl = 0;
  OPAQUE(av); OPAQUE(a0); a1 = a0 ^ 32u; OPAQUE(a1);
#pragma unroll
  for (int g = 0; g < 16; ++g) {
    c.acc[g & 3] = MFMA32(c.fr[g % NF], __builtin_bit_cast(bf16x8, c.pf[g >> 2]), c.acc[g & 3]);
    if (g == 6) fl = flag_ld(c.a_full);
    if (g == 16 - PD) { lds_put(c.myfree, tv + 1u); flag_chk(fl, c.a_full, tv + 1u); }
    if (g + PD < 16) c.fr[(g + PD) % NF] = vfrag(av, g + PD);
    else c.fr[(g + PD) % NF] = NEXTV ? vfrag(a0, g + PD - 16) : kfrag(a0, a1, g + PD - 16);
    if (g < 4) max8<Y>(c, g);
    if (g == 4) c.mx = xhalf_max(c.mx);
    if (g == 5) {
      if (__builtin_expect(__any(c.mx > THR), 0)) {
        const float d = fmaxf(c.mx, 0.f);
        c.al = __builtin_amdgcn_exp2f(-d);
        new_ref<Y>(c, d);
        c.l0 *= c.al; c.l1 *= c.al; c.pend = true;
      }
    }
    if (g >= 6 && (g & 1) == 0) pair_op<Y>(c, (g - 6) >> 1);
    GAP();
  }
  if (__builtin_expect(c.pend, 0)) {
#pragma unroll
    for (int k = 0; k < 4; ++k)
#pragma unroll
      for (int i = 0; i < 16; ++i) c.acc[k][i] *= c.al;
    c.pend = false;
  }
  c.t = tv + 1u;
}

__global__ __launch_bounds__(512, 2) void attn_k(const void* __restrict__ Xp, const void* __restrict__ Kp, const void* __restrict__ Vp,
                                                 const void* __restrict__ Wq, void* __restrict__ Ows) {
  extern __shared__ __attribute__((aligned(16))) char lds[];
  const unsigned ldsb = (unsigned)(size_t)lds;
  const unsigned l = threadIdx.x & 63u, wid = rfl(threadIdx.x >> 6), w4 = wid & 3u;
  unsigned b, h0, rot;
  map_block(blockIdx.x, b, h0, rot);
  const unsigned a_full = ldsb + FLAG_OFF, a_free = ldsb + FLAG_OFF + 64u;
  if (threadIdx.x < 32) lds_put(a_full + threadIdx.x * 4u, 0u);
  asm volatile("s_waitcnt lgkmcnt(0)" ::: "memory");
  __builtin_amdgcn_s_barrier();

  if (wid >= 4) {
    srd_t sK = __builtin_amdgcn_make_buffer_rsrc((char*)Kp - 4096, (short)0, (int)(KV_BYTES + 4096u), 0x00020000);
    srd_t sV = __builtin_amdgcn_make_buffer_rsrc((char*)Vp - 4096, (short)0, (int)(KV_BYTES + 4096u), 0x00020000);
    srd_t sX = __builtin_amdgcn_make_buffer_rsrc((char*)Xp - 4096, (short)0, (int)(X_BYTES + 4096u), 0x00020000);
    srd_t sW = __builtin_amdgcn_make_buffer_rsrc((char*)Wq - 4096, (short)0, (int)(W_BYTES + 4096u), 0x00020000);
    unsigned vk[4], vv[4], vw[4], vx[4];
    const unsigned row = (l >> 2) & 7u, cq = l & 3u, sub = l >> 5;
#pragma unroll
    for (int i = 0; i < 4; ++i) {
      const unsigned rf = 16u * w4 + 8u * (i >> 1) + row, cb = 128u * (i & 1) + 64u * sub, bias = 4096u - 1024u * i;
      vk[i] = rf * 256u + cb + 16u * (cq ^ ((rf >> 2) & 3u)) + bias;
      vv[i] = rf * 256u + cb + 16u * cq + bias;
      vw[i] = rf * 8192u + cb + 16u * cq + bias;
      const unsigned rx = 32u * w4 + 8u * i + row;
      vx[i] = rx * 1280u + 64u * sub + 16u * (cq ^ ((rx >> 2) & 3u)) + bias;
    }
    const unsigned myfull = a_full + w4 * 4u, m0b = ldsb + w4 * 4096u;
    unsigned t = 0, freec = 0, pub = 0;
    auto pre = [&]() {
      if (freec + RING <= t) {
        freec = min4(a_free);
        if (freec + RING <= t) {
          waitv<0>(); lds_put(myfull, t); pub = t;
          unsigned sp = 0;
          do { __builtin_amdgcn_s_sleep(1); freec = min4(a_free); if (++sp > SPIN_CAP) freec = 0x40000000u; } while (freec + RING <= t);
        }
      }
    };
    auto post = [&]() { waitv<8>(); if (t > pub + 1u) { pub = t - 1u; lds_put(myfull, pub); } ++t; };
    for (unsigned r = 0; r < 4; ++r) {
      const unsigned h = h0 + r;
      unsigned c = rot;
      for (unsigned i = 0; i < 10; ++i) {
        pre(); dma4<0>(m0b + slot_of(t), vw, sW, c * 524288u + h * 256u); post();
        pre(); dma4<0>(m0b + slot_of(t), vx, sX, b * 143360u + c * 128u); post();
        c = c == 9u ? 0u : c + 1u;
      }
      const unsigned kvo = (b * 32u + h) * 131072u;
      pre(); dma4<KV_POL>(m0b + slot_of(t), vk, sK, kvo); post();
      for (unsigned kt = 0; kt < 7; ++kt) {
        pre(); dma4<KV_POL>(m0b + slot_of(t), vk, sK, kvo + (kt + 1u) * 16384u); post();
        pre(); dma4<KV_POL>(m0b + slot_of(t), vv, sV, kvo + kt * 16384u); post();
      }
      pre(); dma4<KV_POL>(m0b + slot_of(t), vv, sV, kvo + 7u * 16384u); post();
    }
    waitv<4>(); lds_put(myfull, t - 1u);
    waitv<0>(); lds_put(myfull, t);
    return;
  }

  const unsigned hh = l >> 5, l31 = l & 31u, q4 = (l >> 2) & 3u, blk = (l >> 4) & 1u, p4 = l & 3u;
  const unsigned sp4 = ((p4 & 1u) << 1) | (p4 >> 1);
  CW c;
  c.bk = ldsb + 2048u * (l31 >> 3) + 64u * (l31 & 7u) + 16u * (hh ^ ((l31 >> 2) & 3u));
  c.bv = ldsb + 64u * (4u * hh + q4) + 32u * blk + 8u * p4;
  const unsigned bw = ldsb + 2048u * hh + 64u * q4 + 32u * blk + 8u * sp4;
  const unsigned bx = ldsb + 4096u * w4 + 1024u * (l31 >> 3) + 64u * (l31 & 7u) + 16u * (hh ^ ((l31 >> 2) & 3u));
  c.a_full = a_full + 4u * p4; c.myfree = a_free + w4 * 4u; c.t = 0; c.pend = false; c.al = 1.f;
  srd_t sO = __builtin_amdgcn_make_buffer_rsrc(Ows, (short)0, (int)O_BYTES, 0x00020000);
  const unsigned qb16 = 2u * w4 + blk;
  const unsigned vo = qb16 < 7u ? qb16 * 4096u + hh * 256u + (l & 15u) * 16u : 0x80000000u;

  for (unsigned r = 0; r < 4; ++r) {
#pragma unroll
    for (int k = 0; k < 4; ++k) c.acc[k] = f32x16{};
    bf16x8 xf[2];
    {
      flag_chk(0u, c.a_full, c.t + 1u);
      unsigned aw = bw + slot_of(c.t), ax = bx + slot_of(c.t + 1u);
      OPAQUE(aw); OPAQUE(ax);
      xf[0] = lds_b128(ax);
#pragma unroll
      for (int g = 0; g < PD; ++g) c.fr[g] = wfrag(aw, g);
      GAP();
    }
    for (unsigned i = 0; i < 10; ++i) {
      const unsigned t = c.t, tn = i < 9u ? t + 2u : t;
      unsigned aw = bw + slot_of(t), ax = bx + slot_of(t + 1u), ax1, aw2 = bw + slot_of(tn), ax2 = bx + slot_of(tn + 1u), fl = 0;
      OPAQUE(aw); OPAQUE(ax); ax1 = ax ^ 32u; OPAQUE(ax1); OPAQUE(aw2); OPAQUE(ax2);
#pragma unroll
      for (int g = 0; g < 16; ++g) {
        c.acc[g & 3] = MFMA32(c.fr[g % NF], xf[(g >> 2) & 1], c.acc[g & 3]);
        if (g == 2) fl = flag_ld(c.a_full);
        if (g == 0) xf[1] = lds_b128(ax1);
        if (g == 4) xf[0] = lds_b128(ax + 512u);
        if (g == 8) xf[1] = lds_b128(ax1 + 512u);
        if (g == 16 - PD) { if (i < 9u) lds_put(c.myfree, t + 2u); flag_chk(fl, c.a_full, tn + 1u); }
        if (g == 12) xf[0] = lds_b128(ax2);
        c.fr[(g + PD) % NF] = g + PD < 16 ? wfrag(aw, g + PD) : wfrag(aw2, g + PD - 16);
        GAP();
      }
      c.t = t + 2u;
    }
    lds_put(c.myfree, c.t);
    {
      flag_chk(0u, c.a_full, c.t);
      unsigned a0 = c.bk + slot_of(c.t), a1, n0 = c.bk + slot_of(c.t + 1u), n1, fl = 0;
      OPAQUE(a0); a1 = a0 ^ 32u; OPAQUE(a1); OPAQUE(n0); n1 = n0 ^ 32u; OPAQUE(n1);
#pragma unroll
      for (int g = 0; g < PD; ++g) c.fr[g] = kfrag(a0, a1, g);
#pragma unroll
      for (int k = 0; k < 4; ++k) { c.qf[2 * k] = pack8(c.acc[k], 0, QSCALE); c.qf[2 * k + 1] = pack8(c.acc[k], 8, QSCALE); }
      GAP();
#pragma unroll
      for (int g = 0; g < 16; ++g) {
        c.s[0][g >> 3] = MFMA32(c.fr[g % NF], c.qf[g & 7], (g & 7) == 0 ? f32x16{} : c.s[0][g >> 3]);
        if (g == 2) fl = flag_ld(c.a_full);
        if (g == 16 - PD) { lds_put(c.myfree, c.t + 1u); flag_chk(fl, c.a_full, c.t + 1u); }
        c.fr[(g + PD) % NF] = g + PD < 16 ? kfrag(a0, a1, g + PD) : kfrag(n0, n1, g + PD - 16);
        if (g < 4) c.acc[g] = f32x16{};
        if (g == 11 || g == 12) max8<0>(c, g - 11);
        GAP();
      }
      c.t += 1u;
      max8<0>(c, 2); max8<0>(c, 3);
      c.m = 0.f; c.l0 = 0.f; c.l1 = 0.f;
      new_ref<0>(c, xhalf_max(c.mx));
#pragma unroll
      for (int j = 0; j < 5; ++j) pair_op<0>(c, j);
      GAP();
    }
    for (unsigned it = 0; it < 3; ++it) {
      phaseA<0>(c); phaseB<1, false>(c);
      phaseA<1>(c); phaseB<0, false>(c);
    }
    phaseA<0>(c); phaseB<1, true>(c);
    {
      unsigned av = c.bv + slot_of(c.t);
      OPAQUE(av);
#pragma unroll
      for (int j = 5; j < 8; ++j) pair_op<1>(c, j);
      GAP();
#pragma unroll
      for (int g = 0; g < 16; ++g) {
        c.acc[g & 3] = MFMA32(c.fr[g % NF], __builtin_bit_cast(bf16x8, c.pf[g >> 2]), c.acc[g & 3]);
        if (g + PD < 16) c.fr[(g + PD) % NF] = vfrag(av, g + PD);
        if (g == 16 - PD) lds_put(c.myfree, c.t + 1u);
        if (g < 8) pair_op<1>(c, 8 + g);
        GAP();
      }
      c.t += 1u;
    }
    const float inv = __builtin_amdgcn_rcpf(xhalf_sum(c.l0 + c.l1));
    const unsigned so = vo + (b * 32u + h0 + r) * O_ITEM;
#pragma unroll
    for (int k = 0; k < 4; ++k) {
      __builtin_amdgcn_raw_buffer_store_b128(__builtin_bit_cast(u32x4, pack8(c.acc[k], 0, inv)), sO, so + k * 1024u, 0, 0);
      __builtin_amdgcn_raw_buffer_store_b128(__builtin_bit_cast(u32x4, pack8(c.acc[k], 8, inv)), sO, so + k * 1024u + 512u, 0, 0);
    }
  }
}

constexpr unsigned K2_BUF = 12288u, K2_WAVE = 3u * K2_BUF, LDS2 = 4u * K2_WAVE;
__global__ __launch_bounds__(256, 1) void oproj_k(const void* __restrict__ Ows, const void* __restrict__ Wo, __hip_bfloat16* __restrict__ out) {
  extern __shared__ __attribute__((aligned(16))) char lds[];
  const unsigned ldsb = (unsigned)(size_t)lds;
  const unsigned l = threadIdx.x & 63u, w = rfl(threadIdx.x >> 6);
  const unsigned g = blockIdx.x, xcd = g & 7u, sl = g >> 3, bi = sl >> 3, b = xcd * 4u + bi, j = sl & 7u;
  srd_t sO = __builtin_amdgcn_make_buffer_rsrc((void*)Ows, (short)0, (int)O_BYTES, 0x00020000);
  srd_t sW = __builtin_amdgcn_make_buffer_rsrc((void*)Wo, (short)0, (int)W_BYTES, 0x00020000);
  unsigned vwo[5], vo[4];
#pragma unroll
  for (int i = 0; i < 5; ++i) { const unsigned c = 64u * i + l; vwo[i] = (c / 10u) * 1280u + (c % 10u) * 16u + j * 160u - (i < 4 ? 1024u * i : 0u); }
#pragma unroll
  for (int i = 0; i < 4; ++i) vo[i] = l * 16u + i * (4096u - 1024u);
  const unsigned vw4[4] = {vwo[0], vwo[1], vwo[2], vwo[3]};
  const unsigned base = ldsb + w * K2_WAVE;
  const unsigned rot = (j + 2u * bi) & 7u;
  auto issue = [&](unsigned u, unsigned buf) {
    const unsigned h = w * 8u + (((u >> 2) + rot) & 7u), ks = u & 3u;
    const unsigned m = base + buf * K2_BUF;
    const unsigned so = (b * 32u + h) * O_ITEM + ks * 1024u;
    const unsigned sw = (h * 128u + ks * 32u) * 1280u;
    dma4<0>(m, vw4, sW, sw); dma1(m + 4096u, vwo[4], sW, sw);
    dma4<0>(m + 5120u, vo, sO, so);
#pragma unroll
    for (int i = 0; i < 3; ++i) dma1(m + 5120u + 4096u + 1024u * i, vo[0], sO, so + (4u + i) * 4096u);
  };
  f32x4 acc[5][7];
#pragma unroll
  for (int n = 0; n < 5; ++n)
#pragma unroll
    for (int q = 0; q < 7; ++q) acc[n][q] = f32x4{};
  const unsigned gq = l >> 4, q4 = (l >> 2) & 3u, p4 = l & 3u;
  const unsigned la = 160u * (16u * (gq >> 1) + 4u * (gq & 1u) + q4) + 8u * p4;
  issue(0, 0); issue(1, 1);
  unsigned buf = 0;
  for (unsigned u = 0; u < 32; ++u) {
    const unsigned nb = buf == 0 ? 2u : buf - 1u;
    if (u + 2u < 32u) { issue(u + 2u, nb); waitv<24>(); } else if (u + 1u < 32u) waitv<12>(); else waitv<0>();
    const unsigned m = base + buf * K2_BUF;
    bf16x8 af[5];
#pragma unroll
    for (int n = 0; n < 5; ++n) af[n] = lds_tr2(m + la + 32u * n, m + la + 32u * n + 1280u);
#pragma unroll
    for (int q = 0; q < 7; ++q) {
      const bf16x8 bf = lds_b128(m + 5120u + 1024u * q + 16u * l);
#pragma unroll
      for (int n = 0; n < 5; ++n) acc[n][q] = MFMA16(af[n], bf, acc[n][q]);
    }
    asm volatile("s_waitcnt lgkmcnt(0)" ::: "memory");
    buf = buf == 2u ? 0u : buf + 1u;
  }
  __syncthreads();
  LAS f32x4* red = (LAS f32x4*)(size_t)ldsb;
#pragma unroll
  for (int n = 0; n < 5; ++n)
#pragma unroll
    for (int q = 0; q < 7; ++q) red[(w * 35u + n * 7 + q) * 64u + l] = acc[n][q];
  __syncthreads();
  for (unsigned tI = w; tI < 35u; tI += 4u) {
    const f32x4 v = (red[tI * 64u + l] + red[(35u + tI) * 64u + l]) + (red[(70u + tI) * 64u + l] + red[(105u + tI) * 64u + l]);
    const unsigned n = tI / 7u, q = tI % 7u;
    u32x2 o = {cvtpk(v[0], v[1]), cvtpk(v[2], v[3])};
    *(u32x2*)((char*)out + (size_t)(b * 112u + q * 16u + (l & 15u)) * 1280u + j * 160u + n * 32u + gq * 8u) = o;
  }
}

extern "C" void kernel_launch(void* const* d_in, const int* in_sizes, int n_in, void* d_out, int out_size, void* d_ws, size_t ws_size,
                              hipStream_t stream) {
  (void)in_sizes; (void)n_in; (void)out_size; (void)ws_size;
  (void)hipFuncSetAttribute((const void*)attn_k, hipFuncAttributeMaxDynamicSharedMemorySize, (int)LDS1);
  (void)hipFuncSetAttribute((const void*)oproj_k, hipFuncAttributeMaxDynamicSharedMemorySize, (int)LDS2);
  attn_k<<<256, 512, LDS1, stream>>>(d_in[0], d_in[1], d_in[2], d_in[3], d_ws);
  oproj_k<<<256, 256, LDS2, stream>>>(d_ws, d_in[4], (__hip_bfloat16*)d_out);
}
```

```cpp
#include <hip/hip_runtime.h>
#include <hip/hip_bf16.h>

typedef __bf16 bf16x8 __attribute__((ext_vector_type(8)));
typedef __bf16 bf16x4 __attribute__((ext_vector_type(4)));
typedef __bf16 bf16x2 __attribute__((ext_vector_type(2)));
typedef float f32x16 __attribute__((ext_vector_type(16)));
typedef float f32x4 __attribute__((ext_vector_type(4)));
typedef float f32x2 __attribute__((ext_vector_type(2)));
typedef unsigned u32x4 __attribute__((ext_vector_type(4)));
typedef unsigned u32x2 __attribute__((ext_vector_type(2)));
typedef __amdgpu_buffer_rsrc_t srd_t;
#define LAS __attribute__((address_space(3)))
#define DI __device__ __forceinline__
#define MFMA32(a, b, c) __builtin_amdgcn_mfma_f32_32x32x16_bf16((a), (b), (c), 0, 0, 0)
#define MFMA16(a, b, c) __builtin_amdgcn_mfma_f32_16x16x32_bf16((a), (b), (c), 0, 0, 0)

#define KV_POL 1

constexpr int NB = 32, SQ = 112, DM = 640, NH = 32, SK = 512, DH = 128;
constexpr unsigned KV_BYTES = 134217728u, X_BYTES = 3584u * 1280u, W_BYTES = 640u * 8192u;
constexpr unsigned O_ITEM = 28672u, O_BYTES = 1024u * O_ITEM;
#define INFL 2
#define Q2POS ((j + xcd) % 3u)
constexpr int RING = 8;
constexpr unsigned QS_OFF = 32u << 20, QS_BYTES = 256u * 4u * 32768u;
constexpr unsigned FLAG_OFF = RING * 16384u, LDS1 = FLAG_OFF + 128u;
constexpr int TILES_PER_ITEM = 36, NTILES = 4 * TILES_PER_ITEM;
constexpr unsigned SPIN_CAP = 1u << 22;
constexpr float QSCALE = 0.12751743082f;
constexpr float THR = 8.0f;

DI unsigned rfl(unsigned x) { return __builtin_amdgcn_readfirstlane(x); }
DI unsigned cvtpk(float lo, float hi) { f32x2 v = {lo, hi}; bf16x2 b = __builtin_convertvector(v, bf16x2); return __builtin_bit_cast(unsigned, b); }
template <int N> DI void waitv() { asm volatile("s_waitcnt vmcnt(%c0)" ::"i"(N) : "memory"); }
template <int POL> DI void dma4(unsigned m0v, const unsigned (&v)[4], srd_t srd, unsigned soff) {
#define D4(P) asm volatile("s_mov_b32 m0, %0\n\ts_nop 3\n\tbuffer_load_dwordx4 %1, %5, %6 offen " P " lds\n\tbuffer_load_dwordx4 %2, %5, %6 offen offset:1024 " P " lds\n\t" \
                           "buffer_load_dwordx4 %3, %5, %6 offen offset:2048 " P " lds\n\tbuffer_load_dwordx4 %4, %5, %6 offen offset:3072 " P " lds"                        \
                           ::"s"(m0v), "v"(v[0]), "v"(v[1]), "v"(v[2]), "v"(v[3]), "s"(srd), "s"(soff) : "m0", "memory")
  if constexpr (POL == 0) D4(""); else D4("nt");
#undef D4
}
DI void dma1(unsigned m0v, unsigned v, srd_t srd, unsigned soff) {
  asm volatile("s_mov_b32 m0, %0\n\ts_nop 3\n\tbuffer_load_dwordx4 %1, %2, %3 offen lds" ::"s"(m0v), "v"(v), "s"(srd), "s"(soff) : "m0", "memory");
}
DI bf16x8 lds_b128(unsigned a) { return *(const LAS bf16x8*)(size_t)a; }
DI bf16x8 lds_tr2(unsigned a0, unsigned a1) {
  bf16x4 lo = __builtin_amdgcn_ds_read_tr16_b64_v4bf16((LAS bf16x4*)(size_t)a0);
  bf16x4 hi = __builtin_amdgcn_ds_read_tr16_b64_v4bf16((LAS bf16x4*)(size_t)a1);
  return __builtin_shufflevector(lo, hi, 0, 1, 2, 3, 4, 5, 6, 7);
}
DI unsigned min4(unsigned a) {
  u32x4 v;
  asm volatile("ds_read_b128 %0, %1\n\ts_waitcnt lgkmcnt(0)" : "=v"(v) : "v"(a) : "memory");
  unsigned m = min(min(v[0], v[1]), min(v[2], v[3]));
  return rfl(m);
}
DI void lds_put(unsigned a, unsigned val) { asm volatile("ds_write_b32 %0, %1" ::"v"(a), "v"(val) : "memory"); }
DI bf16x8 pack8(const f32x16& x, int o, float s) {
  u32x4 w = {cvtpk(x[o] * s, x[o + 1] * s), cvtpk(x[o + 2] * s, x[o + 3] * s), cvtpk(x[o + 4] * s, x[o + 5] * s), cvtpk(x[o + 6] * s, x[o + 7] * s)};
  return __builtin_bit_cast(bf16x8, w);
}
DI bf16x8 pack8n(const f32x16& x, int o) {
  u32x4 w = {cvtpk(x[o], x[o + 1]), cvtpk(x[o + 2], x[o + 3]), cvtpk(x[o + 4], x[o + 5]), cvtpk(x[o + 6], x[o + 7])};
  return __builtin_bit_cast(bf16x8, w);
}
DI float xhalf_max(float v) { auto r = __builtin_amdgcn_permlane32_swap(__float_as_uint(v), __float_as_uint(v), false, false); return fmaxf(__uint_as_float(r[0]), __uint_as_float(r[1])); }
DI float xhalf_sum(float v) { auto r = __builtin_amdgcn_permlane32_swap(__float_as_uint(v), __float_as_uint(v), false, false); return __uint_as_float(r[0]) + __uint_as_float(r[1]); }

DI void map_block(unsigned g, unsigned& b, unsigned& h0, unsigned& rot, unsigned& q2pos) {
  const unsigned xcd = g & 7u, sl = g >> 3, j = sl & 3u, bi = sl >> 2;
  b = (xcd >> 1) * 8u + bi; h0 = ((xcd & 1u) * 4u + j) * 4u;
  rot = ((bi * 10u) / 8u + (j * 10u) / 4u) % 10u;
  q2pos = Q2POS;
}

#define GAP() __builtin_amdgcn_sched_barrier(0)
#define STAMP(i)
#define STAMPR(i)
#define DBGX(x)
#define OPAQUE(x) asm volatile("" : "+v"(x))
#define USE(x) asm volatile("" ::"v"(x))
#define SPIN(x) asm volatile("" : "+s"(x))
typedef unsigned long long u64;
constexpr int PD = 6, NF = 8;
constexpr unsigned RMASK = RING * 16384u - 1u;

struct CW {
  f32x16 acc[4], s[2][2], negm;
  bf16x8 qf[8], fr[NF];
  u32x4 pf[4];
  f32x4 lsum;
  bf16x8 ones;
};
struct CS {
  float m, mx;
  unsigned bk, bv, a_full, myfree, tv, l;
  unsigned a0, a1;
  unsigned t, so;
  DBGX(unsigned nslow; unsigned nspin;)
};
DI unsigned nxt(unsigned so) { return (so + 16384u) & RMASK; }
DI unsigned flag_ld(unsigned a) { return *(volatile LAS unsigned*)(size_t)a; }
DI u64 flag_cmp(unsigned v, unsigned tt) { u64 m = __builtin_amdgcn_ballot_w64((int)v <= (int)tt); SPIN(m); return m; }
DI void flag_wait(CS& z, u64 bad, unsigned tt) {
  if (__builtin_expect(bad != 0ull, 0)) {
    unsigned sp = 0, v; DBGX(unsigned t0_ = (unsigned)__builtin_amdgcn_s_memtime();)
    do { v = flag_ld(z.a_full); } while (__builtin_amdgcn_ballot_w64((int)v <= (int)tt) != 0ull && ++sp < SPIN_CAP);
    DBGX(z.nslow += 1; z.nspin += (unsigned)__builtin_amdgcn_s_memtime() - t0_; __builtin_amdgcn_s_waitcnt(0xC07F);)
  }
  asm volatile("" ::: "memory");
}
DI void release(CS& z, unsigned n) { z.tv += n; lds_put(z.myfree, z.tv); }
DI bf16x8 kfrag(unsigned a0, unsigned a1, int g) { return lds_b128(((g & 1) ? a1 : a0) + 8192u * (g >> 3) + 512u * ((g & 7) >> 1)); }
DI bf16x8 vfrag(unsigned a, int g) { return lds_tr2(a + 4096u * (g >> 2) + 512u * (g & 3), a + 4096u * (g >> 2) + 512u * (g & 3) + 2048u); }
DI bf16x8 wfrag(unsigned a, int g) { return lds_tr2(a + 4096u * (g >> 2) + 512u * (g & 3), a + 4096u * (g >> 2) + 512u * (g & 3) + 256u); }
DI void lds_w128(unsigned a, bf16x8 v) { *(LAS bf16x8*)(size_t)a = v; }

enum { OP_E, OP_C, OP_M };
struct SmOp { int kind, idx, cost; };
constexpr int NEC = 48, NOPS = NEC + 16;
struct SmPlan { SmOp op[NOPS]; int st[33]; };
constexpr SmPlan make_plan() {
  SmPlan p{}; int n = 0;
  p.op[n++] = SmOp{OP_E, 0, 8}; p.op[n++] = SmOp{OP_E, 1, 8};
  for (int j = 1; j < 16; ++j) { p.op[n++] = SmOp{OP_E, 2 * j, 8}; p.op[n++] = SmOp{OP_C, j - 1, 4}; p.op[n++] = SmOp{OP_E, 2 * j + 1, 8}; }
  p.op[n++] = SmOp{OP_C, 15, 4};
  for (int i = 0; i < 16; ++i) p.op[n++] = SmOp{OP_M, i, 4};
  int cap[32] = {}, tc = 0, tot = 0;
  for (int g = 0; g < 31; ++g) {
    const int gg = g & 15; const bool vread = (g >= 16) == (gg + PD < 16);
    cap[g] = vread ? 12 : 18;
    if (g >= 16 && (gg & 3) == 1) cap[g] += 8;
    if (gg == 16 - PD) cap[g] -= 12;
    if (gg == 2 || gg == 4 || gg == 6 || gg == 8) cap[g] -= 4;
    tc += cap[g];
  }
  for (int i = 0; i < NOPS; ++i) tot += p.op[i].cost;
  int i = 0, cum = 0, cc = 0;
  for (int g = 0; g < 32; ++g) {
    p.st[g] = i; cc += cap[g];
    while (i < NOPS && (g >= 30 || (long)(2 * cum + p.op[i].cost) * tc <= (long)2 * cc * tot)) { cum += p.op[i].cost; ++i; }
  }
  p.st[32] = NOPS;
  return p;
}
constexpr SmPlan PLAN = make_plan();
constexpr int after_pack(int j) { int i = 0; while (!(PLAN.op[i].kind == OP_C && PLAN.op[i].idx == j)) ++i; return i + 1; }
static_assert(after_pack(3) <= PLAN.st[16] && after_pack(7) <= PLAN.st[20] && after_pack(11) <= PLAN.st[24] && after_pack(15) <= PLAN.st[28], "P fragment deadlines");

template <bool FIRST> DI void negm_set(CW& c, CS& z) {
  const float one = z.l < 32u ? 1.f : 0.f, nm = -z.m;
  if constexpr (FIRST) asm volatile("s_nop 1\n\tv_mfma_f32_32x32x2_f32 %0, %1, %2, 0\n\ts_nop 7" : "=&v"(c.negm) : "v"(one), "v"(nm));
  else asm volatile("s_nop 1\n\tv_mfma_f32_32x32x2_f32 %0, %1, %2, 0\n\ts_nop 7" : "+v"(c.negm) : "v"(one), "v"(nm));
}
template <int Y, bool FIRST> DI void new_ref(CW& c, CS& z, float d) {
  z.m += d;
  negm_set<FIRST>(c, z);
#pragma unroll
  for (int i = 0; i < 16; ++i) { c.s[Y][0][i] -= d; c.s[Y][1][i] -= d; }
}
template <int X, int I> DI void sm_op(CW& c, CS& z) {
  constexpr SmOp o = PLAN.op[I];
  if constexpr (o.kind == OP_M) {
    constexpr int Y = X ^ 1, kb = o.idx >> 3, e = 2 * (o.idx & 7);
    if constexpr (o.idx == 0) z.mx = c.s[Y][0][0];
    else if constexpr (o.idx == 1) z.mx = fmaxf(fmaxf(fmaxf(fmaxf(z.mx, c.s[Y][0][1]), c.s[Y][0][2]), c.s[Y][0][3]), c.s[Y][0][3]);
    else z.mx = fmaxf(fmaxf(z.mx, c.s[Y][kb][e]), c.s[Y][kb][e + 1]);
  } else if constexpr (o.kind == OP_E) {
    float p = __builtin_amdgcn_exp2f(c.s[X][o.idx >> 4][o.idx & 15]); OPAQUE(p);
    c.s[X][o.idx >> 4][o.idx & 15] = p;
  } else {
    unsigned u = cvtpk(c.s[X][o.idx >> 3][2 * (o.idx & 7)], c.s[X][o.idx >> 3][2 * (o.idx & 7) + 1]); OPAQUE(u);
    c.pf[o.idx >> 2][o.idx & 3] = u;
  }
}
template <int X, int LO, int HI> DI void sm_ops(CW& c, CS& z) { if constexpr (LO < HI) { sm_op<X, LO>(c, z); sm_ops<X, LO + 1, HI>(c, z); } }

struct PH { unsigned fl, n0, n1; u64 bad; };
template <int G, bool NEXTK> DI void book(CS& z, PH& p) {
  if constexpr (G == 2) { p.fl = flag_ld(z.a_full); z.t += 1u; SPIN(z.t); }
  if constexpr (G == 3) { z.so = nxt(z.so); SPIN(z.so); }
  if constexpr (G == 4) { p.n0 = (NEXTK ? z.bk : z.bv) + z.so; OPAQUE(p.n0); }
  if constexpr (G == 6 && NEXTK) { p.n1 = p.n0 ^ 32u; OPAQUE(p.n1); }
  if constexpr (G == 8) p.bad = flag_cmp(p.fl, z.t);
  if constexpr (G == 16 - PD) { release(z, 1u); flag_wait(z, p.bad, z.t); }
}
template <int X, int G> DI void gapA(CW& c, CS& z, PH& p) {
  constexpr int Y = X ^ 1;
  c.s[Y][G >> 3] = MFMA32(c.fr[G % NF], c.qf[G & 7], (G & 7) == 0 ? c.negm : c.s[Y][G >> 3]);
  GAP();
  book<G, false>(z, p);
  if constexpr (G + PD < 16) c.fr[(G + PD) % NF] = kfrag(z.a0, z.a1, G + PD); else c.fr[(G + PD) % NF] = vfrag(p.n0, G + PD - 16);
  sm_ops<X, PLAN.st[G], PLAN.st[G + 1]>(c, z);
  if constexpr (G & 1) USE(c.fr[(G + 2) % NF]);
  GAP();
  if constexpr (G < 15) gapA<X, G + 1>(c, z, p); else z.a0 = p.n0;
}
template <int X, bool NEXTK, int G> DI void gapB(CW& c, CS& z, PH& p) {
  u64 big = 0;
  if constexpr (G == 15) { big = __builtin_amdgcn_ballot_w64(z.mx > THR); SPIN(big); GAP(); }
  c.acc[G & 3] = MFMA32(c.fr[G % NF], __builtin_bit_cast(bf16x8, c.pf[G >> 2]), c.acc[G & 3]);
  GAP();
  book<G, NEXTK>(z, p);
  if constexpr (G + PD < 16) c.fr[(G + PD) % NF] = vfrag(z.a0, G + PD);
  else c.fr[(G + PD) % NF] = NEXTK ? kfrag(p.n0, p.n1, G + PD - 16) : vfrag(p.n0, G + PD - 16);
  sm_ops<X, PLAN.st[16 + G], PLAN.st[17 + G]>(c, z);
  if constexpr ((G & 3) == 1) c.lsum = MFMA16(c.ones, __builtin_bit_cast(bf16x8, c.pf[G >> 2]), c.lsum);
  if constexpr (G & 1) USE(c.fr[(G + 2) % NF]);
  GAP();
  if constexpr (G < 15) gapB<X, NEXTK, G + 1>(c, z, p);
  else {
    z.a0 = p.n0; z.a1 = p.n1;
    if (__builtin_expect(big != 0ull, 0)) {
      const float d = fmaxf(xhalf_max(z.mx), 0.f), al = __builtin_amdgcn_exp2f(-d);
      new_ref<X ^ 1, false>(c, z, d);
#pragma unroll
      for (int k = 0; k < 4; ++k) c.acc[k] *= al;
      c.lsum *= al;
    }
  }
}
template <int X, bool NEXTK> DI void period(CW& c, CS& z) { PH p{}; gapA<X, 0>(c, z, p); gapB<X, NEXTK, 0>(c, z, p); }
template <int G> DI void gapF(CW& c, CS& z, PH& p) {
  c.s[0][G >> 3] = MFMA32(c.fr[G % NF], c.qf[G & 7], (G & 7) == 0 ? f32x16{} : c.s[0][G >> 3]);
  GAP();
  book<G, true>(z, p);
  c.fr[(G + PD) % NF] = G + PD < 16 ? kfrag(z.a0, z.a1, G + PD) : kfrag(p.n0, p.n1, G + PD - 16);
  if constexpr (G < 10) { if constexpr (G >= 2) { f32x16& a = c.acc[(G - 2) >> 1];
#pragma unroll
      for (int i = 0; i < 8; ++i) a[8 * (G & 1) + i] = 0.f; } }
  if constexpr (G >= 12) sm_ops<1, NEC + 2 * (G - 12), NEC + 2 * (G - 11)>(c, z);
  GAP();
  if constexpr (G < 15) gapF<G + 1>(c, z, p); else { z.a0 = p.n0; z.a1 = p.n1; }
}
template <int G> DI void gapL(CW& c, CS& z) {
  c.acc[G & 3] = MFMA32(c.fr[G % NF], __builtin_bit_cast(bf16x8, c.pf[G >> 2]), c.acc[G & 3]);
  GAP();
  if constexpr (G + PD < 16) c.fr[(G + PD) % NF] = vfrag(z.a0, G + PD);
  if constexpr (G == 16 - PD) release(z, 1u);
  if constexpr (G < 12) sm_ops<1, after_pack(3 + G), after_pack(4 + G)>(c, z);
  if constexpr ((G & 3) == 1) c.lsum = MFMA16(c.ones, __builtin_bit_cast(bf16x8, c.pf[G >> 2]), c.lsum);
  GAP();
  if constexpr (G < 15) gapL<G + 1>(c, z);
}

__global__ __launch_bounds__(512, 2) void attn_k(const void* __restrict__ Xp, const void* __restrict__ Kp, const void* __restrict__ Vp,
                                                 const void* __restrict__ Wq, void* __restrict__ Ows) {
  extern __shared__ __attribute__((aligned(16))) char lds[];
  const unsigned ldsb = (unsigned)(size_t)lds;
  const unsigned l = threadIdx.x & 63u, wid = rfl(threadIdx.x >> 6), w4 = wid & 3u;
  unsigned b, h0, rot, q2pos;
  map_block(blockIdx.x, b, h0, rot, q2pos);
  const unsigned a_full = ldsb + FLAG_OFF, a_free = ldsb + FLAG_OFF + 64u;
  if (threadIdx.x < 32) lds_put(a_full + threadIdx.x * 4u, 0u);
  asm volatile("s_waitcnt lgkmcnt(0)" ::: "memory");
  __builtin_amdgcn_s_barrier();

  if (wid >= 4) {
    srd_t sK = __builtin_amdgcn_make_buffer_rsrc((char*)Kp - 4096, (short)0, (int)(KV_BYTES + 4096u), 0x00020000);
    srd_t sV = __builtin_amdgcn_make_buffer_rsrc((char*)Vp - 4096, (short)0, (int)(KV_BYTES + 4096u), 0x00020000);
    srd_t sX = __builtin_amdgcn_make_buffer_rsrc((char*)Xp - 4096, (short)0, (int)(X_BYTES + 4096u), 0x00020000);
    srd_t sW = __builtin_amdgcn_make_buffer_rsrc((char*)Wq - 4096, (short)0, (int)(W_BYTES + 4096u), 0x00020000);
    srd_t sS = __builtin_amdgcn_make_buffer_rsrc(Ows, (short)0, (int)(QS_OFF + QS_BYTES), 0x00020000);
    unsigned vk[4], vv[4], vw[4], vx[4], vl[4];
    const unsigned row = (l >> 2) & 7u, cq = l & 3u, sub = l >> 5;
#pragma unroll
    for (int i = 0; i < 4; ++i) {
      const unsigned rf = 16u * w4 + 8u * (i >> 1) + row, cb = 128u * (i & 1) + 64u * sub, bias = 4096u - 1024u * i;
      vk[i] = rf * 256u + cb + 16u * (cq ^ ((rf >> 2) & 3u)) + bias;
      vv[i] = rf * 256u + cb + 16u * cq + bias;
      vw[i] = rf * 8192u + cb + 16u * cq + bias;
      const unsigned rx = 32u * w4 + 8u * i + row;
      vx[i] = rx < 112u ? rx * 1280u + 64u * sub + 16u * (cq ^ ((rx >> 2) & 3u)) + bias : 0x80000000u;
      vl[i] = w4 * 4096u + l * 16u;
    }
    const unsigned myfull = a_full + w4 * 4u, m0b = ldsb + w4 * 4096u;
    constexpr unsigned NONE = 0x80000000u;
    unsigned t = 0, so = 0, freec = 0, dst = NONE; int pub = 1 - INFL; OPAQUE(pub); OPAQUE(dst); DBGX(unsigned npark = 0; unsigned nsp = 0;)
    auto pre = [&](unsigned newdst) {
      if (freec + RING <= t) {
        freec = min4(a_free);
        if (freec + RING <= t) {
          unsigned sp = 0;
          do { __builtin_amdgcn_s_sleep(1); freec = min4(a_free); if (++sp > SPIN_CAP) freec = 0x40000000u; } while (freec + RING <= t);
          DBGX(npark += 1; nsp += sp;)
        }
      }
      const unsigned d = __builtin_amdgcn_readlane(dst, so >> 14);
      if (__builtin_expect(d != NONE, 0)) {
        u32x4 v[4];
#pragma unroll
        for (int i = 0; i < 4; ++i) v[i] = *(const LAS u32x4*)(size_t)(m0b + so + l * 16u + i * 1024u);
#pragma unroll
        for (int i = 0; i < 4; ++i) __builtin_amdgcn_raw_buffer_store_b128(v[i], sS, d + vl[0] + i * 1024u, 0, 0);
      }
      if (d != newdst) dst = l == (so >> 14) ? newdst : dst;
    };
    auto post = [&]() { waitv<4 * INFL>(); lds_put(myfull, pub); ++pub; ++t; so = nxt(so); };
    const unsigned vn[4] = {NONE, NONE, NONE, NONE};
    auto scratch = [&](unsigned d) { pre(d); dma4<0>(m0b + so, vn, sS, 0u); post(); };
    const unsigned qs = QS_OFF + blockIdx.x * 131072u;
    for (unsigned r = 0; r < 4; ++r) {
      const unsigned h = h0 + r;
      const unsigned nq = (r == 0u ? 1u : 0u) + (r == q2pos ? 1u : 0u);
      bool direct = false;
#pragma unroll 1
      for (unsigned k = 0; k < nq; ++k) {
        const unsigned hp = (r == 0u && k == 0u) ? 0u : 2u;
        unsigned c = rot;
#pragma unroll 1
        for (unsigned i = 0; i < 10; ++i) {
          pre(NONE); dma4<0>(m0b + so, vx, sX, b * 143360u + c * 128u); post();
          pre(NONE); dma4<0>(m0b + so, vw, sW, c * 524288u + (h0 + hp) * 256u); post();
          pre(NONE); dma4<0>(m0b + so, vw, sW, c * 524288u + (h0 + hp) * 256u + 256u); post();
          c = c == 9u ? 0u : c + 1u;
        }
        direct = hp == r && k + 1u == nq;
#pragma unroll 1
        for (unsigned e = direct ? 2u : 0u; e < 4u; ++e) scratch(qs + hp * 32768u + e * 16384u);
      }
      if (!direct) {
        pre(NONE); dma4<0>(m0b + so, vl, sS, qs + r * 32768u); post();
        pre(NONE); dma4<0>(m0b + so, vl, sS, qs + r * 32768u + 16384u); post();
      }
      const unsigned kvo = (b * 32u + h) * 131072u;
      pre(NONE); dma4<KV_POL>(m0b + so, vk, sK, kvo); post();
#pragma unroll 1
      for (unsigned kt = 0; kt < 7; ++kt) {
        pre(NONE); dma4<KV_POL>(m0b + so, vk, sK, kvo + (kt + 1u) * 16384u); post();
        pre(NONE); dma4<KV_POL>(m0b + so, vv, sV, kvo + kt * 16384u); post();
      }
      pre(NONE); dma4<KV_POL>(m0b + so, vv, sV, kvo + 7u * 16384u); post();
      scratch((b * 32u + h) * O_ITEM);
      scratch(w4 == 3u ? NONE : (b * 32u + h) * O_ITEM + 16384u);
    }
    waitv<0>(); lds_put(myfull, t);
    t += RING - 2u; so = (so + (RING - 2u) * 16384u) & RMASK;
    pre(NONE); ++t; so = nxt(so); pre(NONE);
    DBGX(if (l == 0) { dbg_slot(Ows, wid)[0] = npark; dbg_slot(Ows, wid)[1] = nsp; })
    return;
  }

  const unsigned hh = l >> 5, l31 = l & 31u, q4 = (l >> 2) & 3u, blk = (l >> 4) & 1u, p4 = l & 3u, g4 = l >> 4, q15 = l & 15u;
  const unsigned sp4 = ((p4 & 1u) << 1) | (p4 >> 1);
  CW c; CS z; z.l = l;
  z.bk = ldsb + 2048u * (l31 >> 3) + 64u * (l31 & 7u) + 16u * (hh ^ ((l31 >> 2) & 3u));
  z.bv = ldsb + 64u * (4u * hh + q4) + 32u * blk + 8u * p4;
  unsigned bw = ldsb + 2048u * hh + 64u * q4 + 32u * blk + 8u * sp4;
  unsigned bx = ldsb + 4096u * w4 + 1024u * (l31 >> 3) + 64u * (l31 & 7u) + 16u * (hh ^ ((l31 >> 2) & 3u));
  z.a_full = a_full + 4u * p4; z.myfree = a_free + w4 * 4u; z.t = 0; z.so = 0; z.tv = 0; DBGX(z.nslow = 0; z.nspin = 0;)
  OPAQUE(z.tv); OPAQUE(z.myfree); OPAQUE(z.a_full); OPAQUE(z.bk); OPAQUE(z.bv); OPAQUE(bw); OPAQUE(bx);
  { const unsigned o1 = (g4 & 1u) == ((q15 >> 2) & 1u) ? 0x3f803f80u : 0u; const u32x4 o4 = {o1, o1, o1, o1}; c.ones = __builtin_bit_cast(bf16x8, o4); OPAQUE(c.ones); }
  STAMPR(11);
  DBGX(unsigned seg[9] = {}; unsigned tlast = 0;)
  STAMP(0);
  for (unsigned r = 0; r < 4; ++r) {
    const unsigned nq = (r == 0u ? 1u : 0u) + (r == q2pos ? 1u : 0u);
    bool direct = false;
#pragma unroll 1
    for (unsigned k = 0; k < nq; ++k) {
      const unsigned hp = (r == 0u && k == 0u) ? 0u : 2u;
      f32x16 qa[2][4];
#pragma unroll
      for (int e = 0; e < 8; ++e) qa[e >> 2][e & 3] = f32x16{};
      bf16x8 xf[4];
      unsigned ax = bx + z.so, ax1, aw, aw2 = 0;
      z.so = nxt(z.so); aw = bw + z.so; z.t += 1u;
      flag_wait(z, 1ull, z.t);
      OPAQUE(aw); OPAQUE(ax); ax1 = ax ^ 32u; OPAQUE(ax1);
      xf[0] = lds_b128(ax); xf[1] = lds_b128(ax1); xf[2] = lds_b128(ax + 512u);
#pragma unroll
      for (int g = 0; g < PD; ++g) c.fr[g] = wfrag(aw, g);
      GAP();
#pragma unroll 1
      for (unsigned i = 0; i < 10; ++i) {
        unsigned fl = 0; u64 bad = 0;
#pragma unroll
        for (int g = 0; g < 32; ++g) {
          qa[g >> 4][g & 3] = MFMA32(c.fr[g % NF], xf[(g >> 2) & 3], qa[g >> 4][g & 3]);
          GAP();
          if (g == 0) xf[3] = lds_b128(ax1 + 512u);
          if (g == 1) release(z, 1u);
          if (g == 2 || g == 14) fl = flag_ld(z.a_full);
          if (g == 3) { z.t += 1u; SPIN(z.t); z.so = nxt(z.so); SPIN(z.so); }
          if (g == 4) { aw2 = bw + z.so; OPAQUE(aw2); }
          if (g == 8) bad = flag_cmp(fl, z.t);
          if (g == 16 - PD) { release(z, 1u); flag_wait(z, bad, z.t); }
          if (g == 11) { z.t += 2u; SPIN(z.t); z.so = nxt(z.so); SPIN(z.so); }
          if (g == 12) { ax = bx + z.so; OPAQUE(ax); }
          if (g == 13) { ax1 = ax ^ 32u; OPAQUE(ax1); z.so = nxt(z.so); SPIN(z.so); }
          if (g == 15) { aw = bw + z.so; OPAQUE(aw); }
          if (g == 18) bad = flag_cmp(fl, z.t);
          if (g == 20) { flag_wait(z, bad, z.t); xf[0] = lds_b128(ax); }
          if (g == 24) xf[1] = lds_b128(ax1);
          if (g == 28) xf[2] = lds_b128(ax + 512u);
          if (g == 32 - PD) release(z, 1u);
          const int n = g + PD;
          c.fr[n % NF] = wfrag((n < 16 || n >= 32) ? aw : aw2, n & 15);
          if (g & 1) USE(c.fr[(g + 2) % NF]);
          GAP();
        }
      }
      z.t -= 1u; z.so = (z.so - 16384u) & RMASK;
      STAMP(1);
      direct = hp == r && k + 1u == nq;
      const unsigned bs = ldsb + w4 * 4096u + l * 16u;
      if (direct) {
#pragma unroll
        for (int e = 0; e < 8; ++e) c.qf[e] = pack8(qa[0][e >> 1], 8 * (e & 1), QSCALE);
      } else {
#pragma unroll
        for (int e = 0; e < 8; ++e) lds_w128(bs + (e < 4 ? z.so : nxt(z.so)) + (e & 3) * 1024u, pack8(qa[0][e >> 1], 8 * (e & 1), QSCALE));
        release(z, 2u); z.t += 2u; z.so = nxt(nxt(z.so));
        flag_wait(z, 1ull, z.t + 1u);
      }
#pragma unroll
      for (int e = 0; e < 8; ++e) lds_w128(bs + (e < 4 ? z.so : nxt(z.so)) + (e & 3) * 1024u, pack8(qa[1][e >> 1], 8 * (e & 1), QSCALE));
      release(z, 2u); z.t += 2u; z.so = nxt(nxt(z.so));
      STAMP(2);
    }
    if (!direct) {
      flag_wait(z, 1ull, z.t + 1u);
      const unsigned bs = ldsb + w4 * 4096u + l * 16u;
#pragma unroll
      for (int e = 0; e < 8; ++e) c.qf[e] = lds_b128(bs + (e < 4 ? z.so : nxt(z.so)) + (e & 3) * 1024u);
      release(z, 2u); z.t += 2u; z.so = nxt(nxt(z.so));
    }
    STAMP(6);
    {
      flag_wait(z, 1ull, z.t);
      z.a0 = z.bk + z.so; OPAQUE(z.a0); z.a1 = z.a0 ^ 32u; OPAQUE(z.a1);
#pragma unroll
      for (int g = 0; g < PD; ++g) c.fr[g] = kfrag(z.a0, z.a1, g);
      GAP();
      PH p{};
      gapF<0>(c, z, p);
      STAMP(3);
      sm_ops<1, NEC + 8, NOPS>(c, z);
      z.m = 0.f; c.lsum = f32x4{};
      new_ref<0, true>(c, z, xhalf_max(z.mx));
      GAP();
    }
    STAMP(4);
#pragma unroll 1
    for (unsigned it = 0; it < 3; ++it) { period<0, true>(c, z); period<1, true>(c, z); }
    period<0, false>(c, z);
    STAMP(5);
    sm_ops<1, 0, after_pack(3)>(c, z);
    GAP();
    gapL<0>(c, z);
    z.t += 1u; z.so = nxt(z.so);
    STAMP(7);
    flag_wait(z, 1ull, z.t + 1u);
    const float inv = __builtin_amdgcn_rcpf(c.lsum[0]);
    const unsigned ao = ldsb + (w4 < 2u ? z.so : nxt(z.so)) + (2u * (w4 & 1u) + blk) * 4096u + hh * 256u + q15 * 16u;
#pragma unroll
    for (int k = 0; k < 4; ++k) { lds_w128(ao + k * 1024u, pack8(c.acc[k], 0, inv)); lds_w128(ao + k * 1024u + 512u, pack8(c.acc[k], 8, inv)); }
    release(z, 2u); z.t += 2u; z.so = nxt(nxt(z.so));
    STAMP(8);
  }
  STAMPR(12);
  DBGX(if (l == 0) for (int i = 0; i < 9; ++i) dbg_slot(Ows, wid)[i] = seg[i];)
  DBGX(if (l == 0) { dbg_slot(Ows, wid)[9] = z.nslow; dbg_slot(Ows, wid)[10] = z.nspin; })
}

constexpr unsigned K2_BUF = 12288u, K2_WAVE = 3u * K2_BUF, LDS2 = 4u * K2_WAVE;
__global__ __launch_bounds__(256, 1) void oproj_k(const void* __restrict__ Ows, const void* __restrict__ Wo, __hip_bfloat16* __restrict__ out) {
  extern __shared__ __attribute__((aligned(16))) char lds[];
  const unsigned ldsb = (unsigned)(size_t)lds;
  const unsigned l = threadIdx.x & 63u, w = rfl(threadIdx.x >> 6);
  const unsigned g = blockIdx.x, xcd = g & 7u, sl = g >> 3, bi = sl >> 3, b = xcd * 4u + bi, j = sl & 7u;
  srd_t sO = __builtin_amdgcn_make_buffer_rsrc((void*)Ows, (short)0, (int)O_BYTES, 0x00020000);
  srd_t sW = __builtin_amdgcn_make_buffer_rsrc((void*)Wo, (short)0, (int)W_BYTES, 0x00020000);
  unsigned vwo[5], vo[4];
#pragma unroll
  for (int i = 0; i < 5; ++i) { const unsigned c = 64u * i + l; vwo[i] = (c / 10u) * 1280u + (c % 10u) * 16u + j * 160u - (i < 4 ? 1024u * i : 0u); }
#pragma unroll
  for (int i = 0; i < 4; ++i) vo[i] = l * 16u + i * (4096u - 1024u);
  const unsigned vw4[4] = {vwo[0], vwo[1], vwo[2], vwo[3]};
  const unsigned base = ldsb + w * K2_WAVE;
  const unsigned rot = (j + 2u * bi) & 7u;
  auto issue = [&](unsigned u, unsigned buf) {
    const unsigned ur = (u + rot) & 7u, h = (u >> 3) * 8u + 2u * w + (ur >> 2), ks = ur & 3u;
    const unsigned m = base + buf * K2_BUF;
    const unsigned so = (b * 32u + h) * O_ITEM + ks * 1024u;
    const unsigned sw = (h * 128u + ks * 32u) * 1280u;
    dma4<0>(m, vw4, sW, sw); dma1(m + 4096u, vwo[4], sW, sw);
    dma4<0>(m + 5120u, vo, sO, so);
#pragma unroll
    for (int i = 0; i < 3; ++i) dma1(m + 5120u + 4096u + 1024u * i, vo[0], sO, so + (4u + i) * 4096u);
  };
  f32x4 acc[5][7];
#pragma unroll
  for (int n = 0; n < 5; ++n)
#pragma unroll
    for (int q = 0; q < 7; ++q) acc[n][q] = f32x4{};
  const unsigned gq = l >> 4, q4 = (l >> 2) & 3u, p4 = l & 3u;
  const unsigned la = 160u * (16u * (gq >> 1) + 4u * (gq & 1u) + q4) + 8u * p4;
  issue(0, 0); issue(1, 1);
  unsigned buf = 0;
  for (unsigned u = 0; u < 32; ++u) {
    const unsigned nb = buf == 0 ? 2u : buf - 1u;
    if (u + 2u < 32u) { issue(u + 2u, nb); waitv<24>(); } else if (u + 1u < 32u) waitv<12>(); else waitv<0>();
    const unsigned m = base + buf * K2_BUF;
    bf16x8 af[5];
#pragma unroll
    for (int n = 0; n < 5; ++n) af[n] = lds_tr2(m + la + 32u * n, m + la + 32u * n + 1280u);
#pragma unroll
    for (int q = 0; q < 7; ++q) {
      const bf16x8 bf = lds_b128(m + 5120u + 1024u * q + 16u * l);
#pragma unroll
      for (int n = 0; n < 5; ++n) acc[n][q] = MFMA16(af[n], bf, acc[n][q]);
    }
    asm volatile("s_waitcnt lgkmcnt(0)" ::: "memory");
    buf = buf == 2u ? 0u : buf + 1u;
  }
  __syncthreads();
  LAS f32x4* red = (LAS f32x4*)(size_t)ldsb;
#pragma unroll
  for (int n = 0; n < 5; ++n)
#pragma unroll
    for (int q = 0; q < 7; ++q) red[(w * 35u + n * 7 + q) * 64u + l] = acc[n][q];
  __syncthreads();
  for (unsigned tI = w; tI < 35u; tI += 4u) {
    const f32x4 v = (red[tI * 64u + l] + red[(35u + tI) * 64u + l]) + (red[(70u + tI) * 64u + l] + red[(105u + tI) * 64u + l]);
    const unsigned n = tI / 7u, q = tI % 7u;
    u32x2 o = {cvtpk(v[0], v[1]), cvtpk(v[2], v[3])};
    *(u32x2*)((char*)out + (size_t)(b * 112u + q * 16u + (l & 15u)) * 1280u + j * 160u + n * 32u + gq * 8u) = o;
  }
}

extern "C" void kernel_launch(void* const* d_in, const int* in_sizes, int n_in, void* d_out, int out_size, void* d_ws, size_t ws_size,
                              hipStream_t stream) {
  (void)in_sizes; (void)n_in; (void)out_size; (void)ws_size;
  (void)hipFuncSetAttribute((const void*)attn_k, hipFuncAttributeMaxDynamicSharedMemorySize, (int)LDS1);
  (void)hipFuncSetAttribute((const void*)oproj_k, hipFuncAttributeMaxDynamicSharedMemorySize, (int)LDS2);
  attn_k<<<256, 512, LDS1, stream>>>(d_in[0], d_in[1], d_in[2], d_in[3], d_ws);
  oproj_k<<<256, 256, LDS2, stream>>>(d_ws, d_in[4], (__hip_bfloat16*)d_out);
}
```

```cpp
#include <hip/hip_runtime.h>
#include <hip/hip_bf16.h>
#include <utility>

typedef __bf16 bf16x8 __attribute__((ext_vector_type(8)));
typedef __bf16 bf16x4 __attribute__((ext_vector_type(4)));
typedef __bf16 bf16x2 __attribute__((ext_vector_type(2)));
typedef float f32x4 __attribute__((ext_vector_type(4)));
typedef float f32x2 __attribute__((ext_vector_type(2)));
typedef unsigned u32x4 __attribute__((ext_vector_type(4)));
typedef unsigned u32x2 __attribute__((ext_vector_type(2)));
typedef unsigned long long u64;
typedef __amdgpu_buffer_rsrc_t srd_t;
#define LAS __attribute__((address_space(3)))
#define DI __device__ __forceinline__
#define GAP() __builtin_amdgcn_sched_barrier(0)
#define OPAQUE(x) asm volatile("" : "+v"(x))
#define SPIN(x) asm volatile("" : "+s"(x))
#define BAR() do { asm volatile("" ::: "memory"); __builtin_amdgcn_s_barrier(); asm volatile("" ::: "memory"); } while (0)

constexpr unsigned KV_BYTES = 134217728u, X_BYTES = 3584u * 1280u, W_BYTES = 640u * 8192u;
constexpr unsigned P_UNIT = 14336u, P_BYTES = 320u * 8u * P_UNIT;
constexpr unsigned REG = 40960u, LDS_ALL = 163840u, FLAGS = 3u * REG + 38912u;
constexpr unsigned SPIN_CAP = 1u << 22;
constexpr float QSCALE = 0.12751743082f;
constexpr float THR = 8.0f;
constexpr unsigned NONE = 0x80000000u;

#define STAMP(i)
#define DBGX(x)

DI unsigned lane_id() { unsigned r; asm volatile("v_mbcnt_lo_u32_b32 %0, -1, 0\n\tv_mbcnt_hi_u32_b32 %0, -1, %0" : "=v"(r)); return r; }
DI unsigned rfl(unsigned x) { return __builtin_amdgcn_readfirstlane(x); }
DI unsigned cvtpk(float lo, float hi) { f32x2 v = {lo, hi}; bf16x2 b = __builtin_convertvector(v, bf16x2); return __builtin_bit_cast(unsigned, b); }
template <int N> DI void waitv() { asm volatile("s_waitcnt vmcnt(%c0)" ::"i"(N) : "memory"); }
DI void setm0(unsigned v) { asm volatile("s_mov_b32 m0, %0" ::"s"(v) : "m0", "memory"); }
template <int NT, int OFF> DI void dma(unsigned v, srd_t srd, unsigned soff) {
  if constexpr (NT) asm volatile("buffer_load_dwordx4 %0, %1, %2 offen offset:%c3 nt lds" ::"v"(v), "s"(srd), "s"(soff), "i"(OFF) : "memory");
  else asm volatile("buffer_load_dwordx4 %0, %1, %2 offen offset:%c3 lds" ::"v"(v), "s"(srd), "s"(soff), "i"(OFF) : "memory");
}
#define LGKM0() __builtin_amdgcn_s_waitcnt(0xC07F)
DI bf16x8 lds_b128(unsigned a) { return *(const LAS bf16x8*)(size_t)a; }
DI bf16x4 lds_tr(unsigned a) { return __builtin_amdgcn_ds_read_tr16_b64_v4bf16((LAS bf16x4*)(size_t)a); }
DI bf16x8 cat(bf16x4 lo, bf16x4 hi) { return __builtin_shufflevector(lo, hi, 0, 1, 2, 3, 4, 5, 6, 7); }
DI bf16x8 lds_tr2(unsigned a0, unsigned a1) {
  bf16x4 lo = __builtin_amdgcn_ds_read_tr16_b64_v4bf16((LAS bf16x4*)(size_t)a0);
  bf16x4 hi = __builtin_amdgcn_ds_read_tr16_b64_v4bf16((LAS bf16x4*)(size_t)a1);
  return __builtin_shufflevector(lo, hi, 0, 1, 2, 3, 4, 5, 6, 7);
}
DI bf16x8 pack2(const f32x4& a, const f32x4& b, float s) {
  u32x4 w = {cvtpk(a[0] * s, a[1] * s), cvtpk(a[2] * s, a[3] * s), cvtpk(b[0] * s, b[1] * s), cvtpk(b[2] * s, b[3] * s)};
  return __builtin_bit_cast(bf16x8, w);
}
DI float col_max(float v) {
  auto r = __builtin_amdgcn_permlane16_swap(__float_as_uint(v), __float_as_uint(v), false, false);
  v = fmaxf(__uint_as_float(r[0]), __uint_as_float(r[1]));
  auto s = __builtin_amdgcn_permlane32_swap(__float_as_uint(v), __float_as_uint(v), false, false);
  return fmaxf(__uint_as_float(s[0]), __uint_as_float(s[1]));
}
DI float col_sum(float v) {
  auto r = __builtin_amdgcn_permlane16_swap(__float_as_uint(v), __float_as_uint(v), false, false);
  v = __uint_as_float(r[0]) + __uint_as_float(r[1]);
  auto s = __builtin_amdgcn_permlane32_swap(__float_as_uint(v), __float_as_uint(v), false, false);
  return __uint_as_float(s[0]) + __uint_as_float(s[1]);
}

template <class F, int... Is> DI void sfor_(F&& f, std::integer_sequence<int, Is...>) { (f(std::integral_constant<int, Is>{}), ...); }
template <int N, class F> DI void sfor(F&& f) { sfor_(f, std::make_integer_sequence<int, N>{}); }
template <int T> DI void mfa(bf16x8 a, bf16x8 b) { asm volatile("v_mfma_f32_16x16x32_bf16 a[%c2:%c3], %0, %1, a[%c2:%c3]" ::"v"(a), "v"(b), "i"(4 * T + 4), "i"(4 * T + 7)); }
template <int T> DI void mfa0(bf16x8 a, bf16x8 b) { asm volatile("v_mfma_f32_16x16x32_bf16 a[%c2:%c3], %0, %1, 0" ::"v"(a), "v"(b), "i"(4 * T + 4), "i"(4 * T + 7)); }
DI float max5(float a, float b, float c, float d, float e) { float r; asm volatile("v_max3_f32 %0, %1, %2, %3\n\tv_max3_f32 %0, %0, %4, %5" : "=&v"(r) : "v"(a), "v"(b), "v"(c), "v"(d), "v"(e)); return r; }
template <int T, int R> DI void mfa0a(bf16x8 a) { asm volatile("v_mfma_f32_16x16x32_bf16 a[%c1:%c2], %0, a[%c3:%c4], 0" ::"v"(a), "i"(4 * T + 4), "i"(4 * T + 7), "i"(4 * R + 4), "i"(4 * R + 7)); }
DI unsigned lds_ld(unsigned a) { return *(volatile LAS unsigned*)(size_t)a; }
DI void lds_st(unsigned a, unsigned v) { *(volatile LAS unsigned*)(size_t)a = v; }
DI void wait_ge(unsigned a, unsigned need) {
  unsigned sp = 0;
  while (__builtin_amdgcn_ballot_w64(lds_ld(a) < need) != 0ull && ++sp < SPIN_CAP) __builtin_amdgcn_s_sleep(1);
  asm volatile("" ::: "memory");
}
DI void mfv(f32x4& d, bf16x8 a, bf16x8 b) { asm volatile("v_mfma_f32_16x16x32_bf16 %0, %1, %2, %0" : "+v"(d) : "v"(a), "v"(b)); }
DI f32x4 mfv0(bf16x8 a, bf16x8 b, f32x4 c) { f32x4 d; asm volatile("v_mfma_f32_16x16x32_bf16 %0, %1, %2, %3" : "=&v"(d) : "v"(a), "v"(b), "v"(c)); return d; }
template <int T> DI f32x4 acc_rd() {
  float x0, x1, x2, x3;
  asm volatile("v_accvgpr_read_b32 %0, a%c4\n\tv_accvgpr_read_b32 %1, a%c5\n\tv_accvgpr_read_b32 %2, a%c6\n\tv_accvgpr_read_b32 %3, a%c7"
               : "=v"(x0), "=v"(x1), "=v"(x2), "=v"(x3) : "i"(4 * T + 4), "i"(4 * T + 5), "i"(4 * T + 6), "i"(4 * T + 7));
  return f32x4{x0, x1, x2, x3};
}
template <int T> DI void acc_wr(f32x4 v) {
  asm volatile("v_accvgpr_write_b32 a%c4, %0\n\tv_accvgpr_write_b32 a%c5, %1\n\tv_accvgpr_write_b32 a%c6, %2\n\tv_accvgpr_write_b32 a%c7, %3"
               ::"v"(v[0]), "v"(v[1]), "v"(v[2]), "v"(v[3]), "i"(4 * T + 4), "i"(4 * T + 5), "i"(4 * T + 6), "i"(4 * T + 7));
}
template <int T> DI void acc_zero() {
  asm volatile("v_accvgpr_write_b32 a%c0, 0\n\tv_accvgpr_write_b32 a%c1, 0\n\tv_accvgpr_write_b32 a%c2, 0\n\tv_accvgpr_write_b32 a%c3, 0"
               ::"i"(4 * T + 4), "i"(4 * T + 5), "i"(4 * T + 6), "i"(4 * T + 7));
}
#define NOPS(n) asm volatile("s_nop " #n)
template <int V> using IC = std::integral_constant<int, V>;

#define HQ 63
#define HO 63
#define NSAFE 2u
struct AttGap { int ex, cv, tr, pc, m0, bk; };
constexpr AttGap att_gap(bool safe, int i, int g) {
  AttGap r{-1, 0, -1, -1, -1, 0};
  if (i == 1 && g >= 8) r.tr = g - 8;
  if (i == 2 && g < 7) r.tr = g + 9;
  if (safe) {
    if (g >= 5 && g < 13) r.ex = g - 5;
    if (g == 13) r.cv = 3;
    if (g == 14) r.cv = 12;
    if (i == 0 && g == 15) { r.pc = -1; r.bk = 1; r.m0 = 0; }
    if (i == 0 && g == 16) r.pc = 0;
    if (i >= 2) { r.pc = g == 3 ? 3 * i - 5 : g == 15 ? 3 * i - 4 : g == 16 ? 3 * i - 3 : -1;
      const int nx = g == 2 ? 3 * i - 5 : g == 14 ? 3 * i - 4 : g == 15 ? 3 * i - 3 : -1; if (nx >= 0 && (nx & 3) == 0) r.m0 = nx; }
    if (i == 3 && g == 4) r.bk = 2;
    if (i == 0 && g == 3) r.bk = 3;
    return r;
  }
  if (i == 0) {
    constexpr int ex[17] = {-1, -1, 0, 1, 2, 3, -1, 4, 5, 6, 7, -1, -1, -1, -1, -1, -1};
    r.ex = ex[g];
    if (g == 0) { r.m0 = 0; r.bk = 1; }
    if (g == 1) r.pc = 0;
    if (g == 6) r.bk = 3;
    if (g == 11) r.pc = 1;
    if (g == 12) r.cv = 3;
    if (g == 13) r.cv = 12;
    if (g == 16) r.pc = 2;
  } else if (i == 1) {
    if (g < 8) r.ex = g;
    if (g == 9 || g == 11 || g == 13 || g == 15) r.cv = 1 << ((g - 9) / 2);
    if (g == 8) r.pc = 3;
    if (g == 12) r.m0 = 4;
    if (g == 14) r.pc = 4;
  } else if (i == 2) {
    if (g >= 7 && g < 15) r.ex = g - 7;
    if (g == 15) r.cv = 3;
    if (g == 16) r.cv = 12;
    if (g == 4) r.pc = 5;
    if (g == 15) r.pc = 6;
  } else if (i == 6) {
    if (g >= 8 && g < 16) r.ex = g - 8;
    if (g == 16) r.cv = 15;
    if (g == 2) r.pc = 14;
    if (g == 7) r.pc = 15;
  } else {
    constexpr int ex[17] = {0, 1, -1, 2, 3, -1, 4, 5, -1, -1, 6, 7, -1, -1, -1, -1, -1};
    r.ex = ex[g];
    if (g == 13) r.cv = 3;
    if (g == 14) r.cv = 12;
    if (i == 3) { if (g == 2) r.pc = 7; if (g == 5) r.m0 = 8; if (g == 9) r.pc = 8; if (g == 16) r.pc = 9; if (g == 12) r.bk = 2; }
    if (i == 4) { if (g == 5) r.pc = 10; if (g == 12) r.pc = 11; if (g == 15) r.m0 = 12; }
    if (i == 5) { if (g == 2) r.pc = 12; if (g == 9) r.pc = 13; }
  }
  return r;
}
constexpr int att_pieces_before(bool safe, int i, int g) { int n = 0; for (int k = 0; k < 17 * i + g; ++k) n += att_gap(safe, k / 17, k % 17).pc >= 0; return n; }
static_assert(att_pieces_before(true, 7, 0) == 16 && att_pieces_before(false, 7, 0) == 16, "pieces");

#define NHX 16
DI void map_block(unsigned g, unsigned& b, unsigned& hg, unsigned& sh) {
  constexpr unsigned HG = NHX / 4, NXH = 32 / NHX, NBX = 32 / HG;
  const unsigned xcd = g & 7u, sl = g >> 3, j = sl % HG, bi = sl / HG;
  b = (xcd / NXH) * NBX + bi; hg = (xcd % NXH) * HG + j;
  sh = ((bi * 10u) / NBX + (j * 10u) / HG) % 10u;
}

__global__ __launch_bounds__(256, 1) void fused_k(const void* __restrict__ Xp, const void* __restrict__ Kp, const void* __restrict__ Vp,
                                                  const void* __restrict__ Wq, const void* __restrict__ Wo, void* __restrict__ Pws) {
  extern __shared__ __attribute__((aligned(16))) char lds[];
  const unsigned ldsb = (unsigned)(size_t)lds;
  const unsigned l = threadIdx.x & 63u, w = rfl(threadIdx.x >> 6);
  const bool early = w < 2u;
  unsigned b, hg, rot;
  map_block(blockIdx.x, b, hg, rot);
  const unsigned h = 4u * hg + w;
  const unsigned g4 = l >> 4, q15 = l & 15u, q4 = (l >> 2) & 3u, p4 = l & 3u;
  DBGX(unsigned seg[8]; unsigned segr[8];)
  STAMP(0);
  srd_t sK = __builtin_amdgcn_make_buffer_rsrc((char*)Kp - 4096, (short)0, (int)(KV_BYTES + 4096u), 0x00020000);
  srd_t sV = __builtin_amdgcn_make_buffer_rsrc((char*)Vp - 4096, (short)0, (int)(KV_BYTES + 4096u), 0x00020000);
  srd_t sX = __builtin_amdgcn_make_buffer_rsrc((char*)Xp - 4096, (short)0, (int)(X_BYTES + 4096u), 0x00020000);
  srd_t sW = __builtin_amdgcn_make_buffer_rsrc((char*)Wq - 4096, (short)0, (int)(W_BYTES + 4096u), 0x00020000);
  const unsigned ring = ldsb + w * REG;
  const unsigned fl = ldsb + FLAGS;
  const unsigned drow = (l >> 2) & 7u, dsub = l >> 5;
  const unsigned kvo = (b * 32u + h) * 131072u;
  unsigned vk[4], vv[2];
#pragma unroll
  for (int i = 0; i < 4; ++i) vk[i] = drow * 256u + 64u * dsub + 16u * (p4 ^ (i & 2u)) + ((i & 1) ? 3200u : 4096u);
#pragma unroll
  for (int i = 0; i < 2; ++i) vv[i] = drow * 256u + 64u * dsub + 16u * (p4 ^ (2u * (drow >> 2))) + (i ? 3200u : 4096u);
  auto chunk = [&](unsigned i) { unsigned c = i + rot; return c >= 10u ? c - 10u : c; };
  auto xoff = [&](unsigned rx, int i) { return rx < 112u ? (b * 112u + rx) * 1280u + 64u * dsub + 16u * (p4 ^ (2u * (i & 1))) + 4096u - 1024u * i : NONE; };
  unsigned bx = ldsb + 1024u * (q15 >> 3) + 64u * (q15 & 7u) + 16u * (g4 ^ (2u * (q15 >> 3)));
  OPAQUE(bx);
  auto xfrag = [&](unsigned a0, unsigned a1, int q) { return lds_b128((q < 4 ? a0 : a1) + ((q >> 1) & 1) * REG + (q & 1) * 2048u); };
  unsigned vw[4];
#pragma unroll
  for (int i = 0; i < 4; ++i) vw[i] = (8u * (i >> 1) + drow) * 8192u + 128u * (i & 1) + 64u * dsub + 16u * p4 + h * 256u + 4096u - 1024u * i;

  asm volatile("" ::: "a255");
  bf16x8 qf[4][7];
  const unsigned hb = ldsb + (w & 1u) * REG + 24576u + l * 16u;
  {
    unsigned v1[4], vx[4];
#pragma unroll
    for (int i = 0; i < 4; ++i) {
      v1[i] = (8u * i + drow) * 8192u + 64u * dsub + 16u * p4 + (4u * hg + (w & 1u)) * 256u + (w >> 1) * 128u + 4096u - 1024u * i;
      vx[i] = xoff(32u * w + 8u * i + drow, i);
    }
    const unsigned mx0 = ring + 24576u;
    auto allW = [&](unsigned s) { setm0(ring + (((s >> 1) + 2u) % 3u) * 8192u + (s & 1u) * 4096u); NOPS(0);
      sfor<4>([&](auto P) { dma<0, 1024 * P>(v1[P], sW, (2u * chunk(s >> 1) + (s & 1u)) * 262144u); }); };
    auto allX = [&](unsigned i) { setm0(mx0 + i * 4096u); NOPS(0); sfor<4>([&](auto P) { dma<0, 1024 * P>(vx[P], sX, chunk(i) * 128u); }); };
    unsigned bw = ring + 1024u * g4 + 64u * q4 + 16u * p4;
    OPAQUE(bw);
    allX(0); allW(0); allW(1);
    sfor<28>([&](auto T) { acc_zero<T>(); });
    allX(1); allW(2); allW(3); allX(2); allW(4); allW(5);
    bf16x8 af[2][4], bf[2][7];
    waitv<28>(); BAR();
    STAMP(1);
#pragma unroll
    for (int d = 0; d < 4; ++d) { const unsigned a = bw + 16384u + 512u * (d >> 1) + 8u * (d & 1); af[0][d] = lds_tr2(a, a + 256u); }
#pragma unroll
    for (int q = 0; q < 7; ++q) bf[0][q] = xfrag(bx + 24576u, bx + 24576u + 2u * REG, q);
    LGKM0();
    unsigned sl = 16384u, xb = 0, cn = chunk(3);
    unsigned aw = bw + 16384u + 4096u, ax = bx + 24576u + 512u, ax2 = ax + 2u * REG;
    OPAQUE(aw); OPAQUE(ax); OPAQUE(ax2);
    srd_t sT = sW; unsigned so = cn * 524288u, hs = 262144u;
#pragma unroll 1
    for (unsigned i = 0; i < 10; ++i) {
      if (i >= 7u) {
        const unsigned j = i - 7u;
        if (early) {
#pragma unroll
          for (int k = 0; k < 4; ++k) v1[k] = j == 1u ? vv[k & 1] : vk[k];
          sT = j == 1u ? sV : sK; so = kvo + (j >> 1) * 8192u; hs = 4096u;
        } else {
#pragma unroll
          for (int k = 0; k < 4; ++k) v1[k] = vw[k];
          so = (2u * chunk(j >> 1) + (j & 1u)) * 262144u; hs = 131072u;
        }
      }
      const unsigned sln = sl == 16384u ? 0u : sl + 8192u, xbn = (xb + 4096u) & 12288u;
      sfor<2>([&](auto E) {
        constexpr int e = E;
        bf16x4 lo;
        unsigned awn = 0, axn = 0;
        sfor<28>([&](auto T) {
          constexpr int t = T, d = t / 7, q = t % 7;
          mfa<t>(af[e][d], bf[e][q]);
          if constexpr (t == 0) { if constexpr (e == 0) waitv<24>(); else waitv<18>(); }
          if constexpr (t == 1) { if constexpr (e == 1) BAR(); setm0(ring + sl + e * 4096u); }
          if constexpr (t % 3 == 2 && t / 3 < 8) {
            constexpr int j = t / 3, dd = j >> 1; const unsigned a = aw + 512u * (dd >> 1) + 8u * (dd & 1);
            if constexpr ((j & 1) == 0) lo = lds_tr(a); else af[e ^ 1][dd] = cat(lo, lds_tr(a + 256u));
          }
          if constexpr (t % 3 == 0 && t >= 3 && t <= 21) bf[e ^ 1][t / 3 - 1] = xfrag(ax, ax2, t / 3 - 1);
          if constexpr (t == 4 || t == 7 || t == 13 || t == 16) { constexpr int p = t == 4 ? 0 : t == 7 ? 1 : t == 13 ? 2 : 3; dma<0, 1024 * p>(v1[p], sT, so); }
          if constexpr (t == 19) setm0(mx0 + ((xb + 12288u) & 12288u));
          if constexpr (t == 22 || t == 25) { constexpr int p = 2 * e + (t == 25); if (i < 9u) dma<0, 1024 * p>(vx[p], sX, cn * 128u); }
          if constexpr (t == 10) { awn = e == 0 ? bw + sln : aw + 4096u; OPAQUE(awn); }
          if constexpr (t == 24) { axn = e == 0 ? bx + 24576u + xbn : ax + 512u; OPAQUE(axn); }
          if constexpr (t == 26) { ax2 = axn + 2u * REG; OPAQUE(ax2); if constexpr (e == 0) { so += hs; SPIN(so); } }
          if constexpr (t == 27) { LGKM0(); aw = awn; ax = axn; }
          GAP();
        });
      });
      sl = sln; xb = xbn; cn = cn == 9u ? 0u : cn + 1u; so = cn * 524288u;
    }
    NOPS(7);
    sfor<14>([&](auto T) { constexpr int k = T / 7, q = T % 7; qf[k][q] = pack2(acc_rd<14 * k + q>(), acc_rd<14 * k + 7 + q>(), QSCALE); OPAQUE(qf[k][q]); });
    waitv<8>();
    if (!early) {
      sfor<14>([&](auto T) { *(LAS bf16x8*)(size_t)(hb + (T / 7) * 2u * REG + (T % 7) * 1024u) = qf[T / 7][T % 7]; });
      if (w == 3u && l < 8u) lds_st(fl + l * 4u, 0u);
    }
    LGKM0(); BAR();
  }
  STAMP(2);
  if (early) {
    sfor<14>([&](auto T) { qf[2 + T / 7][T % 7] = lds_b128(hb + (T / 7) * 2u * REG + (T % 7) * 1024u); });
    LGKM0(); BAR();
    sfor<8>([&](auto P) { if constexpr ((P & 3) == 0) { setm0(ring + 24576u + P * 1024u); NOPS(0); } dma<1, 1024 * (P & 3)>(vv[P & 1], sV, kvo + 8192u + (P >> 2) * 4096u); });
  } else {
    BAR();
    unsigned vx[8];
#pragma unroll
    for (int i = 0; i < 8; ++i) vx[i] = xoff(64u * (w & 1u) + 8u * i + drow, i & 3);
    const unsigned mx0 = ldsb + (w & 1u) * 2u * REG + 32768u;
    const unsigned myf = fl + 16u + (w & 1u) * 4u, otf = fl + 16u + ((w & 1u) ^ 1u) * 4u;
    unsigned bw = ring + 2048u * g4 + 64u * q4 + 16u * p4;
    OPAQUE(bw);
    sfor<8>([&](auto P) { if constexpr ((P & 3) == 0) { setm0(ring + 24576u + P * 1024u); NOPS(0); } dma<0, 1024 * (P & 3)>(vw[P & 3], sW, (2u * chunk(1) + 1u) * 262144u + (P >> 2) * 131072u); });
    sfor<56>([&](auto T) { acc_zero<T>(); });
    bf16x8 af[2][8], bf[2][7];
#pragma unroll
    for (int d = 0; d < 8; ++d) { const unsigned a = bw + 512u * (d >> 1) + 8u * (d & 1); af[0][d] = lds_tr2(a, a + 256u); if (d < 7) bf[0][d] = xfrag(bx + 32768u, bx + 32768u + 2u * REG, d); }
    LGKM0();
    unsigned par = 0, cn = chunk(2), rn = 1;
    unsigned aw = bw + 8192u, ax = bx + 32768u + 512u, ax2 = ax + 2u * REG, rv = 1;
    OPAQUE(aw); OPAQUE(ax); OPAQUE(ax2); OPAQUE(rv);
    auto round = [&](auto TAIL) {
      constexpr bool tail = TAIL;
      unsigned so = tail ? kvo + par * 8192u : cn * 524288u, so2 = 0;
      sfor<2>([&](auto E) {
        constexpr int e = E;
        bf16x4 lo;
        unsigned awn = 0, axn = 0, pv = 0; u64 bad = 0;
        sfor<56>([&](auto T) {
          constexpr int t = T, d = t / 7, q = t % 7;
          mfa<t>(af[e][d], bf[e][q]);
          if constexpr (t == 0) { if constexpr (e == 0) waitv<16>(); else waitv<8>(); }
          if constexpr (e == 1) {
            if constexpr (t == 2) lds_st(myf, rv);
            if constexpr (t == 8) pv = lds_ld(otf);
            if constexpr (t == 14) { bad = __builtin_amdgcn_ballot_w64(pv < rn); SPIN(bad); }
            if constexpr (t == 17) { GAP(); if (__builtin_expect(bad != 0ull, 0)) wait_ge(otf, rn); asm volatile("" ::: "memory"); }
          }
          if constexpr (t % 3 == 1 && t / 3 < 16) {
            constexpr int j = t / 3, dd = j >> 1; const unsigned a = aw + 512u * (dd >> 1) + 8u * (dd & 1);
            if constexpr ((j & 1) == 0) lo = lds_tr(a); else af[e ^ 1][dd] = cat(lo, lds_tr(a + 256u));
          }
          if constexpr (t % 3 == 2 && t >= 20 && t <= 44 && t != 26 && t != 38) { constexpr int qq = (t - 20) / 3 - (t > 26) - (t > 38); bf[e ^ 1][qq] = xfrag(ax, ax2, qq); }
          constexpr int np = (e == 1 && !tail) ? 16 : 8;
          constexpr auto gap_of = [](int p) { return 3 + 3 * ((p * 16) / np); };
          sfor<np>([&](auto P) {
            constexpr int p = P;
            if constexpr ((p & 3) == 0 && t == gap_of(p) - 1) setm0(p >= 8 ? mx0 + par * 4096u + ((p - 8) >> 2) * REG : ring + par * 16384u + e * 8192u + p * 1024u);
            if constexpr (t == gap_of(p)) {
              if constexpr (HQ < 63) waitv<HQ - 1>();
              if constexpr (p >= 14) { if (w == 2u) dma<0, 1024 * (p & 3)>(vx[p & 7], sX, cn * 128u); }
              else if constexpr (p >= 8) dma<0, 1024 * (p & 3)>(vx[p & 7], sX, cn * 128u);
              else if constexpr (!tail) dma<0, 1024 * (p & 3)>(vw[p & 3], sW, p < 4 ? so : so2);
              else if constexpr (e == 0) dma<1, 1024 * (p & 3)>(vk[p & 3], sK, p < 4 ? so : so2);
              else dma<1, 1024 * (p & 3)>(vv[p & 1], sV, p < 4 ? so : so2);
            }
          });
          if constexpr (t == 5) { so2 = so + (tail ? 4096u : 131072u); SPIN(so2); }
          if constexpr (t == 47) { awn = e == 0 ? bw + (par ^ 1u) * 16384u : aw + 8192u; OPAQUE(awn); }
          if constexpr (t == 50) { axn = e == 0 ? bx + 32768u + (par ^ 1u) * 4096u : ax + 512u; OPAQUE(axn); }
          if constexpr (t == 52) { ax2 = axn + 2u * REG; OPAQUE(ax2); }
          if constexpr (t == 53 && e == 1) { rv += 1u; OPAQUE(rv); }
          if constexpr (t == 55) { LGKM0(); aw = awn; ax = axn; if constexpr (e == 0 && !tail) { so += 262144u; SPIN(so); } }
          GAP();
        });
      });
      par ^= 1u; cn = cn == 9u ? 0u : cn + 1u; rn += 1u;
    };
#pragma unroll 1
    for (unsigned i = 0; i < 8; ++i) round(std::false_type{});
#pragma unroll 1
    for (unsigned i = 8; i < 10; ++i) round(std::true_type{});
    lds_st(myf, 11u);
    NOPS(7);
    sfor<28>([&](auto T) { constexpr int k = T / 7, q = T % 7; qf[k][q] = pack2(acc_rd<14 * k + q>(), acc_rd<14 * k + 7 + q>(), QSCALE); OPAQUE(qf[k][q]); });
  }
  STAMP(3);
  {
    bf16x8 ones = __builtin_bit_cast(bf16x8, u32x4{0x3f803f80u, 0x3f803f80u, 0x3f803f80u, 0x3f803f80u});
    OPAQUE(ones);
    unsigned nsafe = NSAFE;
#pragma unroll 1
    for (;;) {
      const unsigned l = lane_id(), g4 = l >> 4, q15 = l & 15u, q4 = (l >> 2) & 3u, p4 = l & 3u;
      unsigned ak = ring + 2048u * (q15 >> 3) + 64u * (q15 & 7u) + 16u * (g4 ^ (2u * (q15 >> 3)));
      unsigned av = ring + 8192u + 2048u * (g4 >> 1) + 64u * (4u * (g4 & 1u) + q4) + 16u * ((p4 >> 1) ^ (2u * (g4 & 1u))) + 8u * (p4 & 1u), av1 = av ^ 32u;
      OPAQUE(ak); OPAQUE(av); OPAQUE(av1);
      bf16x8 kf[2][4], vf[8], pp;
      f32x4 sp0, sp1, nm[7];
#pragma unroll
      for (int d = 0; d < 8; ++d) vf[d] = bf16x8{};
#pragma unroll
      for (int q = 0; q < 7; ++q) { nm[q] = f32x4{}; OPAQUE(nm[q]); }
      pp = bf16x8{}; sp0 = f32x4{-1e30f, -1e30f, -1e30f, -1e30f}; sp1 = sp0;
      OPAQUE(pp);
      waitv<24>();
#pragma unroll
      for (int e = 0; e < 8; ++e) kf[e >> 2][e & 3] = lds_b128(ak + 4096u * (e >> 2) + 512u * (e & 3));
      ak += 16384u;
      auto check = [&](auto Q, u64 big, float mx, bool first) {
        constexpr int q = Q;
        if (__builtin_expect(big != 0ull, 0)) {
          float d = col_max(mx);
          NOPS(7);
          if (first) sfor<9>([&](auto T) { acc_zero<7 * T + q>(); });
          else {
            d = fmaxf(d, 0.f);
            const float al = __builtin_amdgcn_exp2f(-d);
            sfor<9>([&](auto T) { acc_wr<7 * T + q>(acc_rd<7 * T + q>() * al); });
          }
          nm[q] -= d; sp0 -= d; sp1 -= d;
          NOPS(1);
        }
      };
      unsigned so = kvo + 16384u, mk = ring, dl = 16384u;
      unsigned t0 = __builtin_bit_cast(unsigned, THR), t1 = 0xff800000u;
      auto step = [&](unsigned s, auto SAFE) {
        constexpr bool safe = SAFE;
        const int nrec = s < 14u ? (int)(KV_BYTES + 4096u) : 0;
        srd_t sKs = __builtin_amdgcn_make_buffer_rsrc((char*)Kp - 4096, (short)0, nrec, 0x00020000);
        srd_t sVs = __builtin_amdgcn_make_buffer_rsrc((char*)Vp - 4096, (short)0, nrec, 0x00020000);
        unsigned so2 = 0; bf16x4 lo;
        sfor<7>([&](auto I) {
          constexpr int i = I, qs = (i + 6) % 7, qp = (i + 5) % 7;
          f32x4 s0, s1; float mx; u64 big; u32x4 pn;
          sfor<17>([&](auto G) {
            constexpr int g = G;
            constexpr AttGap ag = att_gap(safe, i, g);
            if constexpr (g == 0 && i == 6) waitv<8 + att_pieces_before(safe, 6, 0)>();
            if constexpr (g == 8 && i == 1) waitv<16 + att_pieces_before(safe, 1, 8)>();
            if constexpr (g < 8) {
              if constexpr (g == 0) s0 = mfv0(kf[0][0], qf[0][i], nm[i]);
              else if constexpr (g == 1) s1 = mfv0(kf[1][0], qf[0][i], nm[i]);
              else mfv((g & 1) ? s1 : s0, kf[g & 1][g >> 1], qf[g >> 1][i]);
            } else if constexpr (g < 16) mfa<7 * (g - 8) + qp>(vf[g - 8], pp);
            else mfa<56 + qp>(ones, pp);
            GAP();
            if constexpr (i == 6 && g < 8) kf[g & 1][g >> 1] = lds_b128(ak + 4096u * (g & 1) + 512u * (g >> 1));
            if constexpr (ag.tr >= 0) { constexpr int d = ag.tr >> 1; const unsigned a = ((d & 1) ? av1 : av) + 512u * (d >> 1);
              if constexpr ((ag.tr & 1) == 0) lo = lds_tr(a); else vf[d] = cat(lo, lds_tr(a + 4096u)); }
            if constexpr (safe) {
              if constexpr (g == 0) mx = max5(sp0[0], sp0[1], sp0[2], sp0[3], sp1[0]);
              if constexpr (g == 1) mx = max5(mx, sp1[1], sp1[2], sp1[3], sp1[3]);
              if constexpr (g == 2) { big = __builtin_amdgcn_ballot_w64(mx > __uint_as_float(i == 0 ? t0 : t1)); SPIN(big); }
              if constexpr (g == 4) check(IC<qs>{}, big, mx, (i == 0 ? t0 : t1) == 0xff800000u);
              if constexpr (i == 0 && g == 16) { t0 = t1; SPIN(t0); }
            }
            if constexpr (ag.ex >= 0 && ag.ex < 4) { sp0[ag.ex] = __builtin_amdgcn_exp2f(sp0[ag.ex]); OPAQUE(sp0[ag.ex]); }
            if constexpr (ag.ex >= 4) { sp1[ag.ex - 4] = __builtin_amdgcn_exp2f(sp1[ag.ex - 4]); OPAQUE(sp1[ag.ex - 4]); }
            if constexpr (ag.cv & 1) { pn[0] = cvtpk(sp0[0], sp0[1]); OPAQUE(pn[0]); }
            if constexpr (ag.cv & 2) { pn[1] = cvtpk(sp0[2], sp0[3]); OPAQUE(pn[1]); }
            if constexpr (ag.cv & 4) { pn[2] = cvtpk(sp1[0], sp1[1]); OPAQUE(pn[2]); }
            if constexpr (ag.cv & 8) { pn[3] = cvtpk(sp1[2], sp1[3]); OPAQUE(pn[3]); }
            if constexpr (ag.pc >= 0) { constexpr int p = ag.pc;
              if constexpr (p < 8) dma<1, 1024 * (p & 3)>(vk[p & 3], sKs, p < 4 ? so : so2); else dma<1, 1024 * (p & 3)>(vv[p & 1], sVs, p < 12 ? so : so2); }
            if constexpr (ag.m0 >= 0) setm0(mk + ag.m0 * 1024u);
            if constexpr (ag.bk == 1) { so2 = so + 4096u; SPIN(so2); }
            if constexpr (ag.bk == 2) { av += dl; OPAQUE(av); av1 += dl; OPAQUE(av1); }
            if constexpr (ag.bk == 3) { ak += dl; OPAQUE(ak); }
            if constexpr (i == 6 && g == 15) LGKM0();
            GAP();
          });
          pp = __builtin_bit_cast(bf16x8, pn); sp0 = s0; sp1 = s1;
        });
        so += 8192u; mk += dl; dl = 0u - dl;
        if constexpr (safe) t1 = __builtin_bit_cast(unsigned, THR);
      };
      ak -= 16384u;
      unsigned s = 0;
#pragma unroll 1
      for (; s < nsafe; ++s) step(s, std::true_type{});
#pragma unroll 1
      for (; s < 16u; ++s) step(s, std::false_type{});
      NOPS(1);
      sfor<8>([&](auto D) { mfa<7 * D + 5>(vf[D], pp); });
      mfa<61>(ones, pp);
      GAP();
      float mx = max5(sp0[0], sp0[1], sp0[2], sp0[3], sp1[0]); mx = max5(mx, sp1[1], sp1[2], sp1[3], sp1[3]);
      check(IC<6>{}, __builtin_amdgcn_ballot_w64(mx > THR), mx, false);
#pragma unroll
      for (int k = 0; k < 4; ++k) { sp0[k] = __builtin_amdgcn_exp2f(sp0[k]); sp1[k] = __builtin_amdgcn_exp2f(sp1[k]); }
      pp = pack2(sp0, sp1, 1.f); OPAQUE(pp);
      NOPS(1);
      sfor<8>([&](auto D) { mfa<7 * D + 6>(vf[D], pp); });
      mfa<62>(ones, pp);
      NOPS(7);
      bool bad = false;
      sfor<7>([&](auto Q) { bad |= !(acc_rd<56 + Q>()[0] < 1.0995116e12f); });
      if (__builtin_expect(nsafe == 16u || __builtin_amdgcn_ballot_w64(bad) == 0ull, 1)) break;
      nsafe = 16u;
      waitv<0>();
      sfor<32>([&](auto P) { constexpr int p = P; if constexpr ((p & 3) == 0) { setm0(ring + p * 1024u); NOPS(0); }
        if constexpr ((p >> 3) & 1) dma<1, 1024 * (p & 3)>(vv[p & 1], sV, kvo + (p >> 4) * 8192u + ((p >> 2) & 1) * 4096u);
        else dma<1, 1024 * (p & 3)>(vk[p & 3], sK, kvo + (p >> 4) * 8192u + ((p >> 2) & 1) * 4096u); });
    }
  }
  STAMP(4);
  {
    const unsigned l = lane_id(), g4 = l >> 4, q15 = l & 15u, q4 = (l >> 2) & 3u, p4 = l & 3u, drow = (l >> 2) & 7u;
    srd_t sO = __builtin_amdgcn_make_buffer_rsrc((char*)Wo - 4096, (short)0, (int)(W_BYTES + 4096u), 0x00020000);
    srd_t sP = __builtin_amdgcn_make_buffer_rsrc(Pws, (short)0, (int)P_BYTES, 0x00020000);
    unsigned vo[4];
#pragma unroll
    for (int i = 0; i < 4; ++i) vo[i] = (hg * 512u + 8u * i + drow) * 1280u + (w & 1u) * 640u + 64u * (l >> 5) + 16u * (p4 ^ (2u * (drow >> 2))) + 4096u - 1024u * i;
    const unsigned slot0 = ring + 28672u;
    const unsigned rot8 = (rot * 13u) >> 4, sg0 = early ? 0u : 8u, sg1 = early ? 8u : 10u;
    auto cchunk = [&](unsigned sg) { return sg < 5u ? sg : sg < 8u ? 9u - sg : (sg & 1u); };
    auto sbase = [&](unsigned sg) { return (sg >= 5u ? 327680u : 0u) + cchunk(sg) * 128u; };
    auto allO = [&](unsigned u) { setm0(slot0 + u * 4096u); NOPS(0); sfor<4>([&](auto P) { dma<0, 1024 * P>(vo[P], sO, sbase(sg0) + ((u + rot8) & 7u) * 40960u); }); };
    waitv<0>();
    wait_ge(fl + 16u + (l & 1u) * 4u, 11u);
    allO(0); allO(1);
    sfor<7>([&](auto Q) {
      constexpr int q = Q;
      const float inv = __builtin_amdgcn_rcpf(acc_rd<56 + q>()[0]);
      sfor<4>([&](auto K) { constexpr int k = K; *(LAS bf16x8*)(size_t)(ring + l * 16u + (k * 7 + q) * 1024u) = pack2(acc_rd<14 * k + q>(), acc_rd<14 * k + 7 + q>(), inv); });
    });
    unsigned ba = slot0 + 1024u * (g4 >> 1) + 64u * (4u * (g4 & 1u) + q4) + 16u * ((p4 >> 1) ^ (2u * (g4 & 1u))) + 8u * (p4 & 1u), bb = ldsb + l * 16u;
    OPAQUE(ba); OPAQUE(bb);
    bf16x8 af[2][4], bf[2][7], sel[2];
#pragma unroll
    for (int hf = 0; hf < 2; ++hf) {
      const unsigned one = g4 == (q15 >> 2) ? (p4 & 1u ? 0x3f800000u : 0x00003f80u) : 0u, lo = p4 < 2u ? one : 0u, hi = p4 < 2u ? 0u : one;
      sel[hf] = __builtin_bit_cast(bf16x8, hf ? u32x4{0u, 0u, lo, hi} : u32x4{lo, hi, 0u, 0u}); OPAQUE(sel[hf]);
    }
    if (early) { lds_st(fl + w * 4u, 1u); wait_ge(fl + (l & 1u) * 4u, 1u); }
    else { DBGX(STAMP(5);) LGKM0(); BAR(); DBGX(STAMP(6);) }
    allO(2);
    waitv<8>();
#pragma unroll
    for (int n = 0; n < 4; ++n) { const unsigned a = (ba ^ (32u * (n & 1))) + 512u * (n >> 1); af[0][n] = lds_tr2(a, a + 2048u); }
    unsigned k1 = (1u + rot8) & 7u, o3 = ((3u + rot8) & 7u) * 40960u, sl = 0;
    unsigned aa = ba + 4096u, aa1 = aa ^ 32u;
    OPAQUE(aa); OPAQUE(aa1);
    unsigned bs = bb;
    auto firstB = [&]() { const unsigned ab = bs + rot8 * 7168u + (rot8 & 4u) * 3072u;
#pragma unroll
      for (int q = 0; q < 7; ++q) bf[0][q] = lds_b128(ab + 1024u * q); };
    auto packed = [&](auto T) { return pack2(acc_rd<2 * T>(), acc_rd<2 * T + 1>(), 1.f); };
    auto runseg = [&](unsigned sg) {
      const unsigned sc = sbase(sg), sn = sbase(sg + 1u);
      LGKM0();
      NOPS(1);
#pragma unroll 1
      for (unsigned i = 0; i < 4; ++i) {
        sfor<2>([&](auto E) {
          constexpr int e = E;
          unsigned ab = 0, ab2 = 0, so = 0, s2 = 0; bf16x4 lo;
          sfor<28>([&](auto T) {
            constexpr int t = T, q = t / 4, n = t % 4;
            mfa<7 * n + q>(af[e][n], bf[e][q]);
            if constexpr (t == 0) { waitv<4>(); setm0(slot0 + sl); so = i >= (e ? 2u : 3u) ? sn : sc; SPIN(so); }
            if constexpr (t == 1) { so += o3; SPIN(so); ab2 = (k1 & 4u) * 3072u; SPIN(ab2); }
            if constexpr (t == 3) { ab = k1 * 7168u + ab2; SPIN(ab); }
            if constexpr (t == 5) { ab += bs; OPAQUE(ab); sl = sl == 8192u ? 0u : sl + 4096u; SPIN(sl); }
            if constexpr ((t == 2 || t == 6 || t == 10 || t == 14) && HO < 63) waitv<HO - 1>();
            if constexpr (t == 2 || t == 6 || t == 10 || t == 14) dma<0, 1024 * ((t - 2) >> 2)>(vo[(t - 2) >> 2], sO, so);
            if constexpr (t == 4 || t == 8 || t == 12 || (t >= 16 && t <= 20)) { constexpr int j = t < 16 ? t / 4 - 1 : t - 13; const unsigned a = ((j & 2) ? aa1 : aa) + 512u * (j >> 2);
              if constexpr ((j & 1) == 0) lo = lds_tr(a); else af[e ^ 1][j >> 1] = cat(lo, lds_tr(a + 2048u)); }
            if constexpr ((t >= 7 && t <= 15 && (t & 1)) || t == 21 || t == 22) { constexpr int qq = t <= 15 ? (t - 7) >> 1 : t - 16; bf[e ^ 1][qq] = lds_b128(ab + 1024u * qq); }
            if constexpr (t == 23) { s2 = sl == 8192u ? 0u : sl + 4096u; SPIN(s2); }
            if constexpr (t == 24) { aa = ba + s2; OPAQUE(aa); k1 = (k1 + 1u) & 7u; SPIN(k1); }
            if constexpr (t == 25) { aa1 = aa ^ 32u; OPAQUE(aa1); o3 += 40960u; SPIN(o3); }
            if constexpr (t == 26) { o3 = o3 == 327680u ? 0u : o3; SPIN(o3); }
            if constexpr (t == 27) LGKM0();
            GAP();
          });
        });
      }
      NOPS(7);
    };
    if (early) {
      bf16x8 pk[28];
      firstB();
      sfor<28>([&](auto T) { acc_zero<T>(); }); runseg(0);
      sfor<14>([&](auto T) { pk[T] = packed(T); OPAQUE(pk[T]); });
      sfor<28>([&](auto T) { acc_zero<T>(); }); runseg(1);
      sfor<14>([&](auto T) { pk[14 + T] = packed(T); OPAQUE(pk[14 + T]); });
#pragma unroll 1
      for (unsigned sg = 2; sg < 5u; ++sg) {
        sfor<28>([&](auto T) { acc_zero<T>(); }); runseg(sg);
        if (sg == 2u) sfor<14>([&](auto T) { acc_wr<28 + T>(__builtin_bit_cast(f32x4, packed(T))); });
        if (sg == 3u) sfor<14>([&](auto T) { acc_wr<42 + T>(__builtin_bit_cast(f32x4, packed(T))); });
      }
      lds_st(fl + w * 4u, 2u); wait_ge(fl + (l & 1u) * 4u, 2u);
      sfor<28>([&](auto T) { *(LAS bf16x8*)(size_t)(ring + l * 16u + T * 1024u) = pk[T]; });
      DBGX(STAMP(5);)
      LGKM0(); BAR();
      DBGX(STAMP(6);)
    }
    bs = bb + 2u * REG; OPAQUE(bs);
    firstB();
#pragma unroll 1
    for (unsigned sg = early ? 5u : 8u; sg < sg1; ++sg) {
      if (sg == 6u) sfor<28>([&](auto T) { mfa0a<T, 42 + T / 2>(sel[T & 1]); });
      else if (sg == 7u) sfor<28>([&](auto T) { mfa0a<T, 28 + T / 2>(sel[T & 1]); });
      else if (sg >= 8u) {
        const unsigned a = bb + (w & 1u) * REG + (sg & 1u) * 14336u;
        bf16x8 pk[14];
        sfor<14>([&](auto T) { pk[T] = lds_b128(a + T * 1024u); });
        sfor<28>([&](auto T) { mfa0<T>(sel[T & 1], pk[T / 2]); });
      }
      runseg(sg);
      const unsigned po = ((b * 10u + (w & 1u) * 5u + cchunk(sg)) * 8u + hg) * P_UNIT + l * 16u;
      sfor<14>([&](auto T) { __builtin_amdgcn_raw_buffer_store_b128(__builtin_bit_cast(u32x4, packed(T)), sP, po + T * 1024u, 0, 0); });
    }
  }
  waitv<0>();
  STAMP(7);
}

__global__ __launch_bounds__(448) void reduce_k(const void* __restrict__ Pws, __hip_bfloat16* __restrict__ out) {
  const unsigned l = threadIdx.x & 63u, t = (blockIdx.x & 1u) * 7u + (threadIdx.x >> 6), unit = blockIdx.x >> 1;
  const u32x4* p = (const u32x4*)((const char*)Pws + (size_t)unit * 8u * P_UNIT + t * 1024u + l * 16u);
  u32x4 v[8];
#pragma unroll
  for (int s = 0; s < 8; ++s) v[s] = __builtin_nontemporal_load(p + s * (P_UNIT / 16u));
  float a[8];
#pragma unroll
  for (int e = 0; e < 8; ++e) a[e] = 0.f;
#pragma unroll
  for (int s = 0; s < 8; ++s)
#pragma unroll
    for (int e = 0; e < 4; ++e) { a[2 * e] += __uint_as_float(v[s][e] << 16); a[2 * e + 1] += __uint_as_float(v[s][e] & 0xffff0000u); }
  const unsigned b = unit / 10u, j = unit % 10u;
#pragma unroll
  for (int hf = 0; hf < 2; ++hf) {
    const unsigned ti = 2u * t + hf, n = ti / 7u, q = ti % 7u;
    const u32x2 o = {cvtpk(a[4 * hf], a[4 * hf + 1]), cvtpk(a[4 * hf + 2], a[4 * hf + 3])};
    *(u32x2*)((char*)out + (size_t)(b * 112u + q * 16u + (l & 15u)) * 1280u + j * 128u + n * 32u + (l >> 4) * 8u) = o;
  }
}

extern "C" void kernel_launch(void* const* d_in, const int* in_sizes, int n_in, void* d_out, int out_size, void* d_ws, size_t ws_size,
                              hipStream_t stream) {
  (void)in_sizes; (void)n_in; (void)out_size; (void)ws_size;
  (void)hipFuncSetAttribute((const void*)fused_k, hipFuncAttributeMaxDynamicSharedMemorySize, (int)LDS_ALL);
  fused_k<<<256, 256, LDS_ALL, stream>>>(d_in[0], d_in[1], d_in[2], d_in[3], d_in[4], d_ws);
  reduce_k<<<640, 448, 0, stream>>>(d_ws, (__hip_bfloat16*)d_out);
}
```

```cpp
#include <hip/hip_runtime.h>
#include <hip/hip_bf16.h>
#include <utility>

typedef __bf16 bf16x8 __attribute__((ext_vector_type(8)));
typedef __bf16 bf16x4 __attribute__((ext_vector_type(4)));
typedef __bf16 bf16x2 __attribute__((ext_vector_type(2)));
typedef float f32x4 __attribute__((ext_vector_type(4)));
typedef float f32x2 __attribute__((ext_vector_type(2)));
typedef unsigned u32x4 __attribute__((ext_vector_type(4)));
typedef unsigned u32x2 __attribute__((ext_vector_type(2)));
typedef unsigned long long u64;
typedef __amdgpu_buffer_rsrc_t srd_t;
#define LAS __attribute__((address_space(3)))
#define DI __device__ __forceinline__
#define GAP() __builtin_amdgcn_sched_barrier(0)
#define OPAQUE(x) asm volatile("" : "+v"(x))
#define SPIN(x) asm volatile("" : "+s"(x))
#define BAR() do { asm volatile("" ::: "memory"); __builtin_amdgcn_s_barrier(); asm volatile("" ::: "memory"); } while (0)

constexpr unsigned KV_BYTES = 134217728u, X_BYTES = 3584u * 1280u, W_BYTES = 640u * 8192u;
constexpr unsigned P_UNIT = 14336u, P_BYTES = 320u * 8u * P_UNIT;
constexpr int CODE_BYTES = 52992;
constexpr unsigned REG = 40960u, LDS_ALL = 163840u, FLAGS = 3u * REG + 38912u;
constexpr unsigned SPIN_CAP = 1u << 22;
constexpr float QSCALE = 0.12751743082f;
constexpr float THR = 8.0f;
constexpr unsigned NONE = 0x80000000u;

#define STAMP(i)
#define DBGX(x)

DI unsigned lane_id() { unsigned r; asm volatile("v_mbcnt_lo_u32_b32 %0, -1, 0\n\tv_mbcnt_hi_u32_b32 %0, -1, %0" : "=v"(r)); return r; }
DI unsigned rfl(unsigned x) { return __builtin_amdgcn_readfirstlane(x); }
DI unsigned cvtpk(float lo, float hi) { f32x2 v = {lo, hi}; bf16x2 b = __builtin_convertvector(v, bf16x2); return __builtin_bit_cast(unsigned, b); }
template <int N> DI void waitv() { asm volatile("s_waitcnt vmcnt(%c0)" ::"i"(N) : "memory"); }
DI void setm0(unsigned v) { asm volatile("s_mov_b32 m0, %0" ::"s"(v) : "m0", "memory"); }
template <int NT, int OFF> DI void dma(unsigned v, srd_t srd, unsigned soff) {
  if constexpr (NT) asm volatile("buffer_load_dwordx4 %0, %1, %2 offen offset:%c3 nt lds" ::"v"(v), "s"(srd), "s"(soff), "i"(OFF) : "memory");
  else asm volatile("buffer_load_dwordx4 %0, %1, %2 offen offset:%c3 lds" ::"v"(v), "s"(srd), "s"(soff), "i"(OFF) : "memory");
}
#define LGKM0() __builtin_amdgcn_s_waitcnt(0xC07F)
DI bf16x8 lds_b128(unsigned a) { return *(const LAS bf16x8*)(size_t)a; }
DI bf16x4 lds_tr(unsigned a) { return __builtin_amdgcn_ds_read_tr16_b64_v4bf16((LAS bf16x4*)(size_t)a); }
DI bf16x8 cat(bf16x4 lo, bf16x4 hi) { return __builtin_shufflevector(lo, hi, 0, 1, 2, 3, 4, 5, 6, 7); }
DI bf16x8 lds_tr2(unsigned a0, unsigned a1) {
  bf16x4 lo = __builtin_amdgcn_ds_read_tr16_b64_v4bf16((LAS bf16x4*)(size_t)a0);
  bf16x4 hi = __builtin_amdgcn_ds_read_tr16_b64_v4bf16((LAS bf16x4*)(size_t)a1);
  return __builtin_shufflevector(lo, hi, 0, 1, 2, 3, 4, 5, 6, 7);
}
DI bf16x8 pack2(const f32x4& a, const f32x4& b, float s) {
  u32x4 w = {cvtpk(a[0] * s, a[1] * s), cvtpk(a[2] * s, a[3] * s), cvtpk(b[0] * s, b[1] * s), cvtpk(b[2] * s, b[3] * s)};
  return __builtin_bit_cast(bf16x8, w);
}
DI float col_max(float v) {
  auto r = __builtin_amdgcn_permlane16_swap(__float_as_uint(v), __float_as_uint(v), false, false);
  v = fmaxf(__uint_as_float(r[0]), __uint_as_float(r[1]));
  auto s = __builtin_amdgcn_permlane32_swap(__float_as_uint(v), __float_as_uint(v), false, false);
  return fmaxf(__uint_as_float(s[0]), __uint_as_float(s[1]));
}
DI float col_sum(float v) {
  auto r = __builtin_amdgcn_permlane16_swap(__float_as_uint(v), __float_as_uint(v), false, false);
  v = __uint_as_float(r[0]) + __uint_as_float(r[1]);
  auto s = __builtin_amdgcn_permlane32_swap(__float_as_uint(v), __float_as_uint(v), false, false);
  return __uint_as_float(s[0]) + __uint_as_float(s[1]);
}

template <class F, int... Is> DI void sfor_(F&& f, std::integer_sequence<int, Is...>) { (f(std::integral_constant<int, Is>{}), ...); }
template <int N, class F> DI void sfor(F&& f) { sfor_(f, std::make_integer_sequence<int, N>{}); }
template <int T> DI void mfa(bf16x8 a, bf16x8 b) { asm volatile("v_mfma_f32_16x16x32_bf16 a[%c2:%c3], %0, %1, a[%c2:%c3]" ::"v"(a), "v"(b), "i"(4 * T + 4), "i"(4 * T + 7)); }
template <int T> DI void mfa0(bf16x8 a, bf16x8 b) { asm volatile("v_mfma_f32_16x16x32_bf16 a[%c2:%c3], %0, %1, 0" ::"v"(a), "v"(b), "i"(4 * T + 4), "i"(4 * T + 7)); }
DI float max5(float a, float b, float c, float d, float e) { float r; asm volatile("v_max3_f32 %0, %1, %2, %3\n\tv_max3_f32 %0, %0, %4, %5" : "=&v"(r) : "v"(a), "v"(b), "v"(c), "v"(d), "v"(e)); return r; }
template <int T, int R> DI void mfa0a(bf16x8 a) { asm volatile("v_mfma_f32_16x16x32_bf16 a[%c1:%c2], %0, a[%c3:%c4], 0" ::"v"(a), "i"(4 * T + 4), "i"(4 * T + 7), "i"(4 * R + 4), "i"(4 * R + 7)); }
DI unsigned lds_ld(unsigned a) { return *(volatile LAS unsigned*)(size_t)a; }
DI void lds_st(unsigned a, unsigned v) { *(volatile LAS unsigned*)(size_t)a = v; }
DI void wait_ge(unsigned a, unsigned need) {
  unsigned sp = 0;
  while (__builtin_amdgcn_ballot_w64(lds_ld(a) < need) != 0ull && ++sp < SPIN_CAP) __builtin_amdgcn_s_sleep(1);
  asm volatile("" ::: "memory");
}
DI void mfv(f32x4& d, bf16x8 a, bf16x8 b) { asm volatile("v_mfma_f32_16x16x32_bf16 %0, %1, %2, %0" : "+v"(d) : "v"(a), "v"(b)); }
DI f32x4 mfv0(bf16x8 a, bf16x8 b, f32x4 c) { f32x4 d; asm volatile("v_mfma_f32_16x16x32_bf16 %0, %1, %2, %3" : "=&v"(d) : "v"(a), "v"(b), "v"(c)); return d; }
template <int T> DI f32x4 acc_rd() {
  float x0, x1, x2, x3;
  asm volatile("v_accvgpr_read_b32 %0, a%c4\n\tv_accvgpr_read_b32 %1, a%c5\n\tv_accvgpr_read_b32 %2, a%c6\n\tv_accvgpr_read_b32 %3, a%c7"
               : "=v"(x0), "=v"(x1), "=v"(x2), "=v"(x3) : "i"(4 * T + 4), "i"(4 * T + 5), "i"(4 * T + 6), "i"(4 * T + 7));
  return f32x4{x0, x1, x2, x3};
}
template <int T> DI void acc_wr(f32x4 v) {
  asm volatile("v_accvgpr_write_b32 a%c4, %0\n\tv_accvgpr_write_b32 a%c5, %1\n\tv_accvgpr_write_b32 a%c6, %2\n\tv_accvgpr_write_b32 a%c7, %3"
               ::"v"(v[0]), "v"(v[1]), "v"(v[2]), "v"(v[3]), "i"(4 * T + 4), "i"(4 * T + 5), "i"(4 * T + 6), "i"(4 * T + 7));
}
template <int T> DI void acc_zero() {
  asm volatile("v_accvgpr_write_b32 a%c0, 0\n\tv_accvgpr_write_b32 a%c1, 0\n\tv_accvgpr_write_b32 a%c2, 0\n\tv_accvgpr_write_b32 a%c3, 0"
               ::"i"(4 * T + 4), "i"(4 * T + 5), "i"(4 * T + 6), "i"(4 * T + 7));
}
#define NOPS(n) asm volatile("s_nop " #n)
template <int V> using IC = std::integral_constant<int, V>;

#define HQ 10
#define HO 63
#define NSAFE 2u
struct AttGap { int ex, cv, tr, pc, m0, bk; };
constexpr AttGap att_gap(bool safe, int i, int g) {
  AttGap r{-1, 0, -1, -1, -1, 0};
  if (i == 1 && g >= 8) r.tr = g - 8;
  if (i == 2 && g < 7) r.tr = g + 9;
  if (safe) {
    if (g >= 5 && g < 13) r.ex = g - 5;
    if (g == 13) r.cv = 3;
    if (g == 14) r.cv = 12;
    if (i == 0 && g == 15) { r.pc = -1; r.bk = 1; r.m0 = 0; }
    if (i == 0 && g == 16) r.pc = 0;
    if (i >= 2) { r.pc = g == 3 ? 3 * i - 5 : g == 15 ? 3 * i - 4 : g == 16 ? 3 * i - 3 : -1;
      const int nx = g == 2 ? 3 * i - 5 : g == 14 ? 3 * i - 4 : g == 15 ? 3 * i - 3 : -1; if (nx >= 0 && (nx & 3) == 0) r.m0 = nx; }
    if (i == 3 && g == 4) r.bk = 2;
    if (i == 0 && g == 3) r.bk = 3;
    return r;
  }
  if (i == 0) {
    constexpr int ex[17] = {-1, -1, 0, 1, 2, 3, -1, 4, 5, 6, 7, -1, -1, -1, -1, -1, -1};
    r.ex = ex[g];
    if (g == 0) { r.m0 = 0; r.bk = 1; }
    if (g == 1) r.pc = 0;
    if (g == 6) r.bk = 3;
    if (g == 11) r.pc = 1;
    if (g == 12) r.cv = 3;
    if (g == 13) r.cv = 12;
    if (g == 16) r.pc = 2;
  } else if (i == 1) {
    if (g < 8) r.ex = g;
    if (g == 9 || g == 11 || g == 13 || g == 15) r.cv = 1 << ((g - 9) / 2);
    if (g == 8) r.pc = 3;
    if (g == 12) r.m0 = 4;
    if (g == 14) r.pc = 4;
  } else if (i == 2) {
    if (g >= 7 && g < 15) r.ex = g - 7;
    if (g == 15) r.cv = 3;
    if (g == 16) r.cv = 12;
    if (g == 4) r.pc = 5;
    if (g == 15) r.pc = 6;
  } else if (i == 6) {
    if (g >= 8 && g < 16) r.ex = g - 8;
    if (g == 16) r.cv = 15;
    if (g == 2) r.pc = 14;
    if (g == 7) r.pc = 15;
  } else {
    constexpr int ex[17] = {0, 1, -1, 2, 3, -1, 4, 5, -1, -1, 6, 7, -1, -1, -1, -1, -1};
    r.ex = ex[g];
    if (g == 13) r.cv = 3;
    if (g == 14) r.cv = 12;
    if (i == 3) { if (g == 2) r.pc = 7; if (g == 5) r.m0 = 8; if (g == 9) r.pc = 8; if (g == 16) r.pc = 9; if (g == 12) r.bk = 2; }
    if (i == 4) { if (g == 5) r.pc = 10; if (g == 12) r.pc = 11; if (g == 15) r.m0 = 12; }
    if (i == 5) { if (g == 2) r.pc = 12; if (g == 9) r.pc = 13; }
  }
  return r;
}
constexpr int att_pieces_before(bool safe, int i, int g) { int n = 0; for (int k = 0; k < 17 * i + g; ++k) n += att_gap(safe, k / 17, k % 17).pc >= 0; return n; }
static_assert(att_pieces_before(true, 7, 0) == 16 && att_pieces_before(false, 7, 0) == 16, "pieces");

#define NHX 16
DI void map_block(unsigned g, unsigned& b, unsigned& hg, unsigned& sh) {
  constexpr unsigned HG = NHX / 4, NXH = 32 / NHX, NBX = 32 / HG;
  const unsigned xcd = g & 7u, sl = g >> 3, j = sl % HG, bi = sl / HG;
  b = (xcd / NXH) * NBX + bi; hg = (xcd % NXH) * HG + j;
  sh = ((bi * 10u) / NBX + (j * 10u) / HG) % 10u;
}

__global__ __launch_bounds__(256, 1) void fused_k(const void* __restrict__ Xp, const void* __restrict__ Kp, const void* __restrict__ Vp,
                                                  const void* __restrict__ Wq, const void* __restrict__ Wo, void* __restrict__ Pws) {
  extern __shared__ __attribute__((aligned(16))) char lds[];
  const unsigned ldsb = (unsigned)(size_t)lds;
  const unsigned l = threadIdx.x & 63u, w = rfl(threadIdx.x >> 6);
  const bool early = w < 2u;
  unsigned b, hg, rot;
  map_block(blockIdx.x, b, hg, rot);
  const unsigned h = 4u * hg + w;
  const unsigned g4 = l >> 4, q15 = l & 15u, q4 = (l >> 2) & 3u, p4 = l & 3u;
  DBGX(unsigned seg[8]; unsigned segr[8];)
  STAMP(0);
  {
    u64 pc; asm volatile("s_getpc_b64 %0" : "=s"(pc));
    srd_t sC = __builtin_amdgcn_make_buffer_rsrc((void*)(pc & ~255ull), (short)0, CODE_BYTES, 0x00020000);
    setm0(ldsb + w * REG + 32768u); NOPS(0);
    asm volatile("buffer_load_dword %0, %1, 0 offen lds" ::"v"((((blockIdx.x >> 3) * 4u + w) * 4u + (l & 3u)) * 128u), "s"(sC) : "memory");
  }
  srd_t sK = __builtin_amdgcn_make_buffer_rsrc((char*)Kp - 4096, (short)0, (int)(KV_BYTES + 4096u), 0x00020000);
  srd_t sV = __builtin_amdgcn_make_buffer_rsrc((char*)Vp - 4096, (short)0, (int)(KV_BYTES + 4096u), 0x00020000);
  srd_t sX = __builtin_amdgcn_make_buffer_rsrc((char*)Xp - 4096, (short)0, (int)(X_BYTES + 4096u), 0x00020000);
  srd_t sW = __builtin_amdgcn_make_buffer_rsrc((char*)Wq - 4096, (short)0, (int)(W_BYTES + 4096u), 0x00020000);
  const unsigned ring = ldsb + w * REG;
  const unsigned fl = ldsb + FLAGS;
  const unsigned drow = (l >> 2) & 7u, dsub = l >> 5;
  const unsigned kvo = (b * 32u + h) * 131072u;
  unsigned vk[4], vv[2];
#pragma unroll
  for (int i = 0; i < 4; ++i) vk[i] = drow * 256u + 64u * dsub + 16u * (p4 ^ (i & 2u)) + ((i & 1) ? 3200u : 4096u);
#pragma unroll
  for (int i = 0; i < 2; ++i) vv[i] = drow * 256u + 64u * dsub + 16u * (p4 ^ (2u * (drow >> 2))) + (i ? 3200u : 4096u);
  auto chunk = [&](unsigned i) { unsigned c = i + rot; return c >= 10u ? c - 10u : c; };
  auto xoff = [&](unsigned rx, int i) { return rx < 112u ? (b * 112u + rx) * 1280u + 64u * dsub + 16u * (p4 ^ (2u * (i & 1))) + 4096u - 1024u * i : NONE; };
  unsigned bx = ldsb + 1024u * (q15 >> 3) + 64u * (q15 & 7u) + 16u * (g4 ^ (2u * (q15 >> 3)));
  OPAQUE(bx);
  auto xfrag = [&](unsigned a0, unsigned a1, int q) { return lds_b128((q < 4 ? a0 : a1) + ((q >> 1) & 1) * REG + (q & 1) * 2048u); };
  unsigned vw[4];
#pragma unroll
  for (int i = 0; i < 4; ++i) vw[i] = (8u * (i >> 1) + drow) * 8192u + 128u * (i & 1) + 64u * dsub + 16u * p4 + h * 256u + 4096u - 1024u * i;

  asm volatile("" ::: "a255");
  bf16x8 qf[4][7];
  const unsigned hb = ldsb + (w & 1u) * REG + 24576u + l * 16u;
  {
    unsigned v1[4], vx[4];
#pragma unroll
    for (int i = 0; i < 4; ++i) {
      v1[i] = (8u * i + drow) * 8192u + 64u * dsub + 16u * p4 + (4u * hg + (w & 1u)) * 256u + (w >> 1) * 128u + 4096u - 1024u * i;
      vx[i] = xoff(32u * w + 8u * i + drow, i);
    }
    const unsigned mx0 = ring + 24576u;
    auto allW = [&](unsigned s) { setm0(ring + (((s >> 1) + 2u) % 3u) * 8192u + (s & 1u) * 4096u); NOPS(0);
      sfor<4>([&](auto P) { dma<0, 1024 * P>(v1[P], sW, (2u * chunk(s >> 1) + (s & 1u)) * 262144u); }); };
    auto allX = [&](unsigned i) { setm0(mx0 + i * 4096u); NOPS(0); sfor<4>([&](auto P) { dma<0, 1024 * P>(vx[P], sX, chunk(i) * 128u); }); };
    unsigned bw = ring + 1024u * g4 + 64u * q4 + 16u * p4;
    OPAQUE(bw);
    allX(0); allW(0); allW(1);
    sfor<28>([&](auto T) { acc_zero<T>(); });
    allX(1); allW(2); allW(3); allX(2); allW(4); allW(5);
    bf16x8 af[2][4], bf[2][7];
    waitv<28>(); BAR();
    STAMP(1);
#pragma unroll
    for (int d = 0; d < 4; ++d) { const unsigned a = bw + 16384u + 512u * (d >> 1) + 8u * (d & 1); af[0][d] = lds_tr2(a, a + 256u); }
#pragma unroll
    for (int q = 0; q < 7; ++q) bf[0][q] = xfrag(bx + 24576u, bx + 24576u + 2u * REG, q);
    LGKM0();
    unsigned sl = 16384u, xb = 0, cn = chunk(3);
    unsigned aw = bw + 16384u + 4096u, ax = bx + 24576u + 512u, ax2 = ax + 2u * REG;
    OPAQUE(aw); OPAQUE(ax); OPAQUE(ax2);
    srd_t sT = sW; unsigned so = cn * 524288u, hs = 262144u;
#pragma unroll 1
    for (unsigned i = 0; i < 10; ++i) {
      if (i >= 7u) {
        const unsigned j = i - 7u;
        if (early) {
#pragma unroll
          for (int k = 0; k < 4; ++k) v1[k] = j == 1u ? vv[k & 1] : vk[k];
          sT = j == 1u ? sV : sK; so = kvo + (j >> 1) * 8192u; hs = 4096u;
        } else {
#pragma unroll
          for (int k = 0; k < 4; ++k) v1[k] = vw[k];
          so = (2u * chunk(j >> 1) + (j & 1u)) * 262144u; hs = 131072u;
        }
      }
      const unsigned sln = sl == 16384u ? 0u : sl + 8192u, xbn = (xb + 4096u) & 12288u;
      sfor<2>([&](auto E) {
        constexpr int e = E;
        bf16x4 lo;
        unsigned awn = 0, axn = 0;
        sfor<28>([&](auto T) {
          constexpr int t = T, d = t / 7, q = t % 7;
          mfa<t>(af[e][d], bf[e][q]);
          if constexpr (t == 0) { if constexpr (e == 0) waitv<24>(); else waitv<18>(); }
          if constexpr (t == 1) { if constexpr (e == 1) BAR(); setm0(ring + sl + e * 4096u); }
          if constexpr (t % 3 == 2 && t / 3 < 8) {
            constexpr int j = t / 3, dd = j >> 1; const unsigned a = aw + 512u * (dd >> 1) + 8u * (dd & 1);
            if constexpr ((j & 1) == 0) lo = lds_tr(a); else af[e ^ 1][dd] = cat(lo, lds_tr(a + 256u));
          }
          if constexpr (t % 3 == 0 && t >= 3 && t <= 21) bf[e ^ 1][t / 3 - 1] = xfrag(ax, ax2, t / 3 - 1);
          if constexpr (t == 4 || t == 7 || t == 13 || t == 16) { constexpr int p = t == 4 ? 0 : t == 7 ? 1 : t == 13 ? 2 : 3; dma<0, 1024 * p>(v1[p], sT, so); }
          if constexpr (t == 19) setm0(mx0 + ((xb + 12288u) & 12288u));
          if constexpr (t == 22 || t == 25) { constexpr int p = 2 * e + (t == 25); if (i < 9u) dma<0, 1024 * p>(vx[p], sX, cn * 128u); }
          if constexpr (t == 10) { awn = e == 0 ? bw + sln : aw + 4096u; OPAQUE(awn); }
          if constexpr (t == 24) { axn = e == 0 ? bx + 24576u + xbn : ax + 512u; OPAQUE(axn); }
          if constexpr (t == 26) { ax2 = axn + 2u * REG; OPAQUE(ax2); if constexpr (e == 0) { so += hs; SPIN(so); } }
          if constexpr (t == 27) { LGKM0(); aw = awn; ax = axn; }
          GAP();
        });
      });
      sl = sln; xb = xbn; cn = cn == 9u ? 0u : cn + 1u; so = cn * 524288u;
    }
    NOPS(7);
    sfor<14>([&](auto T) { constexpr int k = T / 7, q = T % 7; qf[k][q] = pack2(acc_rd<14 * k + q>(), acc_rd<14 * k + 7 + q>(), QSCALE); OPAQUE(qf[k][q]); });
    waitv<8>();
    if (!early) {
      sfor<14>([&](auto T) { *(LAS bf16x8*)(size_t)(hb + (T / 7) * 2u * REG + (T % 7) * 1024u) = qf[T / 7][T % 7]; });
      if (w == 3u && l < 8u) lds_st(fl + l * 4u, 0u);
    }
    LGKM0(); BAR();
  }
  STAMP(2);
  if (early) {
    sfor<14>([&](auto T) { qf[2 + T / 7][T % 7] = lds_b128(hb + (T / 7) * 2u * REG + (T % 7) * 1024u); });
    LGKM0(); BAR();
    sfor<8>([&](auto P) { if constexpr ((P & 3) == 0) { setm0(ring + 24576u + P * 1024u); NOPS(0); } dma<1, 1024 * (P & 3)>(vv[P & 1], sV, kvo + 8192u + (P >> 2) * 4096u); });
  } else {
    BAR();
    unsigned vx[8];
#pragma unroll
    for (int i = 0; i < 8; ++i) vx[i] = xoff(64u * (w & 1u) + 8u * i + drow, i & 3);
    const unsigned mx0 = ldsb + (w & 1u) * 2u * REG + 32768u;
    const unsigned myf = fl + 16u + (w & 1u) * 4u, otf = fl + 16u + ((w & 1u) ^ 1u) * 4u;
    unsigned bw = ring + 2048u * g4 + 64u * q4 + 16u * p4;
    OPAQUE(bw);
    sfor<8>([&](auto P) { if constexpr ((P & 3) == 0) { setm0(ring + 24576u + P * 1024u); NOPS(0); } dma<0, 1024 * (P & 3)>(vw[P & 3], sW, (2u * chunk(1) + 1u) * 262144u + (P >> 2) * 131072u); });
    sfor<56>([&](auto T) { acc_zero<T>(); });
    bf16x8 af[2][8], bf[2][7];
#pragma unroll
    for (int d = 0; d < 8; ++d) { const unsigned a = bw + 512u * (d >> 1) + 8u * (d & 1); af[0][d] = lds_tr2(a, a + 256u); if (d < 7) bf[0][d] = xfrag(bx + 32768u, bx + 32768u + 2u * REG, d); }
    LGKM0();
    unsigned par = 0, cn = chunk(2), rn = 1;
    unsigned aw = bw + 8192u, ax = bx + 32768u + 512u, ax2 = ax + 2u * REG, rv = 1;
    OPAQUE(aw); OPAQUE(ax); OPAQUE(ax2); OPAQUE(rv);
    auto round = [&](auto TAIL) {
      constexpr bool tail = TAIL;
      unsigned so = tail ? kvo + par * 8192u : cn * 524288u, so2 = 0;
      sfor<2>([&](auto E) {
        constexpr int e = E;
        bf16x4 lo;
        unsigned awn = 0, axn = 0, pv = 0; u64 bad = 0;
        sfor<56>([&](auto T) {
          constexpr int t = T, d = t / 7, q = t % 7;
          mfa<t>(af[e][d], bf[e][q]);
          if constexpr (t == 0) { if constexpr (e == 0) waitv<16>(); else waitv<8>(); }
          if constexpr (e == 1) {
            if constexpr (t == 2) lds_st(myf, rv);
            if constexpr (t == 8) pv = lds_ld(otf);
            if constexpr (t == 14) { bad = __builtin_amdgcn_ballot_w64(pv < rn); SPIN(bad); }
            if constexpr (t == 17) { GAP(); if (__builtin_expect(bad != 0ull, 0)) wait_ge(otf, rn); asm volatile("" ::: "memory"); }
          }
          if constexpr (t % 3 == 1 && t / 3 < 16) {
            constexpr int j = t / 3, dd = j >> 1; const unsigned a = aw + 512u * (dd >> 1) + 8u * (dd & 1);
            if constexpr ((j & 1) == 0) lo = lds_tr(a); else af[e ^ 1][dd] = cat(lo, lds_tr(a + 256u));
          }
          if constexpr (t % 3 == 2 && t >= 20 && t <= 44 && t != 26 && t != 38) { constexpr int qq = (t - 20) / 3 - (t > 26) - (t > 38); bf[e ^ 1][qq] = xfrag(ax, ax2, qq); }
          constexpr int np = (e == 1 && !tail) ? 16 : 8;
          constexpr auto gap_of = [](int p) { return 3 + 3 * ((p * 16) / np); };
          sfor<np>([&](auto P) {
            constexpr int p = P;
            if constexpr ((p & 3) == 0 && t == gap_of(p) - 1) setm0(p >= 8 ? mx0 + par * 4096u + ((p - 8) >> 2) * REG : ring + par * 16384u + e * 8192u + p * 1024u);
            if constexpr (t == gap_of(p)) {
              if constexpr (HQ < 63) waitv<HQ - 1>();
              if constexpr (p >= 14) { if (w == 2u) dma<0, 1024 * (p & 3)>(vx[p & 7], sX, cn * 128u); }
              else if constexpr (p >= 8) dma<0, 1024 * (p & 3)>(vx[p & 7], sX, cn * 128u);
              else if constexpr (!tail) dma<0, 1024 * (p & 3)>(vw[p & 3], sW, p < 4 ? so : so2);
              else if constexpr (e == 0) dma<1, 1024 * (p & 3)>(vk[p & 3], sK, p < 4 ? so : so2);
              else dma<1, 1024 * (p & 3)>(vv[p & 1], sV, p < 4 ? so : so2);
            }
          });
          if constexpr (t == 5) { so2 = so + (tail ? 4096u : 131072u); SPIN(so2); }
          if constexpr (t == 47) { awn = e == 0 ? bw + (par ^ 1u) * 16384u : aw + 8192u; OPAQUE(awn); }
          if constexpr (t == 50) { axn = e == 0 ? bx + 32768u + (par ^ 1u) * 4096u : ax + 512u; OPAQUE(axn); }
          if constexpr (t == 52) { ax2 = axn + 2u * REG; OPAQUE(ax2); }
          if constexpr (t == 53 && e == 1) { rv += 1u; OPAQUE(rv); }
          if constexpr (t == 55) { LGKM0(); aw = awn; ax = axn; if constexpr (e == 0 && !tail) { so += 262144u; SPIN(so); } }
          GAP();
        });
      });
      par ^= 1u; cn = cn == 9u ? 0u : cn + 1u; rn += 1u;
    };
#pragma unroll 1
    for (unsigned i = 0; i < 8; ++i) round(std::false_type{});
#pragma unroll 1
    for (unsigned i = 8; i < 10; ++i) round(std::true_type{});
    lds_st(myf, 11u);
    NOPS(7);
    sfor<28>([&](auto T) { constexpr int k = T / 7, q = T % 7; qf[k][q] = pack2(acc_rd<14 * k + q>(), acc_rd<14 * k + 7 + q>(), QSCALE); OPAQUE(qf[k][q]); });
  }
  STAMP(3);
  {
    bf16x8 ones = __builtin_bit_cast(bf16x8, u32x4{0x3f803f80u, 0x3f803f80u, 0x3f803f80u, 0x3f803f80u});
    OPAQUE(ones);
    unsigned nsafe = NSAFE;
#pragma unroll 1
    for (;;) {
      const unsigned l = lane_id(), g4 = l >> 4, q15 = l & 15u, q4 = (l >> 2) & 3u, p4 = l & 3u;
      unsigned ak = ring + 2048u * (q15 >> 3) + 64u * (q15 & 7u) + 16u * (g4 ^ (2u * (q15 >> 3)));
      unsigned av = ring + 8192u + 2048u * (g4 >> 1) + 64u * (4u * (g4 & 1u) + q4) + 16u * ((p4 >> 1) ^ (2u * (g4 & 1u))) + 8u * (p4 & 1u), av1 = av ^ 32u;
      OPAQUE(ak); OPAQUE(av); OPAQUE(av1);
      bf16x8 kf[2][4], vf[8], pp;
      f32x4 sp0, sp1, nm[7];
#pragma unroll
      for (int d = 0; d < 8; ++d) vf[d] = bf16x8{};
#pragma unroll
      for (int q = 0; q < 7; ++q) { nm[q] = f32x4{}; OPAQUE(nm[q]); }
      pp = bf16x8{}; sp0 = f32x4{-1e30f, -1e30f, -1e30f, -1e30f}; sp1 = sp0;
      OPAQUE(pp);
      waitv<24>();
#pragma unroll
      for (int e = 0; e < 8; ++e) kf[e >> 2][e & 3] = lds_b128(ak + 4096u * (e >> 2) + 512u * (e & 3));
      ak += 16384u;
      auto check = [&](auto Q, u64 big, float mx, bool first) {
        constexpr int q = Q;
        if (__builtin_expect(big != 0ull, 0)) {
          float d = col_max(mx);
          NOPS(7);
          if (first) sfor<9>([&](auto T) { acc_zero<7 * T + q>(); });
          else {
            d = fmaxf(d, 0.f);
            const float al = __builtin_amdgcn_exp2f(-d);
            sfor<9>([&](auto T) { acc_wr<7 * T + q>(acc_rd<7 * T + q>() * al); });
          }
          nm[q] -= d; sp0 -= d; sp1 -= d;
          NOPS(1);
        }
      };
      unsigned so = kvo + 16384u, mk = ring, dl = 16384u;
      unsigned t0 = __builtin_bit_cast(unsigned, THR), t1 = 0xff800000u;
      auto step = [&](unsigned s, auto SAFE) {
        constexpr bool safe = SAFE;
        const int nrec = s < 14u ? (int)(KV_BYTES + 4096u) : 0;
        srd_t sKs = __builtin_amdgcn_make_buffer_rsrc((char*)Kp - 4096, (short)0, nrec, 0x00020000);
        srd_t sVs = __builtin_amdgcn_make_buffer_rsrc((char*)Vp - 4096, (short)0, nrec, 0x00020000);
        unsigned so2 = 0; bf16x4 lo;
        sfor<7>([&](auto I) {
          constexpr int i = I, qs = (i + 6) % 7, qp = (i + 5) % 7;
          f32x4 s0, s1; float mx; u64 big; u32x4 pn;
          sfor<17>([&](auto G) {
            constexpr int g = G;
            constexpr AttGap ag = att_gap(safe, i, g);
            if constexpr (g == 0 && i == 6) waitv<8 + att_pieces_before(safe, 6, 0)>();
            if constexpr (g == 8 && i == 1) waitv<16 + att_pieces_before(safe, 1, 8)>();
            if constexpr (g < 8) {
              if constexpr (g == 0) s0 = mfv0(kf[0][0], qf[0][i], nm[i]);
              else if constexpr (g == 1) s1 = mfv0(kf[1][0], qf[0][i], nm[i]);
              else mfv((g & 1) ? s1 : s0, kf[g & 1][g >> 1], qf[g >> 1][i]);
            } else if constexpr (g < 16) mfa<7 * (g - 8) + qp>(vf[g - 8], pp);
            else mfa<56 + qp>(ones, pp);
            GAP();
            if constexpr (i == 6 && g < 8) kf[g & 1][g >> 1] = lds_b128(ak + 4096u * (g & 1) + 512u * (g >> 1));
            if constexpr (ag.tr >= 0) { constexpr int d = ag.tr >> 1; const unsigned a = ((d & 1) ? av1 : av) + 512u * (d >> 1);
              if constexpr ((ag.tr & 1) == 0) lo = lds_tr(a); else vf[d] = cat(lo, lds_tr(a + 4096u)); }
            if constexpr (safe) {
              if constexpr (g == 0) mx = max5(sp0[0], sp0[1], sp0[2], sp0[3], sp1[0]);
              if constexpr (g == 1) mx = max5(mx, sp1[1], sp1[2], sp1[3], sp1[3]);
              if constexpr (g == 2) { big = __builtin_amdgcn_ballot_w64(mx > __uint_as_float(i == 0 ? t0 : t1)); SPIN(big); }
              if constexpr (g == 4) check(IC<qs>{}, big, mx, (i == 0 ? t0 : t1) == 0xff800000u);
              if constexpr (i == 0 && g == 16) { t0 = t1; SPIN(t0); }
            }
            if constexpr (ag.ex >= 0 && ag.ex < 4) { sp0[ag.ex] = __builtin_amdgcn_exp2f(sp0[ag.ex]); OPAQUE(sp0[ag.ex]); }
            if constexpr (ag.ex >= 4) { sp1[ag.ex - 4] = __builtin_amdgcn_exp2f(sp1[ag.ex - 4]); OPAQUE(sp1[ag.ex - 4]); }
            if constexpr (ag.cv & 1) { pn[0] = cvtpk(sp0[0], sp0[1]); OPAQUE(pn[0]); }
            if constexpr (ag.cv & 2) { pn[1] = cvtpk(sp0[2], sp0[3]); OPAQUE(pn[1]); }
            if constexpr (ag.cv & 4) { pn[2] = cvtpk(sp1[0], sp1[1]); OPAQUE(pn[2]); }
            if constexpr (ag.cv & 8) { pn[3] = cvtpk(sp1[2], sp1[3]); OPAQUE(pn[3]); }
            if constexpr (ag.pc >= 0) { constexpr int p = ag.pc;
              if constexpr (p < 8) dma<1, 1024 * (p & 3)>(vk[p & 3], sKs, p < 4 ? so : so2); else dma<1, 1024 * (p & 3)>(vv[p & 1], sVs, p < 12 ? so : so2); }
            if constexpr (ag.m0 >= 0) setm0(mk + ag.m0 * 1024u);
            if constexpr (ag.bk == 1) { so2 = so + 4096u; SPIN(so2); }
            if constexpr (ag.bk == 2) { av += dl; OPAQUE(av); av1 += dl; OPAQUE(av1); }
            if constexpr (ag.bk == 3) { ak += dl; OPAQUE(ak); }
            if constexpr (i == 6 && g == 15) LGKM0();
            GAP();
          });
          pp = __builtin_bit_cast(bf16x8, pn); sp0 = s0; sp1 = s1;
        });
        so += 8192u; mk += dl; dl = 0u - dl;
        if constexpr (safe) t1 = __builtin_bit_cast(unsigned, THR);
      };
      ak -= 16384u;
      unsigned s = 0;
#pragma unroll 1
      for (; s < nsafe; ++s) step(s, std::true_type{});
#pragma unroll 1
      for (; s < 16u; ++s) step(s, std::false_type{});
      NOPS(1);
      sfor<8>([&](auto D) { mfa<7 * D + 5>(vf[D], pp); });
      mfa<61>(ones, pp);
      GAP();
      float mx = max5(sp0[0], sp0[1], sp0[2], sp0[3], sp1[0]); mx = max5(mx, sp1[1], sp1[2], sp1[3], sp1[3]);
      check(IC<6>{}, __builtin_amdgcn_ballot_w64(mx > THR), mx, false);
#pragma unroll
      for (int k = 0; k < 4; ++k) { sp0[k] = __builtin_amdgcn_exp2f(sp0[k]); sp1[k] = __builtin_amdgcn_exp2f(sp1[k]); }
      pp = pack2(sp0, sp1, 1.f); OPAQUE(pp);
      NOPS(1);
      sfor<8>([&](auto D) { mfa<7 * D + 6>(vf[D], pp); });
      mfa<62>(ones, pp);
      NOPS(7);
      bool bad = false;
      sfor<7>([&](auto Q) { bad |= !(acc_rd<56 + Q>()[0] < 1.0995116e12f); });
      if (__builtin_expect(nsafe == 16u || __builtin_amdgcn_ballot_w64(bad) == 0ull, 1)) break;
      nsafe = 16u;
      waitv<0>();
      sfor<32>([&](auto P) { constexpr int p = P; if constexpr ((p & 3) == 0) { setm0(ring + p * 1024u); NOPS(0); }
        if constexpr ((p >> 3) & 1) dma<1, 1024 * (p & 3)>(vv[p & 1], sV, kvo + (p >> 4) * 8192u + ((p >> 2) & 1) * 4096u);
        else dma<1, 1024 * (p & 3)>(vk[p & 3], sK, kvo + (p >> 4) * 8192u + ((p >> 2) & 1) * 4096u); });
    }
  }
  STAMP(4);
  {
    const unsigned l = lane_id(), g4 = l >> 4, q15 = l & 15u, q4 = (l >> 2) & 3u, p4 = l & 3u, drow = (l >> 2) & 7u;
    srd_t sO = __builtin_amdgcn_make_buffer_rsrc((char*)Wo - 4096, (short)0, (int)(W_BYTES + 4096u), 0x00020000);
    srd_t sP = __builtin_amdgcn_make_buffer_rsrc(Pws, (short)0, (int)P_BYTES, 0x00020000);
    unsigned vo[4];
#pragma unroll
    for (int i = 0; i < 4; ++i) vo[i] = (hg * 512u + 8u * i + drow) * 1280u + (w & 1u) * 640u + 64u * (l >> 5) + 16u * (p4 ^ (2u * (drow >> 2))) + 4096u - 1024u * i;
    const unsigned slot0 = ring + 28672u;
    const unsigned rot8 = (rot * 13u) >> 4, sg0 = early ? 0u : 8u, sg1 = early ? 8u : 10u;
    auto cchunk = [&](unsigned sg) { return sg < 5u ? sg : sg < 8u ? 9u - sg : (sg & 1u); };
    auto sbase = [&](unsigned sg) { return (sg >= 5u ? 327680u : 0u) + cchunk(sg) * 128u; };
    auto allO = [&](unsigned u) { setm0(slot0 + u * 4096u); NOPS(0); sfor<4>([&](auto P) { dma<0, 1024 * P>(vo[P], sO, sbase(sg0) + ((u + rot8) & 7u) * 40960u); }); };
    waitv<0>();
    wait_ge(fl + 16u + (l & 1u) * 4u, 11u);
    allO(0); allO(1);
    sfor<7>([&](auto Q) {
      constexpr int q = Q;
      const float inv = __builtin_amdgcn_rcpf(acc_rd<56 + q>()[0]);
      sfor<4>([&](auto K) { constexpr int k = K; *(LAS bf16x8*)(size_t)(ring + l * 16u + (k * 7 + q) * 1024u) = pack2(acc_rd<14 * k + q>(), acc_rd<14 * k + 7 + q>(), inv); });
    });
    unsigned ba = slot0 + 1024u * (g4 >> 1) + 64u * (4u * (g4 & 1u) + q4) + 16u * ((p4 >> 1) ^ (2u * (g4 & 1u))) + 8u * (p4 & 1u), bb = ldsb + l * 16u;
    OPAQUE(ba); OPAQUE(bb);
    bf16x8 af[2][4], bf[2][7], sel[2];
#pragma unroll
    for (int hf = 0; hf < 2; ++hf) {
      const unsigned one = g4 == (q15 >> 2) ? (p4 & 1u ? 0x3f800000u : 0x00003f80u) : 0u, lo = p4 < 2u ? one : 0u, hi = p4 < 2u ? 0u : one;
      sel[hf] = __builtin_bit_cast(bf16x8, hf ? u32x4{0u, 0u, lo, hi} : u32x4{lo, hi, 0u, 0u}); OPAQUE(sel[hf]);
    }
    if (early) { lds_st(fl + w * 4u, 1u); wait_ge(fl + (l & 1u) * 4u, 1u); }
    else { DBGX(STAMP(5);) LGKM0(); BAR(); DBGX(STAMP(6);) }
    allO(2);
    waitv<8>();
#pragma unroll
    for (int n = 0; n < 4; ++n) { const unsigned a = (ba ^ (32u * (n & 1))) + 512u * (n >> 1); af[0][n] = lds_tr2(a, a + 2048u); }
    unsigned k1 = (1u + rot8) & 7u, o3 = ((3u + rot8) & 7u) * 40960u, sl = 0;
    unsigned aa = ba + 4096u, aa1 = aa ^ 32u;
    OPAQUE(aa); OPAQUE(aa1);
    unsigned bs = bb;
    auto firstB = [&]() { const unsigned ab = bs + rot8 * 7168u + (rot8 & 4u) * 3072u;
#pragma unroll
      for (int q = 0; q < 7; ++q) bf[0][q] = lds_b128(ab + 1024u * q); };
    auto packed = [&](auto T) { return pack2(acc_rd<2 * T>(), acc_rd<2 * T + 1>(), 1.f); };
    auto runseg = [&](unsigned sg) {
      const unsigned sc = sbase(sg), sn = sbase(sg + 1u);
      LGKM0();
      NOPS(1);
#pragma unroll 1
      for (unsigned i = 0; i < 4; ++i) {
        sfor<2>([&](auto E) {
          constexpr int e = E;
          unsigned ab = 0, ab2 = 0, so = 0, s2 = 0; bf16x4 lo;
          sfor<28>([&](auto T) {
            constexpr int t = T, q = t / 4, n = t % 4;
            mfa<7 * n + q>(af[e][n], bf[e][q]);
            if constexpr (t == 0) { waitv<4>(); setm0(slot0 + sl); so = i >= (e ? 2u : 3u) ? sn : sc; SPIN(so); }
            if constexpr (t == 1) { so += o3; SPIN(so); ab2 = (k1 & 4u) * 3072u; SPIN(ab2); }
            if constexpr (t == 3) { ab = k1 * 7168u + ab2; SPIN(ab); }
            if constexpr (t == 5) { ab += bs; OPAQUE(ab); sl = sl == 8192u ? 0u : sl + 4096u; SPIN(sl); }
            if constexpr ((t == 2 || t == 6 || t == 10 || t == 14) && HO < 63) waitv<HO - 1>();
            if constexpr (t == 2 || t == 6 || t == 10 || t == 14) dma<0, 1024 * ((t - 2) >> 2)>(vo[(t - 2) >> 2], sO, so);
            if constexpr (t == 4 || t == 8 || t == 12 || (t >= 16 && t <= 20)) { constexpr int j = t < 16 ? t / 4 - 1 : t - 13; const unsigned a = ((j & 2) ? aa1 : aa) + 512u * (j >> 2);
              if constexpr ((j & 1) == 0) lo = lds_tr(a); else af[e ^ 1][j >> 1] = cat(lo, lds_tr(a + 2048u)); }
            if constexpr ((t >= 7 && t <= 15 && (t & 1)) || t == 21 || t == 22) { constexpr int qq = t <= 15 ? (t - 7) >> 1 : t - 16; bf[e ^ 1][qq] = lds_b128(ab + 1024u * qq); }
            if constexpr (t == 23) { s2 = sl == 8192u ? 0u : sl + 4096u; SPIN(s2); }
            if constexpr (t == 24) { aa = ba + s2; OPAQUE(aa); k1 = (k1 + 1u) & 7u; SPIN(k1); }
            if constexpr (t == 25) { aa1 = aa ^ 32u; OPAQUE(aa1); o3 += 40960u; SPIN(o3); }
            if constexpr (t == 26) { o3 = o3 == 327680u ? 0u : o3; SPIN(o3); }
            if constexpr (t == 27) LGKM0();
            GAP();
          });
        });
      }
      NOPS(7);
    };
    if (early) {
      bf16x8 pk[28];
      firstB();
      sfor<28>([&](auto T) { acc_zero<T>(); }); runseg(0);
      sfor<14>([&](auto T) { pk[T] = packed(T); OPAQUE(pk[T]); });
      sfor<28>([&](auto T) { acc_zero<T>(); }); runseg(1);
      sfor<14>([&](auto T) { pk[14 + T] = packed(T); OPAQUE(pk[14 + T]); });
#pragma unroll 1
      for (unsigned sg = 2; sg < 5u; ++sg) {
        sfor<28>([&](auto T) { acc_zero<T>(); }); runseg(sg);
        if (sg == 2u) sfor<14>([&](auto T) { acc_wr<28 + T>(__builtin_bit_cast(f32x4, packed(T))); });
        if (sg == 3u) sfor<14>([&](auto T) { acc_wr<42 + T>(__builtin_bit_cast(f32x4, packed(T))); });
      }
      lds_st(fl + w * 4u, 2u); wait_ge(fl + (l & 1u) * 4u, 2u);
      sfor<28>([&](auto T) { *(LAS bf16x8*)(size_t)(ring + l * 16u + T * 1024u) = pk[T]; });
      DBGX(STAMP(5);)
      LGKM0(); BAR();
      DBGX(STAMP(6);)
    }
    bs = bb + 2u * REG; OPAQUE(bs);
    firstB();
#pragma unroll 1
    for (unsigned sg = early ? 5u : 8u; sg < sg1; ++sg) {
      if (sg == 6u) sfor<28>([&](auto T) { mfa0a<T, 42 + T / 2>(sel[T & 1]); });
      else if (sg == 7u) sfor<28>([&](auto T) { mfa0a<T, 28 + T / 2>(sel[T & 1]); });
      else if (sg >= 8u) {
        const unsigned a = bb + (w & 1u) * REG + (sg & 1u) * 14336u;
        bf16x8 pk[14];
        sfor<14>([&](auto T) { pk[T] = lds_b128(a + T * 1024u); });
        sfor<28>([&](auto T) { mfa0<T>(sel[T & 1], pk[T / 2]); });
      }
      runseg(sg);
      const unsigned po = ((b * 10u + (w & 1u) * 5u + cchunk(sg)) * 8u + hg) * P_UNIT + l * 16u;
      sfor<14>([&](auto T) { __builtin_amdgcn_raw_buffer_store_b128(__builtin_bit_cast(u32x4, packed(T)), sP, po + T * 1024u, 0, 0); });
    }
  }
  waitv<0>();
  STAMP(7);
}

__global__ __launch_bounds__(448) void reduce_k(const void* __restrict__ Pws, __hip_bfloat16* __restrict__ out) {
  const unsigned l = threadIdx.x & 63u, t = (blockIdx.x & 1u) * 7u + (threadIdx.x >> 6), unit = blockIdx.x >> 1;
  const u32x4* p = (const u32x4*)((const char*)Pws + (size_t)unit * 8u * P_UNIT + t * 1024u + l * 16u);
  u32x4 v[8];
#pragma unroll
  for (int s = 0; s < 8; ++s) v[s] = __builtin_nontemporal_load(p + s * (P_UNIT / 16u));
  float a[8];
#pragma unroll
  for (int e = 0; e < 8; ++e) a[e] = 0.f;
#pragma unroll
  for (int s = 0; s < 8; ++s)
#pragma unroll
    for (int e = 0; e < 4; ++e) { a[2 * e] += __uint_as_float(v[s][e] << 16); a[2 * e + 1] += __uint_as_float(v[s][e] & 0xffff0000u); }
  const unsigned b = unit / 10u, j = unit % 10u;
#pragma unroll
  for (int hf = 0; hf < 2; ++hf) {
    const unsigned ti = 2u * t + hf, n = ti / 7u, q = ti % 7u;
    const u32x2 o = {cvtpk(a[4 * hf], a[4 * hf + 1]), cvtpk(a[4 * hf + 2], a[4 * hf + 3])};
    *(u32x2*)((char*)out + (size_t)(b * 112u + q * 16u + (l & 15u)) * 1280u + j * 128u + n * 32u + (l >> 4) * 8u) = o;
  }
}

extern "C" void kernel_launch(void* const* d_in, const int* in_sizes, int n_in, void* d_out, int out_size, void* d_ws, size_t ws_size,
                              hipStream_t stream) {
  (void)in_sizes; (void)n_in; (void)out_size; (void)ws_size;
  (void)hipFuncSetAttribute((const void*)fused_k, hipFuncAttributeMaxDynamicSharedMemorySize, (int)LDS_ALL);
  fused_k<<<256, 256, LDS_ALL, stream>>>(d_in[0], d_in[1], d_in[2], d_in[3], d_in[4], d_ws);
  reduce_k<<<640, 448, 0, stream>>>(d_ws, (__hip_bfloat16*)d_out);
}
```

```cpp
#include <hip/hip_runtime.h>
#include <hip/hip_bf16.h>
#include <utility>

typedef __bf16 bf16x8 __attribute__((ext_vector_type(8)));
typedef __bf16 bf16x4 __attribute__((ext_vector_type(4)));
typedef __bf16 bf16x2 __attribute__((ext_vector_type(2)));
typedef float f32x4 __attribute__((ext_vector_type(4)));
typedef float f32x2 __attribute__((ext_vector_type(2)));
typedef unsigned u32x4 __attribute__((ext_vector_type(4)));
typedef unsigned u32x2 __attribute__((ext_vector_type(2)));
typedef unsigned long long u64;
typedef __amdgpu_buffer_rsrc_t srd_t;
#define LAS __attribute__((address_space(3)))
#define DI __device__ __forceinline__
#define GAP() __builtin_amdgcn_sched_barrier(0)
#define OPAQUE(x) asm volatile("" : "+v"(x))
#define SPIN(x) asm volatile("" : "+s"(x))
#define BAR() do { asm volatile("" ::: "memory"); __builtin_amdgcn_s_barrier(); asm volatile("" ::: "memory"); } while (0)

constexpr unsigned KV_BYTES = 134217728u, X_BYTES = 3584u * 1280u, W_BYTES = 640u * 8192u;
constexpr unsigned P_UNIT = 14336u, P_BYTES = 320u * 8u * P_UNIT;
constexpr int CODE_BYTES = 47616;
constexpr unsigned REG = 40960u, LDS_ALL = 163840u, FLAGS = 3u * REG + 38912u;
constexpr unsigned SPIN_CAP = 1u << 22;
constexpr float QSCALE = 0.12751743082f;
constexpr float THR = 8.0f;
constexpr unsigned NONE = 0x80000000u;

#define STAMP(i)
#define DBGX(x)

DI unsigned lane_id() { unsigned r; asm volatile("v_mbcnt_lo_u32_b32 %0, -1, 0\n\tv_mbcnt_hi_u32_b32 %0, -1, %0" : "=v"(r)); return r; }
DI unsigned rfl(unsigned x) { return __builtin_amdgcn_readfirstlane(x); }
DI unsigned cvtpk(float lo, float hi) { f32x2 v = {lo, hi}; bf16x2 b = __builtin_convertvector(v, bf16x2); return __builtin_bit_cast(unsigned, b); }
template <int N> DI void waitv() { asm volatile("s_waitcnt vmcnt(%c0)" ::"i"(N) : "memory"); }
DI void setm0(unsigned v) { asm volatile("s_mov_b32 m0, %0" ::"s"(v) : "m0", "memory"); }
template <int NT, int OFF> DI void dma(unsigned v, srd_t srd, unsigned soff) {
  if constexpr (NT) asm volatile("buffer_load_dwordx4 %0, %1, %2 offen offset:%c3 nt lds" ::"v"(v), "s"(srd), "s"(soff), "i"(OFF) : "memory");
  else asm volatile("buffer_load_dwordx4 %0, %1, %2 offen offset:%c3 lds" ::"v"(v), "s"(srd), "s"(soff), "i"(OFF) : "memory");
}
#define LGKM0() __builtin_amdgcn_s_waitcnt(0xC07F)
DI bf16x8 lds_b128(unsigned a) { return *(const LAS bf16x8*)(size_t)a; }
DI bf16x4 lds_tr(unsigned a) { return __builtin_amdgcn_ds_read_tr16_b64_v4bf16((LAS bf16x4*)(size_t)a); }
DI bf16x8 cat(bf16x4 lo, bf16x4 hi) { return __builtin_shufflevector(lo, hi, 0, 1, 2, 3, 4, 5, 6, 7); }
DI bf16x8 lds_tr2(unsigned a0, unsigned a1) {
  bf16x4 lo = __builtin_amdgcn_ds_read_tr16_b64_v4bf16((LAS bf16x4*)(size_t)a0);
  bf16x4 hi = __builtin_amdgcn_ds_read_tr16_b64_v4bf16((LAS bf16x4*)(size_t)a1);
  return __builtin_shufflevector(lo, hi, 0, 1, 2, 3, 4, 5, 6, 7);
}
DI bf16x8 pack2(const f32x4& a, const f32x4& b, float s) {
  u32x4 w = {cvtpk(a[0] * s, a[1] * s), cvtpk(a[2] * s, a[3] * s), cvtpk(b[0] * s, b[1] * s), cvtpk(b[2] * s, b[3] * s)};
  return __builtin_bit_cast(bf16x8, w);
}
DI float col_max(float v) {
  auto r = __builtin_amdgcn_permlane16_swap(__float_as_uint(v), __float_as_uint(v), false, false);
  v = fmaxf(__uint_as_float(r[0]), __uint_as_float(r[1]));
  auto s = __builtin_amdgcn_permlane32_swap(__float_as_uint(v), __float_as_uint(v), false, false);
  return fmaxf(__uint_as_float(s[0]), __uint_as_float(s[1]));
}
DI float col_sum(float v) {
  auto r = __builtin_amdgcn_permlane16_swap(__float_as_uint(v), __float_as_uint(v), false, false);
  v = __uint_as_float(r[0]) + __uint_as_float(r[1]);
  auto s = __builtin_amdgcn_permlane32_swap(__float_as_uint(v), __float_as_uint(v), false, false);
  return __uint_as_float(s[0]) + __uint_as_float(s[1]);
}

template <class F, int... Is> DI void sfor_(F&& f, std::integer_sequence<int, Is...>) { (f(std::integral_constant<int, Is>{}), ...); }
template <int N, class F> DI void sfor(F&& f) { sfor_(f, std::make_integer_sequence<int, N>{}); }
template <int T> DI void mfa(bf16x8 a, bf16x8 b) { asm volatile("v_mfma_f32_16x16x32_bf16 a[%c2:%c3], %0, %1, a[%c2:%c3]" ::"v"(a), "v"(b), "i"(4 * T + 4), "i"(4 * T + 7)); }
template <int T> DI void mfa0(bf16x8 a, bf16x8 b) { asm volatile("v_mfma_f32_16x16x32_bf16 a[%c2:%c3], %0, %1, 0" ::"v"(a), "v"(b), "i"(4 * T + 4), "i"(4 * T + 7)); }
DI float max5(float a, float b, float c, float d, float e) { float r; asm volatile("v_max3_f32 %0, %1, %2, %3\n\tv_max3_f32 %0, %0, %4, %5" : "=&v"(r) : "v"(a), "v"(b), "v"(c), "v"(d), "v"(e)); return r; }
template <int T, int R> DI void mfa0a(bf16x8 a) { asm volatile("v_mfma_f32_16x16x32_bf16 a[%c1:%c2], %0, a[%c3:%c4], 0" ::"v"(a), "i"(4 * T + 4), "i"(4 * T + 7), "i"(4 * R + 4), "i"(4 * R + 7)); }
DI unsigned lds_ld(unsigned a) { return *(volatile LAS unsigned*)(size_t)a; }
DI void lds_st(unsigned a, unsigned v) { *(volatile LAS unsigned*)(size_t)a = v; }
DI void wait_ge(unsigned a, unsigned need) {
  unsigned sp = 0;
  while (__builtin_amdgcn_ballot_w64(lds_ld(a) < need) != 0ull && ++sp < SPIN_CAP) __builtin_amdgcn_s_sleep(1);
  asm volatile("" ::: "memory");
}
DI void mfv(f32x4& d, bf16x8 a, bf16x8 b) { asm volatile("v_mfma_f32_16x16x32_bf16 %0, %1, %2, %0" : "+v"(d) : "v"(a), "v"(b)); }
DI f32x4 mfv0(bf16x8 a, bf16x8 b, f32x4 c) { f32x4 d; asm volatile("v_mfma_f32_16x16x32_bf16 %0, %1, %2, %3" : "=&v"(d) : "v"(a), "v"(b), "v"(c)); return d; }
DI f32x4 mfv00(bf16x8 a, bf16x8 b) { f32x4 d; asm volatile("v_mfma_f32_16x16x32_bf16 %0, %1, %2, 0" : "=&v"(d) : "v"(a), "v"(b)); return d; }
template <int R> DI void mfv0a(f32x4& d, bf16x8 a) { asm volatile("v_mfma_f32_16x16x32_bf16 %0, %1, a[%c2:%c3], 0" : "+v"(d) : "v"(a), "i"(4 * R + 4), "i"(4 * R + 7)); }
DI void mfv00t(f32x4& d, bf16x8 a, bf16x8 b) { asm volatile("v_mfma_f32_16x16x32_bf16 %0, %1, %2, 0" : "+v"(d) : "v"(a), "v"(b)); }
template <int T> DI float acc_rd1() { float x; asm volatile("v_accvgpr_read_b32 %0, a%c1" : "=v"(x) : "i"(4 * T + 4)); return x; }
template <int T, int OFF> DI void acc_lds_wr(unsigned a) { asm volatile("ds_write_b128 %0, a[%c1:%c2] offset:%c3" ::"v"(a), "i"(4 * T + 4), "i"(4 * T + 7), "i"(OFF) : "memory"); }
template <int T, int OFF> DI void acc_lds_rd(unsigned a) { asm volatile("ds_read_b128 a[%c1:%c2], %0 offset:%c3" ::"v"(a), "i"(4 * T + 4), "i"(4 * T + 7), "i"(OFF) : "memory"); }
template <int T> DI f32x4 acc_rd() {
  float x0, x1, x2, x3;
  asm volatile("v_accvgpr_read_b32 %0, a%c4\n\tv_accvgpr_read_b32 %1, a%c5\n\tv_accvgpr_read_b32 %2, a%c6\n\tv_accvgpr_read_b32 %3, a%c7"
               : "=v"(x0), "=v"(x1), "=v"(x2), "=v"(x3) : "i"(4 * T + 4), "i"(4 * T + 5), "i"(4 * T + 6), "i"(4 * T + 7));
  return f32x4{x0, x1, x2, x3};
}
template <int T> DI void acc_wr(f32x4 v) {
  asm volatile("v_accvgpr_write_b32 a%c4, %0\n\tv_accvgpr_write_b32 a%c5, %1\n\tv_accvgpr_write_b32 a%c6, %2\n\tv_accvgpr_write_b32 a%c7, %3"
               ::"v"(v[0]), "v"(v[1]), "v"(v[2]), "v"(v[3]), "i"(4 * T + 4), "i"(4 * T + 5), "i"(4 * T + 6), "i"(4 * T + 7));
}
template <int T> DI void acc_zero() {
  asm volatile("v_accvgpr_write_b32 a%c0, 0\n\tv_accvgpr_write_b32 a%c1, 0\n\tv_accvgpr_write_b32 a%c2, 0\n\tv_accvgpr_write_b32 a%c3, 0"
               ::"i"(4 * T + 4), "i"(4 * T + 5), "i"(4 * T + 6), "i"(4 * T + 7));
}
#define NOPS(n) asm volatile("s_nop " #n)
template <int V> using IC = std::integral_constant<int, V>;

#define HQ 10
#define HO 63
#define FORCE_SAFE false
constexpr const char* ATT_FAST[7] = {"m0123p4567CDabcdp", "4tt5tt6tt7ttCptDt", "ttt0t1tt2345p67CD", "m0p12p34B5p67pCDm", "p01p23A45p67.pCDm", "p0123p4567CDpab.c", "3kk4kk5kk6kk7pCD."};
constexpr const char* ATT_SAFE[7] = {"xyzmw01234567CDpp", "xyz.w01234567CD..", "xyzpw01234567CDpm", "xyzpw01234567CDpp", "xyzpw01234567CDpp", "xyzpw01234567CDpp", "xyzpw01234567CDpp"};
struct AttGap { int ex, cv, tr, kr, pc, m0, bk; char c; };
constexpr AttGap att_gap(bool safe, int i, int g) {
  const char* const* tb = safe ? ATT_SAFE : ATT_FAST;
  AttGap r{-1, 0, -1, -1, -1, -1, 0, tb[i][g]};
  int nt = 0, nk = 0, np = 0;
  for (int k = 0; k < 17 * i + g; ++k) { const char c = tb[k / 17][k % 17]; nt += c == 't'; nk += c == 'k'; np += c == 'p'; }
  const char c = r.c;
  if (c >= '0' && c <= '7') r.ex = c - '0';
  if (c >= 'a' && c <= 'd') r.ex = 8 + c - 'a';
  if (c == 'C') r.cv = 3;
  if (c == 'D') r.cv = 12;
  if (c == 't') r.tr = nt;
  if (c == 'k') r.kr = nk;
  if (c == 'p') r.pc = np;
  if (c == 'm') { r.m0 = np; if (np == 0) r.bk = 1; }
  if (c == 'B') r.bk = 2;
  if (c == 'A') r.bk = 3;
  if (safe) {
    if (i == 1 && g >= 8) r.tr = g - 8;
    if (i == 2 && g < 7) r.tr = g + 9;
    if (i == 6 && g < 8) r.kr = g;
    if (i == 4 && g == 14) r.m0 = 8;
    if (i == 5 && g == 15) r.m0 = 12;
    if (i == 3 && g == 4) r.bk = 2;
    if (i == 1 && g == 3) r.bk = 3;
  }
  return r;
}
constexpr int att_pieces_before(bool safe, int i, int g) { int n = 0; for (int k = 0; k < 17 * i + g; ++k) n += att_gap(safe, k / 17, k % 17).pc >= 0; return n; }
static_assert(att_pieces_before(true, 7, 0) == 16 && att_pieces_before(false, 7, 0) == 16, "pieces");

#define NHX 16
DI void map_block(unsigned g, unsigned& b, unsigned& hg, unsigned& sh) {
  constexpr unsigned HG = NHX / 4, NXH = 32 / NHX, NBX = 32 / HG;
  const unsigned xcd = g & 7u, sl = g >> 3, j = sl % HG, bi = sl / HG;
  b = (xcd / NXH) * NBX + bi; hg = (xcd % NXH) * HG + j;
  sh = ((bi * 10u) / NBX + (j * 10u) / HG) % 10u;
}

__global__ __launch_bounds__(256, 1) void fused_k(const void* __restrict__ Xp, const void* __restrict__ Kp, const void* __restrict__ Vp,
                                                  const void* __restrict__ Wq, const void* __restrict__ Wo, void* __restrict__ Pws) {
  extern __shared__ __attribute__((aligned(16))) char lds[];
  const unsigned ldsb = (unsigned)(size_t)lds;
  const unsigned l = threadIdx.x & 63u, w = rfl(threadIdx.x >> 6);
  const bool early = w < 2u;
  unsigned b, hg, rot;
  map_block(blockIdx.x, b, hg, rot);
  const unsigned h = 4u * hg + w;
  const unsigned g4 = l >> 4, q15 = l & 15u, q4 = (l >> 2) & 3u, p4 = l & 3u;
  DBGX(unsigned seg[8]; unsigned segr[8];)
  STAMP(0);
  {
    u64 pc; asm volatile("s_getpc_b64 %0" : "=s"(pc));
    srd_t sC = __builtin_amdgcn_make_buffer_rsrc((void*)(pc & ~255ull), (short)0, CODE_BYTES, 0x00020000);
    setm0(ldsb + w * REG + 32768u); NOPS(0);
    asm volatile("buffer_load_dword %0, %1, 0 offen lds" ::"v"((((blockIdx.x >> 3) * 4u + w) * 4u + (l & 3u)) * 128u), "s"(sC) : "memory");
  }
  srd_t sK = __builtin_amdgcn_make_buffer_rsrc((char*)Kp - 4096, (short)0, (int)(KV_BYTES + 4096u), 0x00020000);
  srd_t sV = __builtin_amdgcn_make_buffer_rsrc((char*)Vp - 4096, (short)0, (int)(KV_BYTES + 4096u), 0x00020000);
  srd_t sX = __builtin_amdgcn_make_buffer_rsrc((char*)Xp - 4096, (short)0, (int)(X_BYTES + 4096u), 0x00020000);
  srd_t sW = __builtin_amdgcn_make_buffer_rsrc((char*)Wq - 4096, (short)0, (int)(W_BYTES + 4096u), 0x00020000);
  const unsigned ring = ldsb + w * REG;
  const unsigned fl = ldsb + FLAGS;
  const unsigned drow = (l >> 2) & 7u, dsub = l >> 5;
  const unsigned kvo = (b * 32u + h) * 131072u;
  unsigned vk[4], vv[2];
#pragma unroll
  for (int i = 0; i < 4; ++i) vk[i] = drow * 256u + 64u * dsub + 16u * (p4 ^ (i & 2u)) + ((i & 1) ? 3200u : 4096u);
#pragma unroll
  for (int i = 0; i < 2; ++i) vv[i] = drow * 256u + 64u * dsub + 16u * (p4 ^ (2u * (drow >> 2))) + (i ? 3200u : 4096u);
  auto chunk = [&](unsigned i) { unsigned c = i + rot; return c >= 10u ? c - 10u : c; };
  auto xoff = [&](unsigned rx, int i) { return rx < 112u ? (b * 112u + rx) * 1280u + 64u * dsub + 16u * (p4 ^ (2u * (i & 1))) + 4096u - 1024u * i : NONE; };
  unsigned bx = ldsb + 1024u * (q15 >> 3) + 64u * (q15 & 7u) + 16u * (g4 ^ (2u * (q15 >> 3)));
  OPAQUE(bx);
  auto xfrag = [&](unsigned a0, unsigned a1, int q) { return lds_b128((q < 4 ? a0 : a1) + ((q >> 1) & 1) * REG + (q & 1) * 2048u); };
  unsigned vw[4];
#pragma unroll
  for (int i = 0; i < 4; ++i) vw[i] = (8u * (i >> 1) + drow) * 8192u + 128u * (i & 1) + 64u * dsub + 16u * p4 + h * 256u + 4096u - 1024u * i;

  asm volatile("" ::: "a255");
  bf16x8 qf[4][7];
  const unsigned hb = ldsb + (w & 1u) * REG + 24576u + l * 16u;
  {
    unsigned v1[4], vx[4];
#pragma unroll
    for (int i = 0; i < 4; ++i) {
      v1[i] = (8u * i + drow) * 8192u + 64u * dsub + 16u * p4 + (4u * hg + (w & 1u)) * 256u + (w >> 1) * 128u + 4096u - 1024u * i;
      vx[i] = xoff(32u * w + 8u * i + drow, i);
    }
    const unsigned mx0 = ring + 24576u;
    auto allW = [&](unsigned s) { setm0(ring + (((s >> 1) + 2u) % 3u) * 8192u + (s & 1u) * 4096u); NOPS(0);
      sfor<4>([&](auto P) { dma<0, 1024 * P>(v1[P], sW, (2u * chunk(s >> 1) + (s & 1u)) * 262144u); }); };
    auto allX = [&](unsigned i) { setm0(mx0 + i * 4096u); NOPS(0); sfor<4>([&](auto P) { dma<0, 1024 * P>(vx[P], sX, chunk(i) * 128u); }); };
    unsigned bw = ring + 1024u * g4 + 64u * q4 + 16u * p4;
    OPAQUE(bw);
    f32x4 qa[28];
    allX(0); allW(0); allW(1);
    sfor<28>([&](auto T) { qa[T] = f32x4{}; OPAQUE(qa[T]); });
    allX(1); allW(2); allW(3); allX(2); allW(4); allW(5);
    bf16x8 af[2][4], bf[2][7];
    waitv<28>(); BAR();
    STAMP(1);
#pragma unroll
    for (int d = 0; d < 4; ++d) { const unsigned a = bw + 16384u + 512u * (d >> 1) + 8u * (d & 1); af[0][d] = lds_tr2(a, a + 256u); }
#pragma unroll
    for (int q = 0; q < 7; ++q) bf[0][q] = xfrag(bx + 24576u, bx + 24576u + 2u * REG, q);
    LGKM0();
    unsigned sl = 16384u, xb = 0, cn = chunk(3);
    unsigned aw = bw + 16384u + 4096u, ax = bx + 24576u + 512u, ax2 = ax + 2u * REG;
    OPAQUE(aw); OPAQUE(ax); OPAQUE(ax2);
    srd_t sT = sW; unsigned so = cn * 524288u, hs = 262144u;
#pragma unroll 1
    for (unsigned i = 0; i < 10; ++i) {
      if (i >= 7u) {
        const unsigned j = i - 7u;
        if (early) {
#pragma unroll
          for (int k = 0; k < 4; ++k) v1[k] = j == 1u ? vv[k & 1] : vk[k];
          sT = j == 1u ? sV : sK; so = kvo + (j >> 1) * 8192u; hs = 4096u;
        } else {
#pragma unroll
          for (int k = 0; k < 4; ++k) v1[k] = vw[k];
          so = (2u * chunk(j >> 1) + (j & 1u)) * 262144u; hs = 131072u;
        }
      }
      const unsigned sln = sl == 16384u ? 0u : sl + 8192u, xbn = (xb + 4096u) & 12288u;
      sfor<2>([&](auto E) {
        constexpr int e = E;
        bf16x4 lo;
        unsigned awn = 0, axn = 0;
        sfor<28>([&](auto T) {
          constexpr int t = T, d = t / 7, q = t % 7;
          mfv(qa[t], af[e][d], bf[e][q]);
          GAP();
          if constexpr (t == 0) { if constexpr (e == 0) waitv<24>(); else waitv<18>(); }
          if constexpr (t == 1) { if constexpr (e == 1) BAR(); setm0(ring + sl + e * 4096u); }
          if constexpr (t % 3 == 2 && t / 3 < 8) {
            constexpr int j = t / 3, dd = j >> 1; const unsigned a = aw + 512u * (dd >> 1) + 8u * (dd & 1);
            if constexpr ((j & 1) == 0) lo = lds_tr(a); else af[e ^ 1][dd] = cat(lo, lds_tr(a + 256u));
          }
          if constexpr (t % 3 == 0 && t >= 3 && t <= 21) bf[e ^ 1][t / 3 - 1] = xfrag(ax, ax2, t / 3 - 1);
          if constexpr (t == 4 || t == 7 || t == 13 || t == 16) { constexpr int p = t == 4 ? 0 : t == 7 ? 1 : t == 13 ? 2 : 3; dma<0, 1024 * p>(v1[p], sT, so); }
          if constexpr (t == 19) setm0(mx0 + ((xb + 12288u) & 12288u));
          if constexpr (t == 22 || t == 25) { constexpr int p = 2 * e + (t == 25); if (i < 9u) dma<0, 1024 * p>(vx[p], sX, cn * 128u); }
          if constexpr (t == 10) { awn = e == 0 ? bw + sln : aw + 4096u; OPAQUE(awn); }
          if constexpr (t == 24) { axn = e == 0 ? bx + 24576u + xbn : ax + 512u; OPAQUE(axn); }
          if constexpr (t == 26) { ax2 = axn + 2u * REG; OPAQUE(ax2); if constexpr (e == 0) { so += hs; SPIN(so); } }
          if constexpr (t == 27) { LGKM0(); aw = awn; ax = axn; }
          GAP();
        });
      });
      sl = sln; xb = xbn; cn = cn == 9u ? 0u : cn + 1u; so = cn * 524288u;
    }
    NOPS(7);
    GAP();
    sfor<14>([&](auto T) { constexpr int k = T / 7, q = T % 7; qf[k][q] = pack2(qa[14 * k + q], qa[14 * k + 7 + q], QSCALE); OPAQUE(qf[k][q]); });
    waitv<8>();
    if (!early) {
      sfor<14>([&](auto T) { *(LAS bf16x8*)(size_t)(hb + (T / 7) * 2u * REG + (T % 7) * 1024u) = qf[T / 7][T % 7]; });
      if (w == 3u && l < 8u) lds_st(fl + l * 4u, 0u);
    }
    LGKM0(); BAR();
  }
  STAMP(2);
  if (early) {
    sfor<14>([&](auto T) { qf[2 + T / 7][T % 7] = lds_b128(hb + (T / 7) * 2u * REG + (T % 7) * 1024u); });
    LGKM0(); BAR();
    sfor<8>([&](auto P) { if constexpr ((P & 3) == 0) { setm0(ring + 24576u + P * 1024u); NOPS(0); } dma<1, 1024 * (P & 3)>(vv[P & 1], sV, kvo + 8192u + (P >> 2) * 4096u); });
  } else {
    BAR();
    unsigned vx[8];
#pragma unroll
    for (int i = 0; i < 8; ++i) vx[i] = xoff(64u * (w & 1u) + 8u * i + drow, i & 3);
    const unsigned mx0 = ldsb + (w & 1u) * 2u * REG + 32768u;
    const unsigned myf = fl + 16u + (w & 1u) * 4u, otf = fl + 16u + ((w & 1u) ^ 1u) * 4u;
    unsigned bw = ring + 2048u * g4 + 64u * q4 + 16u * p4;
    OPAQUE(bw);
    sfor<8>([&](auto P) { if constexpr ((P & 3) == 0) { setm0(ring + 24576u + P * 1024u); NOPS(0); } dma<0, 1024 * (P & 3)>(vw[P & 3], sW, (2u * chunk(1) + 1u) * 262144u + (P >> 2) * 131072u); });
    sfor<56>([&](auto T) { acc_zero<T>(); });
    bf16x8 af[2][8], bf[2][7];
#pragma unroll
    for (int d = 0; d < 8; ++d) { const unsigned a = bw + 512u * (d >> 1) + 8u * (d & 1); af[0][d] = lds_tr2(a, a + 256u); if (d < 7) bf[0][d] = xfrag(bx + 32768u, bx + 32768u + 2u * REG, d); }
    LGKM0();
    unsigned par = 0, cn = chunk(2), rn = 1;
    unsigned aw = bw + 8192u, ax = bx + 32768u + 512u, ax2 = ax + 2u * REG, rv = 1;
    OPAQUE(aw); OPAQUE(ax); OPAQUE(ax2); OPAQUE(rv);
    auto round = [&](auto TAIL) {
      constexpr bool tail = TAIL;
      unsigned so = tail ? kvo + par * 8192u : cn * 524288u, so2 = 0;
      sfor<2>([&](auto E) {
        constexpr int e = E;
        bf16x4 lo;
        unsigned awn = 0, axn = 0, pv = 0; u64 bad = 0;
        sfor<56>([&](auto T) {
          constexpr int t = T, d = t / 7, q = t % 7;
          mfa<t>(af[e][d], bf[e][q]);
          if constexpr (t == 0) { if constexpr (e == 0) waitv<16>(); else waitv<8>(); }
          if constexpr (e == 1) {
            if constexpr (t == 2) lds_st(myf, rv);
            if constexpr (t == 8) pv = lds_ld(otf);
            if constexpr (t == 14) { bad = __builtin_amdgcn_ballot_w64(pv < rn); SPIN(bad); }
            if constexpr (t == 17) { GAP(); if (__builtin_expect(bad != 0ull, 0)) wait_ge(otf, rn); asm volatile("" ::: "memory"); }
          }
          if constexpr (t % 3 == 1 && t / 3 < 16) {
            constexpr int j = t / 3, dd = j >> 1; const unsigned a = aw + 512u * (dd >> 1) + 8u * (dd & 1);
            if constexpr ((j & 1) == 0) lo = lds_tr(a); else af[e ^ 1][dd] = cat(lo, lds_tr(a + 256u));
          }
          if constexpr (t % 3 == 2 && t >= 20 && t <= 44 && t != 26 && t != 38) { constexpr int qq = (t - 20) / 3 - (t > 26) - (t > 38); bf[e ^ 1][qq] = xfrag(ax, ax2, qq); }
          constexpr int np = (e == 1 && !tail) ? 16 : 8;
          constexpr auto gap_of = [](int p) { return 3 + 3 * ((p * 16) / np); };
          sfor<np>([&](auto P) {
            constexpr int p = P;
            if constexpr ((p & 3) == 0 && t == gap_of(p) - 1) setm0(p >= 8 ? mx0 + par * 4096u + ((p - 8) >> 2) * REG : ring + par * 16384u + e * 8192u + p * 1024u);
            if constexpr (t == gap_of(p)) {
              if constexpr (HQ < 63) waitv<HQ - 1>();
              if constexpr (p >= 14) { if (w == 2u) dma<0, 1024 * (p & 3)>(vx[p & 7], sX, cn * 128u); }
              else if constexpr (p >= 8) dma<0, 1024 * (p & 3)>(vx[p & 7], sX, cn * 128u);
              else if constexpr (!tail) dma<0, 1024 * (p & 3)>(vw[p & 3], sW, p < 4 ? so : so2);
              else if constexpr (e == 0) dma<1, 1024 * (p & 3)>(vk[p & 3], sK, p < 4 ? so : so2);
              else dma<1, 1024 * (p & 3)>(vv[p & 1], sV, p < 4 ? so : so2);
            }
          });
          if constexpr (t == 5) { so2 = so + (tail ? 4096u : 131072u); SPIN(so2); }
          if constexpr (t == 47) { awn = e == 0 ? bw + (par ^ 1u) * 16384u : aw + 8192u; OPAQUE(awn); }
          if constexpr (t == 50) { axn = e == 0 ? bx + 32768u + (par ^ 1u) * 4096u : ax + 512u; OPAQUE(axn); }
          if constexpr (t == 52) { ax2 = axn + 2u * REG; OPAQUE(ax2); }
          if constexpr (t == 53 && e == 1) { rv += 1u; OPAQUE(rv); }
          if constexpr (t == 55) { LGKM0(); aw = awn; ax = axn; if constexpr (e == 0 && !tail) { so += 262144u; SPIN(so); } }
          GAP();
        });
      });
      par ^= 1u; cn = cn == 9u ? 0u : cn + 1u; rn += 1u;
    };
#pragma unroll 1
    for (unsigned i = 0; i < 8; ++i) round(std::false_type{});
#pragma unroll 1
    for (unsigned i = 8; i < 10; ++i) round(std::true_type{});
    lds_st(myf, 11u);
    NOPS(7);
    sfor<28>([&](auto T) { constexpr int k = T / 7, q = T % 7; qf[k][q] = pack2(acc_rd<14 * k + q>(), acc_rd<14 * k + 7 + q>(), QSCALE); OPAQUE(qf[k][q]); });
  }
  STAMP(3);
  float lsum[7];
  {
    bf16x8 ones = __builtin_bit_cast(bf16x8, u32x4{0x3f803f80u, 0x3f803f80u, 0x3f803f80u, 0x3f803f80u});
    OPAQUE(ones);
    unsigned nsafe = FORCE_SAFE ? 16u : 1u;
#pragma unroll 1
    for (;;) {
      const unsigned l = lane_id(), g4 = l >> 4, q15 = l & 15u, q4 = (l >> 2) & 3u, p4 = l & 3u;
      unsigned ak = ring + 2048u * (q15 >> 3) + 64u * (q15 & 7u) + 16u * (g4 ^ (2u * (q15 >> 3)));
      unsigned av = ring + 8192u + 2048u * (g4 >> 1) + 64u * (4u * (g4 & 1u) + q4) + 16u * ((p4 >> 1) ^ (2u * (g4 & 1u))) + 8u * (p4 & 1u), av1 = av ^ 32u;
      OPAQUE(ak); OPAQUE(av); OPAQUE(av1);
      bf16x8 kf[2][4], vf[8], pp;
      f32x4 sp0, sp1, nm[7];
      waitv<24>();
#pragma unroll
      for (int e = 0; e < 8; ++e) kf[e >> 2][e & 3] = lds_b128(ak + 4096u * (e >> 2) + 512u * (e & 3));
      ak += 16384u;
      auto check = [&](auto Q, u64 big, float mx) {
        constexpr int q = Q;
        if (__builtin_expect(big != 0ull, 0)) {
          const float d = fmaxf(col_max(mx), 0.f), al = __builtin_amdgcn_exp2f(-d);
          NOPS(7);
          sfor<9>([&](auto T) { acc_wr<7 * T + q>(acc_rd<7 * T + q>() * al); });
          nm[q] -= d; sp0 -= d; sp1 -= d;
          NOPS(1);
        }
      };
      auto setref = [&](auto Q, float mx) { constexpr int q = Q; const float d = col_max(mx); nm[q] = f32x4{-d, -d, -d, -d}; OPAQUE(nm[q]); sp0 -= d; sp1 -= d; };
      unsigned so = kvo + 16384u, mk = ring, dl = 16384u;
      auto step = [&](unsigned s, auto MODE) {
        constexpr bool safe = MODE != 0, first = MODE == 2;
        const int nrec = s < 14u ? (int)(KV_BYTES + 4096u) : 0;
        srd_t sKs = __builtin_amdgcn_make_buffer_rsrc((char*)Kp - 4096, (short)0, nrec, 0x00020000);
        srd_t sVs = __builtin_amdgcn_make_buffer_rsrc((char*)Vp - 4096, (short)0, nrec, 0x00020000);
        unsigned so2 = 0; bf16x4 lo;
        sfor<7>([&](auto I) {
          constexpr int i = I, qs = (i + 6) % 7, qp = (i + 5) % 7;
          f32x4 s0, s1; float mx; u64 big; u32x4 pn;
          sfor<17>([&](auto G) {
            constexpr int g = G;
            constexpr AttGap ag = att_gap(safe, i, g);
            constexpr bool rev = !safe && i == 1;
            constexpr int m = rev ? (g < 9 ? g + 8 : g - 9) : g;
            if constexpr (g == 0 && i == 6) waitv<8 + att_pieces_before(safe, 6, 0)>();
            if constexpr (g == (safe ? 8 : 0) && i == 1) waitv<16 + att_pieces_before(safe, 1, g)>();
            if constexpr (m < 2 && first) (m ? s1 : s0) = mfv00(kf[m][0], qf[0][i]);
            else if constexpr (m < 2) (m ? s1 : s0) = mfv0(kf[m][0], qf[0][i], nm[i]);
            else if constexpr (m < 8) mfv((m & 1) ? s1 : s0, kf[m & 1][m >> 1], qf[m >> 1][i]);
            else if constexpr (first && i < 2) acc_zero<7 * (m - 8) + qp>();
            else if constexpr (first) mfa0<7 * (m - 8) + qp>(m < 16 ? vf[(m - 8) & 7] : ones, pp);
            else mfa<7 * (m - 8) + qp>(m < 16 ? vf[(m - 8) & 7] : ones, pp);
            GAP();
            if constexpr (ag.kr >= 0) kf[ag.kr & 1][ag.kr >> 1] = lds_b128(ak + 4096u * (ag.kr & 1) + 512u * (ag.kr >> 1));
            if constexpr (ag.tr >= 0) { constexpr int d = ag.tr >> 1; const unsigned a = ((d & 1) ? av1 : av) + 512u * (d >> 1);
              if constexpr ((ag.tr & 1) == 0) lo = lds_tr(a); else vf[d] = cat(lo, lds_tr(a + 4096u)); }
            if constexpr (!(first && i == 0)) {
              if constexpr (ag.c == 'x') mx = max5(sp0[0], sp0[1], sp0[2], sp0[3], sp1[0]);
              if constexpr (ag.c == 'y') mx = max5(mx, sp1[1], sp1[2], sp1[3], sp1[3]);
              if constexpr (ag.c == 'z' && first) setref(IC<qs>{}, mx);
              if constexpr (ag.c == 'z' && !first) { big = __builtin_amdgcn_ballot_w64(mx > THR); SPIN(big); }
              if constexpr (ag.c == 'w' && !first) check(IC<qs>{}, big, mx);
              if constexpr (ag.ex >= 0 && ag.ex < 4) { sp0[ag.ex] = __builtin_amdgcn_exp2f(sp0[ag.ex]); OPAQUE(sp0[ag.ex]); }
              if constexpr (ag.ex >= 4 && ag.ex < 8) { sp1[ag.ex - 4] = __builtin_amdgcn_exp2f(sp1[ag.ex - 4]); OPAQUE(sp1[ag.ex - 4]); }
              if constexpr (ag.ex >= 8) { s0[ag.ex - 8] = __builtin_amdgcn_exp2f(s0[ag.ex - 8]); OPAQUE(s0[ag.ex - 8]); }
              if constexpr (ag.cv & 1) { pn[0] = cvtpk(sp0[0], sp0[1]); OPAQUE(pn[0]); }
              if constexpr (ag.cv & 2) { pn[1] = cvtpk(sp0[2], sp0[3]); OPAQUE(pn[1]); }
              if constexpr (ag.cv & 4) { pn[2] = cvtpk(sp1[0], sp1[1]); OPAQUE(pn[2]); }
              if constexpr (ag.cv & 8) { pn[3] = cvtpk(sp1[2], sp1[3]); OPAQUE(pn[3]); }
            }
            if constexpr (ag.pc >= 0) { constexpr int p = ag.pc;
              if constexpr (p < 8) dma<1, 1024 * (p & 3)>(vk[p & 3], sKs, p < 4 ? so : so2); else dma<1, 1024 * (p & 3)>(vv[p & 1], sVs, p < 12 ? so : so2); }
            if constexpr (ag.m0 >= 0) setm0(mk + ag.m0 * 1024u);
            if constexpr (ag.bk == 1) { so2 = so + 4096u; SPIN(so2); }
            if constexpr (ag.bk == 2) { av += dl; OPAQUE(av); av1 += dl; OPAQUE(av1); }
            if constexpr (ag.bk == 3) { ak += dl; OPAQUE(ak); }
            if constexpr (i == 6 && g == 16) LGKM0();
            GAP();
          });
          if constexpr (!(first && i == 0)) pp = __builtin_bit_cast(bf16x8, pn);
          sp0 = s0; sp1 = s1;
        });
        so += 8192u; mk += dl; dl = 0u - dl;
      };
      ak -= 16384u;
      step(0u, IC<2>{});
      { float mx = max5(sp0[0], sp0[1], sp0[2], sp0[3], sp1[0]); mx = max5(mx, sp1[1], sp1[2], sp1[3], sp1[3]); setref(IC<6>{}, mx); GAP(); }
      unsigned s = 1;
#pragma unroll 1
      for (; s < nsafe; ++s) step(s, IC<1>{});
#pragma unroll 1
      for (; s < 16u; ++s) step(s, IC<0>{});
      { float mx = max5(sp0[0], sp0[1], sp0[2], sp0[3], sp1[0]); mx = max5(mx, sp1[1], sp1[2], sp1[3], sp1[3]);
        check(IC<6>{}, __builtin_amdgcn_ballot_w64(mx > THR), mx); }
      NOPS(1);
      sfor<9>([&](auto D) {
        mfa<7 * D + 5>(D < 8 ? vf[D & 7] : ones, pp);
        GAP();
        constexpr int d = D;
        if constexpr (d < 4) { sp0[d] = __builtin_amdgcn_exp2f(sp0[d]); OPAQUE(sp0[d]); } else if constexpr (d < 8) { sp1[d - 4] = __builtin_amdgcn_exp2f(sp1[d - 4]); OPAQUE(sp1[d - 4]); }
        GAP();
      });
      pp = pack2(sp0, sp1, 1.f); OPAQUE(pp);
      NOPS(1);
      mfa<62>(ones, pp);
      sfor<8>([&](auto D) { mfa<7 * D + 6>(vf[D], pp); });
      bool bad = false;
      sfor<7>([&](auto Q) { lsum[Q] = acc_rd1<56 + Q>(); bad |= !(lsum[Q] < 1.0995116e12f); });
      if (__builtin_expect(nsafe == 16u || __builtin_amdgcn_ballot_w64(bad) == 0ull, 1)) break;
      nsafe = 16u;
      waitv<0>();
      sfor<32>([&](auto P) { constexpr int p = P; if constexpr ((p & 3) == 0) { setm0(ring + p * 1024u); NOPS(0); }
        if constexpr ((p >> 3) & 1) dma<1, 1024 * (p & 3)>(vv[p & 1], sV, kvo + (p >> 4) * 8192u + ((p >> 2) & 1) * 4096u);
        else dma<1, 1024 * (p & 3)>(vk[p & 3], sK, kvo + (p >> 4) * 8192u + ((p >> 2) & 1) * 4096u); });
    }
  }
  STAMP(4);
  {
    const unsigned l = lane_id(), g4 = l >> 4, q15 = l & 15u, q4 = (l >> 2) & 3u, p4 = l & 3u, drow = (l >> 2) & 7u;
    srd_t sO = __builtin_amdgcn_make_buffer_rsrc((char*)Wo - 4096, (short)0, (int)(W_BYTES + 4096u), 0x00020000);
    srd_t sP = __builtin_amdgcn_make_buffer_rsrc(Pws, (short)0, (int)P_BYTES, 0x00020000);
    unsigned vo[4];
#pragma unroll
    for (int i = 0; i < 4; ++i) vo[i] = (hg * 512u + 8u * i + drow) * 1280u + (w & 1u) * 640u + 64u * (l >> 5) + 16u * (p4 ^ (2u * (drow >> 2))) + 4096u - 1024u * i;
    const unsigned slot0 = ring + 28672u;
    const unsigned rot8 = (rot * 13u) >> 4, sg0 = early ? 0u : 8u;
    auto cchunk = [&](unsigned sg) { return (unsigned)(0x1023443210ull >> (4u * sg)) & 7u; };
    auto sbase = [&](unsigned sg) { return ((0x7e0u >> sg) & 1u) * 327680u + cchunk(sg) * 128u; };
    auto allO = [&](unsigned u) { setm0(slot0 + u * 4096u); NOPS(0); sfor<4>([&](auto P) { dma<0, 1024 * P>(vo[P], sO, sbase(sg0) + ((u + rot8) & 7u) * 40960u); }); };
    waitv<0>();
    wait_ge(fl + 16u + (l & 1u) * 4u, 11u);
    allO(0); allO(1);
    sfor<7>([&](auto Q) {
      constexpr int q = Q;
      const float inv = __builtin_amdgcn_rcpf(lsum[q]);
      sfor<4>([&](auto K) { constexpr int k = K; *(LAS bf16x8*)(size_t)(ring + l * 16u + (k * 7 + q) * 1024u) = pack2(acc_rd<14 * k + q>(), acc_rd<14 * k + 7 + q>(), inv); });
    });
    unsigned ba = slot0 + 1024u * (g4 >> 1) + 64u * (4u * (g4 & 1u) + q4) + 16u * ((p4 >> 1) ^ (2u * (g4 & 1u))) + 8u * (p4 & 1u), bb = ldsb + l * 16u;
    OPAQUE(ba); OPAQUE(bb);
    bf16x8 af[2][4], bf[2][7];
    if (early) { lds_st(fl + w * 4u, 1u); wait_ge(fl + (l & 1u) * 4u, 1u); }
    else { DBGX(STAMP(5);) LGKM0(); BAR(); DBGX(STAMP(6);) }
    allO(2);
    waitv<8>();
#pragma unroll
    for (int n = 0; n < 4; ++n) { const unsigned a = (ba ^ (32u * (n & 1))) + 512u * (n >> 1); af[0][n] = lds_tr2(a, a + 2048u); }
    unsigned k1 = (1u + rot8) & 7u, o3 = ((3u + rot8) & 7u) * 40960u, sl = 0;
    unsigned aa = ba + 4096u, aa1 = aa ^ 32u;
    OPAQUE(aa); OPAQUE(aa1);
    unsigned bs = bb;
    auto firstB = [&]() { const unsigned ab = bs + rot8 * 7168u + (rot8 & 4u) * 3072u;
#pragma unroll
      for (int q = 0; q < 7; ++q) bf[0][q] = lds_b128(ab + 1024u * q); };
    f32x4 oa[28];
    sfor<28>([&](auto T) { f32x4 u; asm volatile("" : "=v"(u)); oa[T] = u; });
    auto unpack = [&](auto R) {
      const unsigned ll = lane_id(), one = (ll >> 4) == ((ll >> 2) & 3u) ? (ll & 1u ? 0x3f800000u : 0x00003f80u) : 0u, lo = (ll & 2u) ? 0u : one, hi = (ll & 2u) ? one : 0u;
      bf16x8 sel[2] = {__builtin_bit_cast(bf16x8, u32x4{lo, hi, 0u, 0u}), __builtin_bit_cast(bf16x8, u32x4{0u, 0u, lo, hi})};
      OPAQUE(sel[0]); OPAQUE(sel[1]); NOPS(1);
      sfor<28>([&](auto T) { mfv0a<R + T / 2>(oa[T], sel[T & 1]); });
    };
    auto packed = [&](auto T) { return pack2(oa[2 * T], oa[2 * T + 1], 1.f); };
    auto pairU = [&](unsigned i, unsigned sc, unsigned sn, auto ZERO) {
      {
        sfor<2>([&](auto E) {
          constexpr int e = E;
          unsigned ab = 0, ab2 = 0, so = 0, s2 = 0; bf16x4 lo;
          sfor<28>([&](auto T) {
            constexpr int t = T, q = t / 4, n = t % 4;
            if constexpr (ZERO && e == 0) mfv00t(oa[7 * n + q], af[e][n], bf[e][q]); else mfv(oa[7 * n + q], af[e][n], bf[e][q]);
            GAP();
            if constexpr (t == 0) { waitv<4>(); setm0(slot0 + sl); so = ((i + (e ? 2u : 1u)) >> 2) * sn; SPIN(so); }
            if constexpr (t == 1) { so += o3 + sc; SPIN(so); }
            if constexpr (t == 2) { ab2 = (k1 & 4u) * 3072u; SPIN(ab2); }
            if constexpr (t == 3) { ab = k1 * 7168u + ab2; SPIN(ab); }
            if constexpr (t == 5) { ab += bs; OPAQUE(ab); sl = sl == 8192u ? 0u : sl + 4096u; SPIN(sl); }
            if constexpr ((t == 2 || t == 6 || t == 10 || t == 14) && HO < 63) waitv<HO - 1>();
            if constexpr (t == 2 || t == 6 || t == 10 || t == 14) dma<0, 1024 * ((t - 2) >> 2)>(vo[(t - 2) >> 2], sO, so);
            if constexpr (t == 4 || t == 8 || t == 12 || (t >= 16 && t <= 20)) { constexpr int j = t < 16 ? t / 4 - 1 : t - 13; const unsigned a = ((j & 2) ? aa1 : aa) + 512u * (j >> 2);
              if constexpr ((j & 1) == 0) lo = lds_tr(a); else af[e ^ 1][j >> 1] = cat(lo, lds_tr(a + 2048u)); }
            if constexpr ((t >= 7 && t <= 15 && (t & 1)) || t == 21 || t == 22) { constexpr int qq = t <= 15 ? (t - 7) >> 1 : t - 16; bf[e ^ 1][qq] = lds_b128(ab + 1024u * qq); }
            if constexpr (t == 23) { s2 = sl == 8192u ? 0u : sl + 4096u; SPIN(s2); }
            if constexpr (t == 24) { aa = ba + s2; OPAQUE(aa); k1 = (k1 + 1u) & 7u; SPIN(k1); }
            if constexpr (t == 25) { aa1 = aa ^ 32u; OPAQUE(aa1); o3 += 40960u; SPIN(o3); }
            if constexpr (t == 26) { o3 = o3 == 327680u ? 0u : o3; SPIN(o3); }
            if constexpr (t == 27) LGKM0();
            GAP();
          });
        });
      }
    };
    if (early) {
      firstB(); LGKM0();
#pragma unroll 1
      for (unsigned sg = 0; sg < 5u; ++sg) {
        const unsigned sc = sbase(sg), sn = sbase(sg + 1u) - sc;
        pairU(0u, sc, sn, std::true_type{});
#pragma unroll 1
        for (unsigned i = 1; i < 4; ++i) pairU(i, sc, sn, std::false_type{});
        NOPS(7); GAP();
        sfor<4>([&](auto C) { if (sg == C) sfor<14>([&](auto T) { acc_wr<14 * C + T>(__builtin_bit_cast(f32x4, packed(T))); }); });
      }
      lds_st(fl + w * 4u, 2u); wait_ge(fl + (lane_id() & 1u) * 4u, 2u);
      const unsigned a = bb + w * REG;
      sfor<28>([&](auto T) { acc_lds_wr<T, T * 1024>(a); });
      DBGX(STAMP(5);)
      asm volatile("s_waitcnt lgkmcnt(0)" ::: "memory"); BAR();
      DBGX(STAMP(6);)
    } else {
      const unsigned a = bb + (w & 1u) * REG;
      sfor<28>([&](auto T) { acc_lds_rd<28 + T, T * 1024>(a); });
      asm volatile("s_waitcnt lgkmcnt(0)" ::: "memory");
      unpack(IC<28>{});
    }
    bs = bb + 2u * REG; OPAQUE(bs);
    firstB();
    LGKM0();
    auto lastseg = [&](unsigned sg) {
      const unsigned sc = sbase(sg), sn = sbase(sg + 1u) - sc;
#pragma unroll 1
      for (unsigned i = 0; i < 4; ++i) pairU(i, sc, sn, std::false_type{});
      NOPS(7); GAP();
      const unsigned po = ((b * 10u + (w & 1u) * 5u + cchunk(sg)) * 8u + hg) * P_UNIT + lane_id() * 16u;
      sfor<14>([&](auto T) { __builtin_amdgcn_raw_buffer_store_b128(__builtin_bit_cast(u32x4, packed(T)), sP, po + T * 1024u, 0, 0); });
      GAP();
    };
    lastseg(early ? 5u : 8u);
    unpack(IC<42>{});
    lastseg(early ? 6u : 9u);
    if (early) {
      unpack(IC<28>{});
      lastseg(7u);
    }
  }
  waitv<0>();
  STAMP(7);
}

__global__ __launch_bounds__(448) void reduce_k(const void* __restrict__ Pws, __hip_bfloat16* __restrict__ out) {
  const unsigned l = threadIdx.x & 63u, t = (blockIdx.x & 1u) * 7u + (threadIdx.x >> 6), unit = blockIdx.x >> 1;
  const u32x4* p = (const u32x4*)((const char*)Pws + (size_t)unit * 8u * P_UNIT + t * 1024u + l * 16u);
  u32x4 v[8];
#pragma unroll
  for (int s = 0; s < 8; ++s) v[s] = __builtin_nontemporal_load(p + s * (P_UNIT / 16u));
  float a[8];
#pragma unroll
  for (int e = 0; e < 8; ++e) a[e] = 0.f;
#pragma unroll
  for (int s = 0; s < 8; ++s)
#pragma unroll
    for (int e = 0; e < 4; ++e) { a[2 * e] += __uint_as_float(v[s][e] << 16); a[2 * e + 1] += __uint_as_float(v[s][e] & 0xffff0000u); }
  const unsigned b = unit / 10u, j = unit % 10u;
#pragma unroll
  for (int hf = 0; hf < 2; ++hf) {
    const unsigned ti = 2u * t + hf, n = ti / 7u, q = ti % 7u;
    const u32x2 o = {cvtpk(a[4 * hf], a[4 * hf + 1]), cvtpk(a[4 * hf + 2], a[4 * hf + 3])};
    *(u32x2*)((char*)out + (size_t)(b * 112u + q * 16u + (l & 15u)) * 1280u + j * 128u + n * 32u + (l >> 4) * 8u) = o;
  }
}

extern "C" void kernel_launch(void* const* d_in, const int* in_sizes, int n_in, void* d_out, int out_size, void* d_ws, size_t ws_size,
                              hipStream_t stream) {
  (void)in_sizes; (void)n_in; (void)out_size; (void)ws_size;
  (void)hipFuncSetAttribute((const void*)fused_k, hipFuncAttributeMaxDynamicSharedMemorySize, (int)LDS_ALL);
  fused_k<<<256, 256, LDS_ALL, stream>>>(d_in[0], d_in[1], d_in[2], d_in[3], d_in[4], d_ws);
  reduce_k<<<640, 448, 0, stream>>>(d_ws, (__hip_bfloat16*)d_out);
}
```

```cpp
#include <hip/hip_runtime.h>
#include <hip/hip_bf16.h>
#include <utility>

typedef __bf16 bf16x8 __attribute__((ext_vector_type(8)));
typedef __bf16 bf16x4 __attribute__((ext_vector_type(4)));
typedef __bf16 bf16x2 __attribute__((ext_vector_type(2)));
typedef float f32x4 __attribute__((ext_vector_type(4)));
typedef float f32x2 __attribute__((ext_vector_type(2)));
typedef unsigned u32x4 __attribute__((ext_vector_type(4)));
typedef unsigned u32x2 __attribute__((ext_vector_type(2)));
typedef unsigned long long u64;
typedef __amdgpu_buffer_rsrc_t srd_t;
#define LAS __attribute__((address_space(3)))
#define DI __device__ __forceinline__
#define GAP() __builtin_amdgcn_sched_barrier(0)
#define OPAQUE(x) asm volatile("" : "+v"(x))
#define SPIN(x) asm volatile("" : "+s"(x))
#define BAR() do { asm volatile("" ::: "memory"); __builtin_amdgcn_s_barrier(); asm volatile("" ::: "memory"); } while (0)

constexpr unsigned KV_BYTES = 134217728u, X_BYTES = 3584u * 1280u, W_BYTES = 640u * 8192u;
constexpr unsigned P_UNIT = 14336u, P_BYTES = 320u * 8u * P_UNIT;
constexpr unsigned F_BYTES = 32u * 512u;
constexpr int CODE_BYTES = 52608;
constexpr unsigned REG = 40960u, LDS_ALL = 163840u, FLAGS = 3u * REG + 38912u;
constexpr unsigned SPIN_CAP = 1u << 22;
constexpr float QSCALE = 0.12751743082f;
constexpr float THR = 8.0f;
constexpr unsigned NONE = 0x80000000u;

#define STAMP(i)
#define DBGX(x)

DI unsigned lane_id() { unsigned r; asm volatile("v_mbcnt_lo_u32_b32 %0, -1, 0\n\tv_mbcnt_hi_u32_b32 %0, -1, %0" : "=v"(r)); return r; }
DI unsigned rfl(unsigned x) { return __builtin_amdgcn_readfirstlane(x); }
DI unsigned cvtpk(float lo, float hi) { f32x2 v = {lo, hi}; bf16x2 b = __builtin_convertvector(v, bf16x2); return __builtin_bit_cast(unsigned, b); }
template <int N> DI void waitv() { asm volatile("s_waitcnt vmcnt(%c0)" ::"i"(N) : "memory"); }
DI void setm0(unsigned v) { asm volatile("s_mov_b32 m0, %0" ::"s"(v) : "m0", "memory"); }
template <int NT, int OFF> DI void dma(unsigned v, srd_t srd, unsigned soff) {
  if constexpr (NT) asm volatile("buffer_load_dwordx4 %0, %1, %2 offen offset:%c3 nt lds" ::"v"(v), "s"(srd), "s"(soff), "i"(OFF) : "memory");
  else asm volatile("buffer_load_dwordx4 %0, %1, %2 offen offset:%c3 lds" ::"v"(v), "s"(srd), "s"(soff), "i"(OFF) : "memory");
}
#define LGKM0() __builtin_amdgcn_s_waitcnt(0xC07F)
DI bf16x8 lds_b128(unsigned a) { return *(const LAS bf16x8*)(size_t)a; }
DI bf16x4 lds_tr(unsigned a) { return __builtin_amdgcn_ds_read_tr16_b64_v4bf16((LAS bf16x4*)(size_t)a); }
DI bf16x8 cat(bf16x4 lo, bf16x4 hi) { return __builtin_shufflevector(lo, hi, 0, 1, 2, 3, 4, 5, 6, 7); }
DI bf16x8 lds_tr2(unsigned a0, unsigned a1) {
  bf16x4 lo = __builtin_amdgcn_ds_read_tr16_b64_v4bf16((LAS bf16x4*)(size_t)a0);
  bf16x4 hi = __builtin_amdgcn_ds_read_tr16_b64_v4bf16((LAS bf16x4*)(size_t)a1);
  return __builtin_shufflevector(lo, hi, 0, 1, 2, 3, 4, 5, 6, 7);
}
DI bf16x8 pack2(const f32x4& a, const f32x4& b, float s) {
  u32x4 w = {cvtpk(a[0] * s, a[1] * s), cvtpk(a[2] * s, a[3] * s), cvtpk(b[0] * s, b[1] * s), cvtpk(b[2] * s, b[3] * s)};
  return __builtin_bit_cast(bf16x8, w);
}
DI float col_max(float v) {
  auto r = __builtin_amdgcn_permlane16_swap(__float_as_uint(v), __float_as_uint(v), false, false);
  v = fmaxf(__uint_as_float(r[0]), __uint_as_float(r[1]));
  auto s = __builtin_amdgcn_permlane32_swap(__float_as_uint(v), __float_as_uint(v), false, false);
  return fmaxf(__uint_as_float(s[0]), __uint_as_float(s[1]));
}
DI float col_sum(float v) {
  auto r = __builtin_amdgcn_permlane16_swap(__float_as_uint(v), __float_as_uint(v), false, false);
  v = __uint_as_float(r[0]) + __uint_as_float(r[1]);
  auto s = __builtin_amdgcn_permlane32_swap(__float_as_uint(v), __float_as_uint(v), false, false);
  return __uint_as_float(s[0]) + __uint_as_float(s[1]);
}

template <class F, int... Is> DI void sfor_(F&& f, std::integer_sequence<int, Is...>) { (f(std::integral_constant<int, Is>{}), ...); }
template <int N, class F> DI void sfor(F&& f) { sfor_(f, std::make_integer_sequence<int, N>{}); }
template <int T> DI void mfa(bf16x8 a, bf16x8 b) { asm volatile("v_mfma_f32_16x16x32_bf16 a[%c2:%c3], %0, %1, a[%c2:%c3]" ::"v"(a), "v"(b), "i"(4 * T + 4), "i"(4 * T + 7)); }
template <int T> DI void mfa0(bf16x8 a, bf16x8 b) { asm volatile("v_mfma_f32_16x16x32_bf16 a[%c2:%c3], %0, %1, 0" ::"v"(a), "v"(b), "i"(4 * T + 4), "i"(4 * T + 7)); }
DI float max5(float a, float b, float c, float d, float e) { float r; asm volatile("v_max3_f32 %0, %1, %2, %3\n\tv_max3_f32 %0, %0, %4, %5" : "=&v"(r) : "v"(a), "v"(b), "v"(c), "v"(d), "v"(e)); return r; }
template <int T, int R> DI void mfa0a(bf16x8 a) { asm volatile("v_mfma_f32_16x16x32_bf16 a[%c1:%c2], %0, a[%c3:%c4], 0" ::"v"(a), "i"(4 * T + 4), "i"(4 * T + 7), "i"(4 * R + 4), "i"(4 * R + 7)); }
DI unsigned lds_ld(unsigned a) { return *(volatile LAS unsigned*)(size_t)a; }
DI void lds_st(unsigned a, unsigned v) { *(volatile LAS unsigned*)(size_t)a = v; }
DI void wait_ge(unsigned a, unsigned need) {
  unsigned sp = 0;
  while (__builtin_amdgcn_ballot_w64(lds_ld(a) < need) != 0ull && ++sp < SPIN_CAP) __builtin_amdgcn_s_sleep(1);
  asm volatile("" ::: "memory");
}
DI void mfv(f32x4& d, bf16x8 a, bf16x8 b) { asm volatile("v_mfma_f32_16x16x32_bf16 %0, %1, %2, %0" : "+v"(d) : "v"(a), "v"(b)); }
DI f32x4 mfv0(bf16x8 a, bf16x8 b, f32x4 c) { f32x4 d; asm volatile("v_mfma_f32_16x16x32_bf16 %0, %1, %2, %3" : "=&v"(d) : "v"(a), "v"(b), "v"(c)); return d; }
DI f32x4 mfv00(bf16x8 a, bf16x8 b) { f32x4 d; asm volatile("v_mfma_f32_16x16x32_bf16 %0, %1, %2, 0" : "=&v"(d) : "v"(a), "v"(b)); return d; }
template <int R> DI void mfv0a(f32x4& d, bf16x8 a) { asm volatile("v_mfma_f32_16x16x32_bf16 %0, %1, a[%c2:%c3], 0" : "+v"(d) : "v"(a), "i"(4 * R + 4), "i"(4 * R + 7)); }
DI void mfv00t(f32x4& d, bf16x8 a, bf16x8 b) { asm volatile("v_mfma_f32_16x16x32_bf16 %0, %1, %2, 0" : "+v"(d) : "v"(a), "v"(b)); }
template <int T> DI float acc_rd1() { float x; asm volatile("v_accvgpr_read_b32 %0, a%c1" : "=v"(x) : "i"(4 * T + 4)); return x; }
template <int T, int OFF> DI void acc_lds_wr(unsigned a) { asm volatile("ds_write_b128 %0, a[%c1:%c2] offset:%c3" ::"v"(a), "i"(4 * T + 4), "i"(4 * T + 7), "i"(OFF) : "memory"); }
template <int T, int OFF> DI void acc_lds_rd(unsigned a) { asm volatile("ds_read_b128 a[%c1:%c2], %0 offset:%c3" ::"v"(a), "i"(4 * T + 4), "i"(4 * T + 7), "i"(OFF) : "memory"); }
template <int T> DI f32x4 acc_rd() {
  float x0, x1, x2, x3;
  asm volatile("v_accvgpr_read_b32 %0, a%c4\n\tv_accvgpr_read_b32 %1, a%c5\n\tv_accvgpr_read_b32 %2, a%c6\n\tv_accvgpr_read_b32 %3, a%c7"
               : "=v"(x0), "=v"(x1), "=v"(x2), "=v"(x3) : "i"(4 * T + 4), "i"(4 * T + 5), "i"(4 * T + 6), "i"(4 * T + 7));
  return f32x4{x0, x1, x2, x3};
}
template <int T> DI void acc_wr(f32x4 v) {
  asm volatile("v_accvgpr_write_b32 a%c4, %0\n\tv_accvgpr_write_b32 a%c5, %1\n\tv_accvgpr_write_b32 a%c6, %2\n\tv_accvgpr_write_b32 a%c7, %3"
               ::"v"(v[0]), "v"(v[1]), "v"(v[2]), "v"(v[3]), "i"(4 * T + 4), "i"(4 * T + 5), "i"(4 * T + 6), "i"(4 * T + 7));
}
template <int T> DI void acc_zero() {
  asm volatile("v_accvgpr_write_b32 a%c0, 0\n\tv_accvgpr_write_b32 a%c1, 0\n\tv_accvgpr_write_b32 a%c2, 0\n\tv_accvgpr_write_b32 a%c3, 0"
               ::"i"(4 * T + 4), "i"(4 * T + 5), "i"(4 * T + 6), "i"(4 * T + 7));
}
#define NOPS(n) asm volatile("s_nop " #n)
template <int V> using IC = std::integral_constant<int, V>;

#define HQ 10
#define HO 63
#define FORCE_SAFE false
constexpr const char* ATT_FAST[7] = {"m0123p4567CDabcdp", "4tt5tt6tt7ttCptDt", "ttt0t1tt2345p67CD", "m0p12p34B5p67pCDm", "p01p23A45p67.pCDm", "p0123p4567CDpab.c", "3kk4kk5kk6kk7pCD."};
constexpr const char* ATT_SAFE[7] = {"xyzmw01234567CDpp", "xyz.w01234567CD..", "xyzpw01234567CDpm", "xyzpw01234567CDpp", "xyzpw01234567CDpp", "xyzpw01234567CDpp", "xyzpw01234567CDpp"};
struct AttGap { int ex, cv, tr, kr, pc, m0, bk; char c; };
constexpr AttGap att_gap(bool safe, int i, int g) {
  const char* const* tb = safe ? ATT_SAFE : ATT_FAST;
  AttGap r{-1, 0, -1, -1, -1, -1, 0, tb[i][g]};
  int nt = 0, nk = 0, np = 0;
  for (int k = 0; k < 17 * i + g; ++k) { const char c = tb[k / 17][k % 17]; nt += c == 't'; nk += c == 'k'; np += c == 'p'; }
  const char c = r.c;
  if (c >= '0' && c <= '7') r.ex = c - '0';
  if (c >= 'a' && c <= 'd') r.ex = 8 + c - 'a';
  if (c == 'C') r.cv = 3;
  if (c == 'D') r.cv = 12;
  if (c == 't') r.tr = nt;
  if (c == 'k') r.kr = nk;
  if (c == 'p') r.pc = np;
  if (c == 'm') { r.m0 = np; if (np == 0) r.bk = 1; }
  if (c == 'B') r.bk = 2;
  if (c == 'A') r.bk = 3;
  if (safe) {
    if (i == 1 && g >= 8) r.tr = g - 8;
    if (i == 2 && g < 7) r.tr = g + 9;
    if (i == 6 && g < 8) r.kr = g;
    if (i == 4 && g == 14) r.m0 = 8;
    if (i == 5 && g == 15) r.m0 = 12;
    if (i == 3 && g == 4) r.bk = 2;
    if (i == 1 && g == 3) r.bk = 3;
  }
  return r;
}
constexpr int att_pieces_before(bool safe, int i, int g) { int n = 0; for (int k = 0; k < 17 * i + g; ++k) n += att_gap(safe, k / 17, k % 17).pc >= 0; return n; }
static_assert(att_pieces_before(true, 7, 0) == 16 && att_pieces_before(false, 7, 0) == 16, "pieces");

#define NHX 16
DI void map_block(unsigned g, unsigned& b, unsigned& hg, unsigned& sh) {
  constexpr unsigned HG = NHX / 4, NXH = 32 / NHX, NBX = 32 / HG;
  const unsigned xcd = g & 7u, sl = g >> 3, j = sl % HG, bi = sl / HG;
  b = (xcd / NXH) * NBX + bi; hg = (xcd % NXH) * HG + j;
  sh = ((bi * 10u) / NBX + (j * 10u) / HG) % 10u;
}

__global__ __launch_bounds__(256, 1) void fused_k(const void* __restrict__ Xp, const void* __restrict__ Kp, const void* __restrict__ Vp,
                                                  const void* __restrict__ Wq, const void* __restrict__ Wo, void* __restrict__ Pws,
                                                  void* __restrict__ Out) {
  extern __shared__ __attribute__((aligned(16))) char lds[];
  const unsigned ldsb = (unsigned)(size_t)lds;
  const unsigned l = threadIdx.x & 63u, w = rfl(threadIdx.x >> 6);
  const bool early = w < 2u;
  unsigned b, hg, rot;
  map_block(blockIdx.x, b, hg, rot);
  const unsigned h = 4u * hg + w;
  const unsigned g4 = l >> 4, q15 = l & 15u, q4 = (l >> 2) & 3u, p4 = l & 3u;
  DBGX(unsigned seg[8]; unsigned segr[8];)
  STAMP(0);
  {
    u64 pc; asm volatile("s_getpc_b64 %0" : "=s"(pc));
    srd_t sC = __builtin_amdgcn_make_buffer_rsrc((void*)(pc & ~255ull), (short)0, CODE_BYTES, 0x00020000);
    setm0(ldsb + w * REG + 32768u); NOPS(0);
    asm volatile("buffer_load_dword %0, %1, 0 offen lds" ::"v"((((blockIdx.x >> 3) * 4u + w) * 4u + (l & 3u)) * 128u), "s"(sC) : "memory");
  }
  u64* const gfl = (u64*)Pws + b * 64u;
  u32x2 epv;
  asm volatile("global_load_dwordx2 %0, %1, %2 offset:256 sc1" : "=v"(epv) : "v"(0u), "s"(gfl) : "memory");
  srd_t sK = __builtin_amdgcn_make_buffer_rsrc((char*)Kp - 4096, (short)0, (int)(KV_BYTES + 4096u), 0x00020000);
  srd_t sV = __builtin_amdgcn_make_buffer_rsrc((char*)Vp - 4096, (short)0, (int)(KV_BYTES + 4096u), 0x00020000);
  srd_t sX = __builtin_amdgcn_make_buffer_rsrc((char*)Xp - 4096, (short)0, (int)(X_BYTES + 4096u), 0x00020000);
  srd_t sW = __builtin_amdgcn_make_buffer_rsrc((char*)Wq - 4096, (short)0, (int)(W_BYTES + 4096u), 0x00020000);
  const unsigned ring = ldsb + w * REG;
  const unsigned fl = ldsb + FLAGS;
  const unsigned drow = (l >> 2) & 7u, dsub = l >> 5;
  const unsigned kvo = (b * 32u + h) * 131072u;
  unsigned vk[4], vv[2];
#pragma unroll
  for (int i = 0; i < 4; ++i) vk[i] = drow * 256u + 64u * dsub + 16u * (p4 ^ (i & 2u)) + ((i & 1) ? 3200u : 4096u);
#pragma unroll
  for (int i = 0; i < 2; ++i) vv[i] = drow * 256u + 64u * dsub + 16u * (p4 ^ (2u * (drow >> 2))) + (i ? 3200u : 4096u);
  auto chunk = [&](unsigned i) { unsigned c = i + rot; return c >= 10u ? c - 10u : c; };
  auto xoff = [&](unsigned rx, int i) { return rx < 112u ? (b * 112u + rx) * 1280u + 64u * dsub + 16u * (p4 ^ (2u * (i & 1))) + 4096u - 1024u * i : NONE; };
  unsigned bx = ldsb + 1024u * (q15 >> 3) + 64u * (q15 & 7u) + 16u * (g4 ^ (2u * (q15 >> 3)));
  OPAQUE(bx);
  auto xfrag = [&](unsigned a0, unsigned a1, int q) { return lds_b128((q < 4 ? a0 : a1) + ((q >> 1) & 1) * REG + (q & 1) * 2048u); };
  unsigned vw[4];
#pragma unroll
  for (int i = 0; i < 4; ++i) vw[i] = (8u * (i >> 1) + drow) * 8192u + 128u * (i & 1) + 64u * dsub + 16u * p4 + h * 256u + 4096u - 1024u * i;

  asm volatile("" ::: "a255");
  bf16x8 qf[4][7];
  u64 ep;
  const unsigned hb = ldsb + (w & 1u) * REG + 24576u + l * 16u;
  {
    unsigned v1[4], vx[4];
#pragma unroll
    for (int i = 0; i < 4; ++i) {
      v1[i] = (8u * i + drow) * 8192u + 64u * dsub + 16u * p4 + (4u * hg + (w & 1u)) * 256u + (w >> 1) * 128u + 4096u - 1024u * i;
      vx[i] = xoff(32u * w + 8u * i + drow, i);
    }
    const unsigned mx0 = ring + 24576u;
    auto allW = [&](unsigned s) { setm0(ring + (((s >> 1) + 2u) % 3u) * 8192u + (s & 1u) * 4096u); NOPS(0);
      sfor<4>([&](auto P) { dma<0, 1024 * P>(v1[P], sW, (2u * chunk(s >> 1) + (s & 1u)) * 262144u); }); };
    auto allX = [&](unsigned i) { setm0(mx0 + i * 4096u); NOPS(0); sfor<4>([&](auto P) { dma<0, 1024 * P>(vx[P], sX, chunk(i) * 128u); }); };
    unsigned bw = ring + 1024u * g4 + 64u * q4 + 16u * p4;
    OPAQUE(bw);
    f32x4 qa[28];
    allX(0); allW(0); allW(1);
    sfor<28>([&](auto T) { qa[T] = f32x4{}; OPAQUE(qa[T]); });
    allX(1); allW(2); allW(3); allX(2); allW(4); allW(5);
    bf16x8 af[2][4], bf[2][7];
    waitv<28>();
    OPAQUE(epv);
    ep = ((u64)rfl(epv[1]) << 32) | rfl(epv[0]);
    BAR();
    STAMP(1);
#pragma unroll
    for (int d = 0; d < 4; ++d) { const unsigned a = bw + 16384u + 512u * (d >> 1) + 8u * (d & 1); af[0][d] = lds_tr2(a, a + 256u); }
#pragma unroll
    for (int q = 0; q < 7; ++q) bf[0][q] = xfrag(bx + 24576u, bx + 24576u + 2u * REG, q);
    LGKM0();
    unsigned sl = 16384u, xb = 0, cn = chunk(3);
    unsigned aw = bw + 16384u + 4096u, ax = bx + 24576u + 512u, ax2 = ax + 2u * REG;
    OPAQUE(aw); OPAQUE(ax); OPAQUE(ax2);
    srd_t sT = sW; unsigned so = cn * 524288u, hs = 262144u;
#pragma unroll 1
    for (unsigned i = 0; i < 10; ++i) {
      if (i >= 7u) {
        const unsigned j = i - 7u;
        if (early) {
#pragma unroll
          for (int k = 0; k < 4; ++k) v1[k] = j == 1u ? vv[k & 1] : vk[k];
          sT = j == 1u ? sV : sK; so = kvo + (j >> 1) * 8192u; hs = 4096u;
        } else {
#pragma unroll
          for (int k = 0; k < 4; ++k) v1[k] = vw[k];
          so = (2u * chunk(j >> 1) + (j & 1u)) * 262144u; hs = 131072u;
        }
      }
      const unsigned sln = sl == 16384u ? 0u : sl + 8192u, xbn = (xb + 4096u) & 12288u;
      sfor<2>([&](auto E) {
        constexpr int e = E;
        bf16x4 lo;
        unsigned awn = 0, axn = 0;
        sfor<28>([&](auto T) {
          constexpr int t = T, d = t / 7, q = t % 7;
          mfv(qa[t], af[e][d], bf[e][q]);
          GAP();
          if constexpr (t == 0) { if constexpr (e == 0) waitv<24>(); else waitv<18>(); }
          if constexpr (t == 1) { if constexpr (e == 1) BAR(); setm0(ring + sl + e * 4096u); }
          if constexpr (t % 3 == 2 && t / 3 < 8) {
            constexpr int j = t / 3, dd = j >> 1; const unsigned a = aw + 512u * (dd >> 1) + 8u * (dd & 1);
            if constexpr ((j & 1) == 0) lo = lds_tr(a); else af[e ^ 1][dd] = cat(lo, lds_tr(a + 256u));
          }
          if constexpr (t % 3 == 0 && t >= 3 && t <= 21) bf[e ^ 1][t / 3 - 1] = xfrag(ax, ax2, t / 3 - 1);
          if constexpr (t == 4 || t == 7 || t == 13 || t == 16) { constexpr int p = t == 4 ? 0 : t == 7 ? 1 : t == 13 ? 2 : 3; dma<0, 1024 * p>(v1[p], sT, so); }
          if constexpr (t == 19) setm0(mx0 + ((xb + 12288u) & 12288u));
          if constexpr (t == 22 || t == 25) { constexpr int p = 2 * e + (t == 25); if (i < 9u) dma<0, 1024 * p>(vx[p], sX, cn * 128u); }
          if constexpr (t == 10) { awn = e == 0 ? bw + sln : aw + 4096u; OPAQUE(awn); }
          if constexpr (t == 24) { axn = e == 0 ? bx + 24576u + xbn : ax + 512u; OPAQUE(axn); }
          if constexpr (t == 26) { ax2 = axn + 2u * REG; OPAQUE(ax2); if constexpr (e == 0) { so += hs; SPIN(so); } }
          if constexpr (t == 27) { LGKM0(); aw = awn; ax = axn; }
          GAP();
        });
      });
      sl = sln; xb = xbn; cn = cn == 9u ? 0u : cn + 1u; so = cn * 524288u;
    }
    NOPS(7);
    GAP();
    sfor<14>([&](auto T) { constexpr int k = T / 7, q = T % 7; qf[k][q] = pack2(qa[14 * k + q], qa[14 * k + 7 + q], QSCALE); OPAQUE(qf[k][q]); });
    waitv<8>();
    if (!early) {
      sfor<14>([&](auto T) { *(LAS bf16x8*)(size_t)(hb + (T / 7) * 2u * REG + (T % 7) * 1024u) = qf[T / 7][T % 7]; });
      if (w == 3u && l < 8u) lds_st(fl + l * 4u, 0u);
    }
    LGKM0(); BAR();
  }
  STAMP(2);
  if (early) {
    sfor<14>([&](auto T) { qf[2 + T / 7][T % 7] = lds_b128(hb + (T / 7) * 2u * REG + (T % 7) * 1024u); });
    LGKM0(); BAR();
    sfor<8>([&](auto P) { if constexpr ((P & 3) == 0) { setm0(ring + 24576u + P * 1024u); NOPS(0); } dma<1, 1024 * (P & 3)>(vv[P & 1], sV, kvo + 8192u + (P >> 2) * 4096u); });
  } else {
    BAR();
    unsigned vx[8];
#pragma unroll
    for (int i = 0; i < 8; ++i) vx[i] = xoff(64u * (w & 1u) + 8u * i + drow, i & 3);
    const unsigned mx0 = ldsb + (w & 1u) * 2u * REG + 32768u;
    const unsigned myf = fl + 16u + (w & 1u) * 4u, otf = fl + 16u + ((w & 1u) ^ 1u) * 4u;
    unsigned bw = ring + 2048u * g4 + 64u * q4 + 16u * p4;
    OPAQUE(bw);
    sfor<8>([&](auto P) { if constexpr ((P & 3) == 0) { setm0(ring + 24576u + P * 1024u); NOPS(0); } dma<0, 1024 * (P & 3)>(vw[P & 3], sW, (2u * chunk(1) + 1u) * 262144u + (P >> 2) * 131072u); });
    sfor<56>([&](auto T) { acc_zero<T>(); });
    bf16x8 af[2][8], bf[2][7];
#pragma unroll
    for (int d = 0; d < 8; ++d) { const unsigned a = bw + 512u * (d >> 1) + 8u * (d & 1); af[0][d] = lds_tr2(a, a + 256u); if (d < 7) bf[0][d] = xfrag(bx + 32768u, bx + 32768u + 2u * REG, d); }
    LGKM0();
    unsigned par = 0, cn = chunk(2), rn = 1;
    unsigned aw = bw + 8192u, ax = bx + 32768u + 512u, ax2 = ax + 2u * REG, rv = 1;
    OPAQUE(aw); OPAQUE(ax); OPAQUE(ax2); OPAQUE(rv);
    auto round = [&](auto TAIL) {
      constexpr bool tail = TAIL;
      unsigned so = tail ? kvo + par * 8192u : cn * 524288u, so2 = 0;
      sfor<2>([&](auto E) {
        constexpr int e = E;
        bf16x4 lo;
        unsigned awn = 0, axn = 0, pv = 0; u64 bad = 0;
        sfor<56>([&](auto T) {
          constexpr int t = T, d = t / 7, q = t % 7;
          mfa<t>(af[e][d], bf[e][q]);
          if constexpr (t == 0) { if constexpr (e == 0) waitv<16>(); else waitv<8>(); }
          if constexpr (e == 1) {
            if constexpr (t == 2) lds_st(myf, rv);
            if constexpr (t == 8) pv = lds_ld(otf);
            if constexpr (t == 14) { bad = __builtin_amdgcn_ballot_w64(pv < rn); SPIN(bad); }
            if constexpr (t == 17) { GAP(); if (__builtin_expect(bad != 0ull, 0)) wait_ge(otf, rn); asm volatile("" ::: "memory"); }
          }
          if constexpr (t % 3 == 1 && t / 3 < 16) {
            constexpr int j = t / 3, dd = j >> 1; const unsigned a = aw + 512u * (dd >> 1) + 8u * (dd & 1);
            if constexpr ((j & 1) == 0) lo = lds_tr(a); else af[e ^ 1][dd] = cat(lo, lds_tr(a + 256u));
          }
          if constexpr (t % 3 == 2 && t >= 20 && t <= 44 && t != 26 && t != 38) { constexpr int qq = (t - 20) / 3 - (t > 26) - (t > 38); bf[e ^ 1][qq] = xfrag(ax, ax2, qq); }
          constexpr int np = (e == 1 && !tail) ? 16 : 8;
          constexpr auto gap_of = [](int p) { return 3 + 3 * ((p * 16) / np); };
          sfor<np>([&](auto P) {
            constexpr int p = P;
            if constexpr ((p & 3) == 0 && t == gap_of(p) - 1) setm0(p >= 8 ? mx0 + par * 4096u + ((p - 8) >> 2) * REG : ring + par * 16384u + e * 8192u + p * 1024u);
            if constexpr (t == gap_of(p)) {
              if constexpr (HQ < 63) waitv<HQ - 1>();
              if constexpr (p >= 14) { if (w == 2u) dma<0, 1024 * (p & 3)>(vx[p & 7], sX, cn * 128u); }
              else if constexpr (p >= 8) dma<0, 1024 * (p & 3)>(vx[p & 7], sX, cn * 128u);
              else if constexpr (!tail) dma<0, 1024 * (p & 3)>(vw[p & 3], sW, p < 4 ? so : so2);
              else if constexpr (e == 0) dma<1, 1024 * (p & 3)>(vk[p & 3], sK, p < 4 ? so : so2);
              else dma<1, 1024 * (p & 3)>(vv[p & 1], sV, p < 4 ? so : so2);
            }
          });
          if constexpr (t == 5) { so2 = so + (tail ? 4096u : 131072u); SPIN(so2); }
          if constexpr (t == 47) { awn = e == 0 ? bw + (par ^ 1u) * 16384u : aw + 8192u; OPAQUE(awn); }
          if constexpr (t == 50) { axn = e == 0 ? bx + 32768u + (par ^ 1u) * 4096u : ax + 512u; OPAQUE(axn); }
          if constexpr (t == 52) { ax2 = axn + 2u * REG; OPAQUE(ax2); }
          if constexpr (t == 53 && e == 1) { rv += 1u; OPAQUE(rv); }
          if constexpr (t == 55) { LGKM0(); aw = awn; ax = axn; if constexpr (e == 0 && !tail) { so += 262144u; SPIN(so); } }
          GAP();
        });
      });
      par ^= 1u; cn = cn == 9u ? 0u : cn + 1u; rn += 1u;
    };
#pragma unroll 1
    for (unsigned i = 0; i < 8; ++i) round(std::false_type{});
#pragma unroll 1
    for (unsigned i = 8; i < 10; ++i) round(std::true_type{});
    lds_st(myf, 11u);
    NOPS(7);
    sfor<28>([&](auto T) { constexpr int k = T / 7, q = T % 7; qf[k][q] = pack2(acc_rd<14 * k + q>(), acc_rd<14 * k + 7 + q>(), QSCALE); OPAQUE(qf[k][q]); });
  }
  STAMP(3);
  float lsum[7];
  {
    bf16x8 ones = __builtin_bit_cast(bf16x8, u32x4{0x3f803f80u, 0x3f803f80u, 0x3f803f80u, 0x3f803f80u});
    OPAQUE(ones);
    unsigned nsafe = FORCE_SAFE ? 16u : 1u;
#pragma unroll 1
    for (;;) {
      const unsigned l = lane_id(), g4 = l >> 4, q15 = l & 15u, q4 = (l >> 2) & 3u, p4 = l & 3u;
      unsigned ak = ring + 2048u * (q15 >> 3) + 64u * (q15 & 7u) + 16u * (g4 ^ (2u * (q15 >> 3)));
      unsigned av = ring + 8192u + 2048u * (g4 >> 1) + 64u * (4u * (g4 & 1u) + q4) + 16u * ((p4 >> 1) ^ (2u * (g4 & 1u))) + 8u * (p4 & 1u), av1 = av ^ 32u;
      OPAQUE(ak); OPAQUE(av); OPAQUE(av1);
      bf16x8 kf[2][4], vf[8], pp;
      f32x4 sp0, sp1, nm[7];
      waitv<24>();
#pragma unroll
      for (int e = 0; e < 8; ++e) kf[e >> 2][e & 3] = lds_b128(ak + 4096u * (e >> 2) + 512u * (e & 3));
      ak += 16384u;
      auto check = [&](auto Q, u64 big, float mx) {
        constexpr int q = Q;
        if (__builtin_expect(big != 0ull, 0)) {
          const float d = fmaxf(col_max(mx), 0.f), al = __builtin_amdgcn_exp2f(-d);
          NOPS(7);
          sfor<9>([&](auto T) { acc_wr<7 * T + q>(acc_rd<7 * T + q>() * al); });
          nm[q] -= d; sp0 -= d; sp1 -= d;
          NOPS(1);
        }
      };
      auto setref = [&](auto Q, float mx) { constexpr int q = Q; const float d = col_max(mx); nm[q] = f32x4{-d, -d, -d, -d}; OPAQUE(nm[q]); sp0 -= d; sp1 -= d; };
      unsigned so = kvo + 16384u, mk = ring, dl = 16384u;
      auto step = [&](unsigned s, auto MODE) {
        constexpr bool safe = MODE != 0, first = MODE == 2;
        const int nrec = s < 14u ? (int)(KV_BYTES + 4096u) : 0;
        srd_t sKs = __builtin_amdgcn_make_buffer_rsrc((char*)Kp - 4096, (short)0, nrec, 0x00020000);
        srd_t sVs = __builtin_amdgcn_make_buffer_rsrc((char*)Vp - 4096, (short)0, nrec, 0x00020000);
        unsigned so2 = 0; bf16x4 lo;
        sfor<7>([&](auto I) {
          constexpr int i = I, qs = (i + 6) % 7, qp = (i + 5) % 7;
          f32x4 s0, s1; float mx; u64 big; u32x4 pn;
          sfor<17>([&](auto G) {
            constexpr int g = G;
            constexpr AttGap ag = att_gap(safe, i, g);
            constexpr bool rev = !safe && i == 1;
            constexpr int m = rev ? (g < 9 ? g + 8 : g - 9) : g;
            if constexpr (g == 0 && i == 6) waitv<8 + att_pieces_before(safe, 6, 0)>();
            if constexpr (g == (safe ? 8 : 0) && i == 1) waitv<16 + att_pieces_before(safe, 1, g)>();
            if constexpr (m < 2 && first) (m ? s1 : s0) = mfv00(kf[m][0], qf[0][i]);
            else if constexpr (m < 2) (m ? s1 : s0) = mfv0(kf[m][0], qf[0][i], nm[i]);
            else if constexpr (m < 8) mfv((m & 1) ? s1 : s0, kf[m & 1][m >> 1], qf[m >> 1][i]);
            else if constexpr (first && i < 2) acc_zero<7 * (m - 8) + qp>();
            else if constexpr (first) mfa0<7 * (m - 8) + qp>(m < 16 ? vf[(m - 8) & 7] : ones, pp);
            else mfa<7 * (m - 8) + qp>(m < 16 ? vf[(m - 8) & 7] : ones, pp);
            GAP();
            if constexpr (ag.kr >= 0) kf[ag.kr & 1][ag.kr >> 1] = lds_b128(ak + 4096u * (ag.kr & 1) + 512u * (ag.kr >> 1));
            if constexpr (ag.tr >= 0) { constexpr int d = ag.tr >> 1; const unsigned a = ((d & 1) ? av1 : av) + 512u * (d >> 1);
              if constexpr ((ag.tr & 1) == 0) lo = lds_tr(a); else vf[d] = cat(lo, lds_tr(a + 4096u)); }
            if constexpr (!(first && i == 0)) {
              if constexpr (ag.c == 'x') mx = max5(sp0[0], sp0[1], sp0[2], sp0[3], sp1[0]);
              if constexpr (ag.c == 'y') mx = max5(mx, sp1[1], sp1[2], sp1[3], sp1[3]);
              if constexpr (ag.c == 'z' && first) setref(IC<qs>{}, mx);
              if constexpr (ag.c == 'z' && !first) { big = __builtin_amdgcn_ballot_w64(mx > THR); SPIN(big); }
              if constexpr (ag.c == 'w' && !first) check(IC<qs>{}, big, mx);
              if constexpr (ag.ex >= 0 && ag.ex < 4) { sp0[ag.ex] = __builtin_amdgcn_exp2f(sp0[ag.ex]); OPAQUE(sp0[ag.ex]); }
              if constexpr (ag.ex >= 4 && ag.ex < 8) { sp1[ag.ex - 4] = __builtin_amdgcn_exp2f(sp1[ag.ex - 4]); OPAQUE(sp1[ag.ex - 4]); }
              if constexpr (ag.ex >= 8) { s0[ag.ex - 8] = __builtin_amdgcn_exp2f(s0[ag.ex - 8]); OPAQUE(s0[ag.ex - 8]); }
              if constexpr (ag.cv & 1) { pn[0] = cvtpk(sp0[0], sp0[1]); OPAQUE(pn[0]); }
              if constexpr (ag.cv & 2) { pn[1] = cvtpk(sp0[2], sp0[3]); OPAQUE(pn[1]); }
              if constexpr (ag.cv & 4) { pn[2] = cvtpk(sp1[0], sp1[1]); OPAQUE(pn[2]); }
              if constexpr (ag.cv & 8) { pn[3] = cvtpk(sp1[2], sp1[3]); OPAQUE(pn[3]); }
            }
            if constexpr (ag.pc >= 0) { constexpr int p = ag.pc;
              if constexpr (p < 8) dma<1, 1024 * (p & 3)>(vk[p & 3], sKs, p < 4 ? so : so2); else dma<1, 1024 * (p & 3)>(vv[p & 1], sVs, p < 12 ? so : so2); }
            if constexpr (ag.m0 >= 0) setm0(mk + ag.m0 * 1024u);
            if constexpr (ag.bk == 1) { so2 = so + 4096u; SPIN(so2); }
            if constexpr (ag.bk == 2) { av += dl; OPAQUE(av); av1 += dl; OPAQUE(av1); }
            if constexpr (ag.bk == 3) { ak += dl; OPAQUE(ak); }
            if constexpr (i == 6 && g == 16) LGKM0();
            GAP();
          });
          if constexpr (!(first && i == 0)) pp = __builtin_bit_cast(bf16x8, pn);
          sp0 = s0; sp1 = s1;
        });
        so += 8192u; mk += dl; dl = 0u - dl;
      };
      ak -= 16384u;
      step(0u, IC<2>{});
      { float mx = max5(sp0[0], sp0[1], sp0[2], sp0[3], sp1[0]); mx = max5(mx, sp1[1], sp1[2], sp1[3], sp1[3]); setref(IC<6>{}, mx); GAP(); }
      unsigned s = 1;
#pragma unroll 1
      for (; s < nsafe; ++s) step(s, IC<1>{});
#pragma unroll 1
      for (; s < 16u; ++s) step(s, IC<0>{});
      { float mx = max5(sp0[0], sp0[1], sp0[2], sp0[3], sp1[0]); mx = max5(mx, sp1[1], sp1[2], sp1[3], sp1[3]);
        check(IC<6>{}, __builtin_amdgcn_ballot_w64(mx > THR), mx); }
      NOPS(1);
      sfor<9>([&](auto D) {
        mfa<7 * D + 5>(D < 8 ? vf[D & 7] : ones, pp);
        GAP();
        constexpr int d = D;
        if constexpr (d < 4) { sp0[d] = __builtin_amdgcn_exp2f(sp0[d]); OPAQUE(sp0[d]); } else if constexpr (d < 8) { sp1[d - 4] = __builtin_amdgcn_exp2f(sp1[d - 4]); OPAQUE(sp1[d - 4]); }
        GAP();
      });
      pp = pack2(sp0, sp1, 1.f); OPAQUE(pp);
      NOPS(1);
      mfa<62>(ones, pp);
      sfor<8>([&](auto D) { mfa<7 * D + 6>(vf[D], pp); });
      bool bad = false;
      sfor<7>([&](auto Q) { lsum[Q] = acc_rd1<56 + Q>(); bad |= !(lsum[Q] < 1.0995116e12f); });
      if (__builtin_expect(nsafe == 16u || __builtin_amdgcn_ballot_w64(bad) == 0ull, 1)) break;
      nsafe = 16u;
      waitv<0>();
      sfor<32>([&](auto P) { constexpr int p = P; if constexpr ((p & 3) == 0) { setm0(ring + p * 1024u); NOPS(0); }
        if constexpr ((p >> 3) & 1) dma<1, 1024 * (p & 3)>(vv[p & 1], sV, kvo + (p >> 4) * 8192u + ((p >> 2) & 1) * 4096u);
        else dma<1, 1024 * (p & 3)>(vk[p & 3], sK, kvo + (p >> 4) * 8192u + ((p >> 2) & 1) * 4096u); });
    }
  }
  STAMP(4);
  {
    const unsigned l = lane_id(), g4 = l >> 4, q15 = l & 15u, q4 = (l >> 2) & 3u, p4 = l & 3u, drow = (l >> 2) & 7u;
    srd_t sO = __builtin_amdgcn_make_buffer_rsrc((char*)Wo - 4096, (short)0, (int)(W_BYTES + 4096u), 0x00020000);
    srd_t sP = __builtin_amdgcn_make_buffer_rsrc((char*)Pws + F_BYTES, (short)0, (int)P_BYTES, 0x00020000);
    unsigned vo[4];
#pragma unroll
    for (int i = 0; i < 4; ++i) vo[i] = (hg * 512u + 8u * i + drow) * 1280u + (w & 1u) * 640u + 64u * (l >> 5) + 16u * p4 + 4096u - 1024u * i;
    const unsigned slot0 = ring + 28672u;
    const unsigned rot8 = (rot * 13u) >> 4, sg0 = early ? 0u : 8u;
    auto cchunk = [&](unsigned sg) { return (unsigned)(0x1023443210ull >> (4u * sg)) & 7u; };
    auto sbase = [&](unsigned sg) { return ((0x7e0u >> sg) & 1u) * 327680u + cchunk(sg) * 128u; };
    auto allO = [&](unsigned u) { setm0(slot0 + u * 4096u); NOPS(0); sfor<4>([&](auto P) { dma<0, 1024 * P>(vo[P], sO, sbase(sg0) + ((u + rot8) & 7u) * 40960u); }); };
    waitv<0>();
    wait_ge(fl + 16u + (l & 1u) * 4u, 11u);
    allO(0); allO(1);
    sfor<7>([&](auto Q) {
      constexpr int q = Q;
      const float inv = __builtin_amdgcn_rcpf(lsum[q]);
      sfor<4>([&](auto K) { constexpr int k = K; *(LAS bf16x8*)(size_t)(ring + l * 16u + (k * 7 + q) * 1024u) = pack2(acc_rd<14 * k + q>(), acc_rd<14 * k + 7 + q>(), inv); });
    });
    unsigned ba = slot0 + 1024u * (g4 >> 1) + 64u * (4u * (g4 & 1u) + q4) + 16u * p4, bb = ldsb + l * 16u;
    OPAQUE(ba); OPAQUE(bb);
    bf16x8 af[2][4], bf[2][7];
    if (early) { lds_st(fl + w * 4u, 1u); wait_ge(fl + (l & 1u) * 4u, 1u); }
    else { DBGX(STAMP(5);) LGKM0(); BAR(); DBGX(STAMP(6);) }
    allO(2);
    waitv<8>();
#pragma unroll
    for (int n = 0; n < 4; ++n) { const unsigned a = ba + 8u * (n & 1) + 512u * (n >> 1); af[0][n] = lds_tr2(a, a + 2048u); }
    unsigned k1 = (1u + rot8) & 7u, o3 = ((3u + rot8) & 7u) * 40960u, sl = 0;
    unsigned aa = ba + 4096u;
    OPAQUE(aa);
    unsigned bs = bb;
    auto firstB = [&]() { const unsigned ab = bs + rot8 * 7168u + (rot8 & 4u) * 3072u;
#pragma unroll
      for (int q = 0; q < 7; ++q) bf[0][q] = lds_b128(ab + 1024u * q); };
    f32x4 oa[28];
    sfor<28>([&](auto T) { f32x4 u; asm volatile("" : "=v"(u)); oa[T] = u; });
    auto unpack = [&](auto R) {
      const unsigned ll = lane_id(), one = (ll >> 4) == ((ll >> 2) & 3u) ? (ll & 1u ? 0x3f800000u : 0x00003f80u) : 0u, lo = (ll & 2u) ? 0u : one, hi = (ll & 2u) ? one : 0u;
      bf16x8 sel[2] = {__builtin_bit_cast(bf16x8, u32x4{lo, hi, 0u, 0u}), __builtin_bit_cast(bf16x8, u32x4{0u, 0u, lo, hi})};
      OPAQUE(sel[0]); OPAQUE(sel[1]); NOPS(1);
      sfor<28>([&](auto T) { mfv0a<R + T / 2>(oa[T], sel[T & 1]); });
    };
    auto packed = [&](auto T) { return pack2(oa[2 * T], oa[2 * T + 1], 1.f); };
    auto gput = [&](unsigned idx, u64 val) {
      asm volatile("s_mov_b64 exec, 1\n\tglobal_store_dwordx2 %0, %1, %2 sc1\n\ts_mov_b64 exec, -1" ::"v"(idx * 8u), "v"(val), "s"(gfl) : "memory"); };
    auto pairU = [&](unsigned i, unsigned sc, unsigned sn, auto ZERO, auto AFTER) {
      {
        sfor<2>([&](auto E) {
          constexpr int e = E;
          unsigned ab = 0, ab2 = 0, so = 0, s2 = 0; bf16x4 lo;
          sfor<28>([&](auto T) {
            constexpr int t = T, q = t / 4, n = t % 4;
            if constexpr (ZERO && e == 0) mfv00t(oa[t], af[e][n], bf[e][q]); else mfv(oa[t], af[e][n], bf[e][q]);
            GAP();
            if constexpr (t == 0) {
              if constexpr (AFTER) asm volatile("s_cmp_eq_u32 %0, 0\n\ts_cbranch_scc1 1f\n\ts_waitcnt vmcnt(4)\n1:\n\ts_waitcnt vmcnt(18)" ::"s"(i) : "scc", "memory"); else waitv<4>();
              setm0(slot0 + sl); so = ((i + (e ? 2u : 1u)) >> 2) * sn; SPIN(so); }
            if constexpr (t == 1) { so += o3 + sc; SPIN(so); }
            if constexpr (t == 2) { ab2 = (k1 & 4u) * 3072u; SPIN(ab2); }
            if constexpr (t == 3) { ab = k1 * 7168u + ab2; SPIN(ab); }
            if constexpr (t == 5) { ab += bs; OPAQUE(ab); sl = sl == 8192u ? 0u : sl + 4096u; SPIN(sl); }
            if constexpr ((t == 2 || t == 6 || t == 10 || t == 14) && HO < 63) waitv<HO - 1>();
            if constexpr (t == 2 || t == 6 || t == 10 || t == 14) dma<0, 1024 * ((t - 2) >> 2)>(vo[(t - 2) >> 2], sO, so);
            if constexpr (t == 4 || t == 8 || t == 12 || (t >= 16 && t <= 20)) { constexpr int j = t < 16 ? t / 4 - 1 : t - 13; const unsigned a = aa + 4u * (j & 2) + 512u * (j >> 2);
              if constexpr ((j & 1) == 0) lo = lds_tr(a); else af[e ^ 1][j >> 1] = cat(lo, lds_tr(a + 2048u)); }
            if constexpr ((t >= 7 && t <= 15 && (t & 1)) || t == 21 || t == 22) { constexpr int qq = t <= 15 ? (t - 7) >> 1 : t - 16; bf[e ^ 1][qq] = lds_b128(ab + 1024u * qq); }
            if constexpr (t == 23) { s2 = sl == 8192u ? 0u : sl + 4096u; SPIN(s2); }
            if constexpr (t == 24) { aa = ba + s2; OPAQUE(aa); k1 = (k1 + 1u) & 7u; SPIN(k1); }
            if constexpr (t == 25) { o3 += 40960u; SPIN(o3); }
            if constexpr (t == 26) { o3 = o3 == 327680u ? 0u : o3; SPIN(o3); }
            if constexpr (t == 27) LGKM0();
            GAP();
          });
        });
      }
    };
    if (early) {
      firstB(); LGKM0();
#pragma unroll 1
      for (unsigned sg = 0; sg < 5u; ++sg) {
        const unsigned sc = sbase(sg), sn = sbase(sg + 1u) - sc;
        pairU(0u, sc, sn, std::true_type{}, std::false_type{});
#pragma unroll 1
        for (unsigned i = 1; i < 4; ++i) pairU(i, sc, sn, std::false_type{}, std::false_type{});
        NOPS(7); GAP();
        sfor<4>([&](auto C) { if (sg == C) sfor<14>([&](auto T) { acc_wr<14 * C + T>(__builtin_bit_cast(f32x4, packed(T))); }); });
      }
      lds_st(fl + w * 4u, 2u); wait_ge(fl + (lane_id() & 1u) * 4u, 2u);
      const unsigned a = bb + w * REG;
      sfor<28>([&](auto T) { acc_lds_wr<T, T * 1024>(a); });
      DBGX(STAMP(5);)
      asm volatile("s_waitcnt lgkmcnt(0)" ::: "memory"); BAR();
      DBGX(STAMP(6);)
    } else {
      const unsigned a = bb + (w & 1u) * REG;
      sfor<28>([&](auto T) { acc_lds_rd<28 + T, T * 1024>(a); });
      asm volatile("s_waitcnt lgkmcnt(0)" ::: "memory");
      unpack(IC<28>{});
    }
    bs = bb + 2u * REG; OPAQUE(bs);
    firstB();
    LGKM0();
    auto lastseg = [&](unsigned sg, auto AFTER) {
      const unsigned sc = sbase(sg), sn = sbase(sg + 1u) - sc;
#pragma unroll 1
      for (unsigned i = 0; i < 4; ++i) {
        pairU(i, sc, sn, std::false_type{}, AFTER);
      }
      NOPS(7); GAP();
      const unsigned po = ((b * 10u + (w & 1u) * 5u + cchunk(sg)) * 8u + hg) * P_UNIT + lane_id() * 16u;
      sfor<14>([&](auto T) { __builtin_amdgcn_raw_buffer_store_b128(__builtin_bit_cast(u32x4, packed(T)), sP, po + T * 1024u, 0, 16); });
      GAP();
    };
    lastseg(early ? 5u : 8u, std::false_type{});
    unpack(IC<42>{});
    lastseg(early ? 6u : 9u, std::true_type{});
    if (early) {
      unpack(IC<28>{});
      lastseg(7u, std::true_type{});
    }
    waitv<0>();
    DBGX(STAMP(7);)
    BAR();
    const unsigned ll = lane_id();
    if (w == 0u) {
      gput(hg, ep + 1ull);
      unsigned sp = 0;
      while (__builtin_amdgcn_ballot_w64(__hip_atomic_load(gfl + (ll & 7u), __ATOMIC_RELAXED, __HIP_MEMORY_SCOPE_AGENT) != ep + 1ull) != 0ull && ++sp < SPIN_CAP) __builtin_amdgcn_s_sleep(2);
      if (hg == 0u) gput(32u, ep + 1ull);
    }
    BAR();
    srd_t sOut = __builtin_amdgcn_make_buffer_rsrc(Out, (short)0, (int)X_BYTES, 0x00020000);
    u32x4 v[3][2][8];
    unsigned oo[3];
#pragma unroll
    for (int j = 0; j < 3; ++j) {
      const unsigned u = hg + 8u * (w + 4u * j), ch = (u * 37u) >> 8, q = u - 7u * ch;
      const unsigned pi = u < 70u ? (b * 10u + ch) * 8u * P_UNIT + q * 2048u + ll * 16u : NONE;
      oo[j] = u < 70u ? (b * 112u + q * 16u + (ll & 15u)) * 1280u + ch * 128u + (ll >> 4) * 16u : NONE;
      if (j < 2 || w == 0u) {
#pragma unroll
        for (int k = 0; k < 8; ++k) { v[j][0][k] = __builtin_amdgcn_raw_buffer_load_b128(sP, pi + k * P_UNIT, 0, 16); v[j][1][k] = __builtin_amdgcn_raw_buffer_load_b128(sP, pi + k * P_UNIT + 1024u, 0, 16); }
      }
    }
#pragma unroll
    for (int j = 0; j < 3; ++j) {
      if (j < 2 || w == 0u) {
#pragma unroll
        for (int r = 0; r < 2; ++r) {
          u32x4 o;
#pragma unroll
          for (int e = 0; e < 4; ++e) {
            float lo = __uint_as_float(v[j][r][0][e] << 16), hi = __uint_as_float(v[j][r][0][e] & 0xffff0000u);
#pragma unroll
            for (int k = 1; k < 8; ++k) { lo += __uint_as_float(v[j][r][k][e] << 16); hi += __uint_as_float(v[j][r][k][e] & 0xffff0000u); }
            o[e] = cvtpk(lo, hi);
          }
          __builtin_amdgcn_raw_buffer_store_b128(o, sOut, oo[j] + r * 64u, 0, 0);
        }
      }
    }
  }
}

extern "C" void kernel_launch(void* const* d_in, const int* in_sizes, int n_in, void* d_out, int out_size, void* d_ws, size_t ws_size,
                              hipStream_t stream) {
  (void)in_sizes; (void)n_in; (void)out_size; (void)ws_size;
  (void)hipFuncSetAttribute((const void*)fused_k, hipFuncAttributeMaxDynamicSharedMemorySize, (int)LDS_ALL);
  fused_k<<<256, 256, LDS_ALL, stream>>>(d_in[0], d_in[1], d_in[2], d_in[3], d_in[4], d_ws, d_out);
}
```

```cpp
#include <hip/hip_runtime.h>
#include <hip/hip_bf16.h>
#include <utility>

typedef __bf16 bf16x8 __attribute__((ext_vector_type(8)));
typedef __bf16 bf16x4 __attribute__((ext_vector_type(4)));
typedef __bf16 bf16x2 __attribute__((ext_vector_type(2)));
typedef float f32x4 __attribute__((ext_vector_type(4)));
typedef float f32x2 __attribute__((ext_vector_type(2)));
typedef unsigned u32x4 __attribute__((ext_vector_type(4)));
typedef unsigned u32x2 __attribute__((ext_vector_type(2)));
typedef unsigned long long u64;
typedef __amdgpu_buffer_rsrc_t srd_t;
#define LAS __attribute__((address_space(3)))
#define DI __device__ __forceinline__
#define GAP() __builtin_amdgcn_sched_barrier(0)
#define OPAQUE(x) asm volatile("" : "+v"(x))
#define SPIN(x) asm volatile("" : "+s"(x))
#define BAR() do { asm volatile("" ::: "memory"); __builtin_amdgcn_s_barrier(); asm volatile("" ::: "memory"); } while (0)

constexpr unsigned KV_BYTES = 134217728u, X_BYTES = 3584u * 1280u, W_BYTES = 640u * 8192u;
constexpr unsigned P_UNIT = 14336u, P_BYTES = 320u * 8u * P_UNIT;
constexpr unsigned F_BYTES = 32u * 512u;
constexpr int CODE_BYTES = 52608;
constexpr unsigned REG = 40960u, LDS_ALL = 163840u, FLAGS = 3u * REG + 38912u;
constexpr unsigned SPIN_CAP = 1u << 22;
constexpr float QSCALE = 0.12751743082f;
constexpr float THR = 8.0f;
constexpr unsigned NONE = 0x80000000u;

#define STAMP(i)
#define DBGX(x)

DI unsigned lane_id() { unsigned r; asm volatile("v_mbcnt_lo_u32_b32 %0, -1, 0\n\tv_mbcnt_hi_u32_b32 %0, -1, %0" : "=v"(r)); return r; }
DI unsigned rfl(unsigned x) { return __builtin_amdgcn_readfirstlane(x); }
DI unsigned cvtpk(float lo, float hi) { f32x2 v = {lo, hi}; bf16x2 b = __builtin_convertvector(v, bf16x2); return __builtin_bit_cast(unsigned, b); }
template <int N> DI void waitv() { asm volatile("s_waitcnt vmcnt(%c0)" ::"i"(N) : "memory"); }
DI void setm0(unsigned v) { asm volatile("s_mov_b32 m0, %0" ::"s"(v) : "m0", "memory"); }
template <int NT, int OFF> DI void dma(unsigned v, srd_t srd, unsigned soff) {
  if constexpr (NT) asm volatile("buffer_load_dwordx4 %0, %1, %2 offen offset:%c3 nt lds" ::"v"(v), "s"(srd), "s"(soff), "i"(OFF) : "memory");
  else asm volatile("buffer_load_dwordx4 %0, %1, %2 offen offset:%c3 lds" ::"v"(v), "s"(srd), "s"(soff), "i"(OFF) : "memory");
}
#define LGKM0() __builtin_amdgcn_s_waitcnt(0xC07F)
DI bf16x8 lds_b128(unsigned a) { return *(const LAS bf16x8*)(size_t)a; }
DI bf16x4 lds_tr(unsigned a) { return __builtin_amdgcn_ds_read_tr16_b64_v4bf16((LAS bf16x4*)(size_t)a); }
DI bf16x8 cat(bf16x4 lo, bf16x4 hi) { return __builtin_shufflevector(lo, hi, 0, 1, 2, 3, 4, 5, 6, 7); }
DI bf16x8 lds_tr2(unsigned a0, unsigned a1) {
  bf16x4 lo = __builtin_amdgcn_ds_read_tr16_b64_v4bf16((LAS bf16x4*)(size_t)a0);
  bf16x4 hi = __builtin_amdgcn_ds_read_tr16_b64_v4bf16((LAS bf16x4*)(size_t)a1);
  return __builtin_shufflevector(lo, hi, 0, 1, 2, 3, 4, 5, 6, 7);
}
DI bf16x8 pack2(const f32x4& a, const f32x4& b, float s) {
  u32x4 w = {cvtpk(a[0] * s, a[1] * s), cvtpk(a[2] * s, a[3] * s), cvtpk(b[0] * s, b[1] * s), cvtpk(b[2] * s, b[3] * s)};
  return __builtin_bit_cast(bf16x8, w);
}
DI float col_max(float v) {
  auto r = __builtin_amdgcn_permlane16_swap(__float_as_uint(v), __float_as_uint(v), false, false);
  v = fmaxf(__uint_as_float(r[0]), __uint_as_float(r[1]));
  auto s = __builtin_amdgcn_permlane32_swap(__float_as_uint(v), __float_as_uint(v), false, false);
  return fmaxf(__uint_as_float(s[0]), __uint_as_float(s[1]));
}
DI float col_sum(float v) {
  auto r = __builtin_amdgcn_permlane16_swap(__float_as_uint(v), __float_as_uint(v), false, false);
  v = __uint_as_float(r[0]) + __uint_as_float(r[1]);
  auto s = __builtin_amdgcn_permlane32_swap(__float_as_uint(v), __float_as_uint(v), false, false);
  return __uint_as_float(s[0]) + __uint_as_float(s[1]);
}

template <class F, int... Is> DI void sfor_(F&& f, std::integer_sequence<int, Is...>) { (f(std::integral_constant<int, Is>{}), ...); }
template <int N, class F> DI void sfor(F&& f) { sfor_(f, std::make_integer_sequence<int, N>{}); }
template <int T> DI void mfa(bf16x8 a, bf16x8 b) { asm volatile("v_mfma_f32_16x16x32_bf16 a[%c2:%c3], %0, %1, a[%c2:%c3]" ::"v"(a), "v"(b), "i"(4 * T + 4), "i"(4 * T + 7)); }
template <int T> DI void mfa0(bf16x8 a, bf16x8 b) { asm volatile("v_mfma_f32_16x16x32_bf16 a[%c2:%c3], %0, %1, 0" ::"v"(a), "v"(b), "i"(4 * T + 4), "i"(4 * T + 7)); }
DI float max5(float a, float b, float c, float d, float e) { float r; asm volatile("v_max3_f32 %0, %1, %2, %3\n\tv_max3_f32 %0, %0, %4, %5" : "=&v"(r) : "v"(a), "v"(b), "v"(c), "v"(d), "v"(e)); return r; }
template <int T, int R> DI void mfa0a(bf16x8 a) { asm volatile("v_mfma_f32_16x16x32_bf16 a[%c1:%c2], %0, a[%c3:%c4], 0" ::"v"(a), "i"(4 * T + 4), "i"(4 * T + 7), "i"(4 * R + 4), "i"(4 * R + 7)); }
DI unsigned lds_ld(unsigned a) { return *(volatile LAS unsigned*)(size_t)a; }
DI void lds_st(unsigned a, unsigned v) { *(volatile LAS unsigned*)(size_t)a = v; }
DI void wait_ge(unsigned a, unsigned need) {
  unsigned sp = 0;
  while (__builtin_amdgcn_ballot_w64(lds_ld(a) < need) != 0ull && ++sp < SPIN_CAP) __builtin_amdgcn_s_sleep(1);
  asm volatile("" ::: "memory");
}
DI void mfv(f32x4& d, bf16x8 a, bf16x8 b) { asm volatile("v_mfma_f32_16x16x32_bf16 %0, %1, %2, %0" : "+v"(d) : "v"(a), "v"(b)); }
DI f32x4 mfv0(bf16x8 a, bf16x8 b, f32x4 c) { f32x4 d; asm volatile("v_mfma_f32_16x16x32_bf16 %0, %1, %2, %3" : "=&v"(d) : "v"(a), "v"(b), "v"(c)); return d; }
DI f32x4 mfv00(bf16x8 a, bf16x8 b) { f32x4 d; asm volatile("v_mfma_f32_16x16x32_bf16 %0, %1, %2, 0" : "=&v"(d) : "v"(a), "v"(b)); return d; }
template <int R> DI void mfv0a(f32x4& d, bf16x8 a) { asm volatile("v_mfma_f32_16x16x32_bf16 %0, %1, a[%c2:%c3], 0" : "+v"(d) : "v"(a), "i"(4 * R + 4), "i"(4 * R + 7)); }
DI void mfv00t(f32x4& d, bf16x8 a, bf16x8 b) { asm volatile("v_mfma_f32_16x16x32_bf16 %0, %1, %2, 0" : "+v"(d) : "v"(a), "v"(b)); }
template <int T> DI float acc_rd1() { float x; asm volatile("v_accvgpr_read_b32 %0, a%c1" : "=v"(x) : "i"(4 * T + 4)); return x; }
template <int T, int OFF> DI void acc_lds_wr(unsigned a) { asm volatile("ds_write_b128 %0, a[%c1:%c2] offset:%c3" ::"v"(a), "i"(4 * T + 4), "i"(4 * T + 7), "i"(OFF) : "memory"); }
template <int T, int OFF> DI void acc_lds_rd(unsigned a) { asm volatile("ds_read_b128 a[%c1:%c2], %0 offset:%c3" ::"v"(a), "i"(4 * T + 4), "i"(4 * T + 7), "i"(OFF) : "memory"); }
template <int T> DI f32x4 acc_rd() {
  float x0, x1, x2, x3;
  asm volatile("v_accvgpr_read_b32 %0, a%c4\n\tv_accvgpr_read_b32 %1, a%c5\n\tv_accvgpr_read_b32 %2, a%c6\n\tv_accvgpr_read_b32 %3, a%c7"
               : "=v"(x0), "=v"(x1), "=v"(x2), "=v"(x3) : "i"(4 * T + 4), "i"(4 * T + 5), "i"(4 * T + 6), "i"(4 * T + 7));
  return f32x4{x0, x1, x2, x3};
}
template <int T> DI void acc_wr(f32x4 v) {
  asm volatile("v_accvgpr_write_b32 a%c4, %0\n\tv_accvgpr_write_b32 a%c5, %1\n\tv_accvgpr_write_b32 a%c6, %2\n\tv_accvgpr_write_b32 a%c7, %3"
               ::"v"(v[0]), "v"(v[1]), "v"(v[2]), "v"(v[3]), "i"(4 * T + 4), "i"(4 * T + 5), "i"(4 * T + 6), "i"(4 * T + 7));
}
template <int T> DI void acc_zero() {
  asm volatile("v_accvgpr_write_b32 a%c0, 0\n\tv_accvgpr_write_b32 a%c1, 0\n\tv_accvgpr_write_b32 a%c2, 0\n\tv_accvgpr_write_b32 a%c3, 0"
               ::"i"(4 * T + 4), "i"(4 * T + 5), "i"(4 * T + 6), "i"(4 * T + 7));
}
#define NOPS(n) asm volatile("s_nop " #n)
template <int V> using IC = std::integral_constant<int, V>;

#define HQ 10
#define HO 8
#define KROTM 15u
#define FORCE_SAFE false
constexpr const char* ATT_FAST[7] = {"m0123p4567CDabcdp", "4tt5tt6tt7ttCptDt", "ttt0t1tt2345p67CD", "m0p12p34B5p67pCDm", "p01p23A45p67.pCDm", "p0123p4567CDpab.c", "3kk4kk5kk6kk7pCD."};
constexpr const char* ATT_SAFE[7] = {"xyzmw01234567CDpp", "xyz.w01234567CD..", "xyzpw01234567CDpm", "xyzpw01234567CDpp", "xyzpw01234567CDpp", "xyzpw01234567CDpp", "xyzpw01234567CDpp"};
struct AttGap { int ex, cv, tr, kr, pc, m0, bk; char c; };
constexpr AttGap att_gap(bool safe, int i, int g) {
  const char* const* tb = safe ? ATT_SAFE : ATT_FAST;
  AttGap r{-1, 0, -1, -1, -1, -1, 0, tb[i][g]};
  int nt = 0, nk = 0, np = 0;
  for (int k = 0; k < 17 * i + g; ++k) { const char c = tb[k / 17][k % 17]; nt += c == 't'; nk += c == 'k'; np += c == 'p'; }
  const char c = r.c;
  if (c >= '0' && c <= '7') r.ex = c - '0';
  if (c >= 'a' && c <= 'd') r.ex = 8 + c - 'a';
  if (c == 'C') r.cv = 3;
  if (c == 'D') r.cv = 12;
  if (c == 't') r.tr = nt;
  if (c == 'k') r.kr = nk;
  if (c == 'p') r.pc = np;
  if (c == 'm') { r.m0 = np; if (np == 0) r.bk = 1; }
  if (c == 'B') r.bk = 2;
  if (c == 'A') r.bk = 3;
  if (safe) {
    if (i == 1 && g >= 8) r.tr = g - 8;
    if (i == 2 && g < 7) r.tr = g + 9;
    if (i == 6 && g < 8) r.kr = g;
    if (i == 4 && g == 14) r.m0 = 8;
    if (i == 5 && g == 15) r.m0 = 12;
    if (i == 3 && g == 4) r.bk = 2;
    if (i == 1 && g == 3) r.bk = 3;
  }
  return r;
}
constexpr int att_pieces_before(bool safe, int i, int g) { int n = 0; for (int k = 0; k < 17 * i + g; ++k) n += att_gap(safe, k / 17, k % 17).pc >= 0; return n; }
static_assert(att_pieces_before(true, 7, 0) == 16 && att_pieces_before(false, 7, 0) == 16, "pieces");

#define NHX 8
DI void map_block(unsigned g, unsigned& b, unsigned& hg, unsigned& sh) {
  constexpr unsigned HG = NHX / 4, NXH = 32 / NHX, NBX = 32 / HG;
  const unsigned xcd = g & 7u, sl = g >> 3, j = sl % HG, bi = sl / HG;
  b = (xcd / NXH) * NBX + bi; hg = (xcd % NXH) * HG + j;
  sh = ((bi * 10u) / NBX + (j * 10u) / HG) % 10u;
}

__global__ __launch_bounds__(256, 1) void fused_k(const void* __restrict__ Xp, const void* __restrict__ Kp, const void* __restrict__ Vp,
                                                  const void* __restrict__ Wq, const void* __restrict__ Wo, void* __restrict__ Pws,
                                                  void* __restrict__ Out) {
  extern __shared__ __attribute__((aligned(16))) char lds[];
  const unsigned ldsb = (unsigned)(size_t)lds;
  const unsigned l = threadIdx.x & 63u, w = rfl(threadIdx.x >> 6);
  const bool early = w < 2u;
  unsigned b, hg, rot;
  map_block(blockIdx.x, b, hg, rot);
  const unsigned h = 4u * hg + w;
  const unsigned g4 = l >> 4, q15 = l & 15u, q4 = (l >> 2) & 3u, p4 = l & 3u;
  DBGX(unsigned seg[8]; unsigned segr[8];)
  STAMP(0);
  {
    u64 pc; asm volatile("s_getpc_b64 %0" : "=s"(pc));
    srd_t sC = __builtin_amdgcn_make_buffer_rsrc((void*)(pc & ~255ull), (short)0, CODE_BYTES, 0x00020000);
    setm0(ldsb + w * REG + 32768u); NOPS(0);
    asm volatile("buffer_load_dword %0, %1, 0 offen lds" ::"v"((((blockIdx.x >> 3) * 4u + w) * 4u + (l & 3u)) * 128u), "s"(sC) : "memory");
  }
  u64* const gfl = (u64*)Pws + b * 64u;
  u32x2 epv;
  asm volatile("global_load_dwordx2 %0, %1, %2 offset:256 sc1" : "=v"(epv) : "v"(0u), "s"(gfl) : "memory");
  srd_t sK = __builtin_amdgcn_make_buffer_rsrc((char*)Kp - 4096, (short)0, (int)(KV_BYTES + 4096u), 0x00020000);
  srd_t sV = __builtin_amdgcn_make_buffer_rsrc((char*)Vp - 4096, (short)0, (int)(KV_BYTES + 4096u), 0x00020000);
  srd_t sX = __builtin_amdgcn_make_buffer_rsrc((char*)Xp - 4096, (short)0, (int)(X_BYTES + 4096u), 0x00020000);
  srd_t sW = __builtin_amdgcn_make_buffer_rsrc((char*)Wq - 4096, (short)0, (int)(W_BYTES + 4096u), 0x00020000);
  const unsigned ring = ldsb + w * REG;
  const unsigned fl = ldsb + FLAGS;
  const unsigned drow = (l >> 2) & 7u, dsub = l >> 5;
  unsigned kh = (blockIdx.x * 4u + w) * 2654435761u; kh ^= kh >> 15; kh *= 2246822519u; kh ^= kh >> 13;
  const unsigned kvb = (b * 32u + h) * 131072u, krot = (kh & KROTM) * 8192u;
  auto kvs = [&](unsigned s) { return kvb + ((krot + s * 8192u) & 0x1ffffu); };
  unsigned vk[4], vv[2];
#pragma unroll
  for (int i = 0; i < 4; ++i) vk[i] = drow * 256u + 64u * dsub + 16u * (p4 ^ (i & 2u)) + ((i & 1) ? 3200u : 4096u);
#pragma unroll
  for (int i = 0; i < 2; ++i) vv[i] = drow * 256u + 64u * dsub + 16u * (p4 ^ (2u * (drow >> 2))) + (i ? 3200u : 4096u);
  auto chunk = [&](unsigned i) { unsigned c = i + rot; return c >= 10u ? c - 10u : c; };
  auto xoff = [&](unsigned rx, int i) { return rx < 112u ? (b * 112u + rx) * 1280u + 64u * dsub + 16u * (p4 ^ (2u * (i & 1))) + 4096u - 1024u * i : NONE; };
  unsigned bx = ldsb + 1024u * (q15 >> 3) + 64u * (q15 & 7u) + 16u * (g4 ^ (2u * (q15 >> 3)));
  OPAQUE(bx);
  auto xfrag = [&](unsigned a0, unsigned a1, int q) { return lds_b128((q < 4 ? a0 : a1) + ((q >> 1) & 1) * REG + (q & 1) * 2048u); };
  unsigned vw[4];
#pragma unroll
  for (int i = 0; i < 4; ++i) vw[i] = (8u * (i >> 1) + drow) * 8192u + 128u * (i & 1) + 64u * dsub + 16u * p4 + h * 256u + 4096u - 1024u * i;

  asm volatile("" ::: "a255");
  bf16x8 qf[4][7];
  u64 ep;
  const unsigned hb = ldsb + (w & 1u) * REG + 24576u + l * 16u;
  {
    unsigned v1[4], vx[4];
#pragma unroll
    for (int i = 0; i < 4; ++i) {
      v1[i] = (8u * i + drow) * 8192u + 64u * dsub + 16u * p4 + (4u * hg + (w & 1u)) * 256u + (w >> 1) * 128u + 4096u - 1024u * i;
      vx[i] = xoff(32u * w + 8u * i + drow, i);
    }
    const unsigned mx0 = ring + 24576u;
    auto allW = [&](unsigned s) { setm0(ring + (((s >> 1) + 2u) % 3u) * 8192u + (s & 1u) * 4096u); NOPS(0);
      sfor<4>([&](auto P) { dma<0, 1024 * P>(v1[P], sW, (2u * chunk(s >> 1) + (s & 1u)) * 262144u); }); };
    auto allX = [&](unsigned i) { setm0(mx0 + i * 4096u); NOPS(0); sfor<4>([&](auto P) { dma<0, 1024 * P>(vx[P], sX, chunk(i) * 128u); }); };
    unsigned bw = ring + 1024u * g4 + 64u * q4 + 16u * p4;
    OPAQUE(bw);
    f32x4 qa[28];
    allX(0); allW(0); allW(1);
    sfor<28>([&](auto T) { qa[T] = f32x4{}; OPAQUE(qa[T]); });
    allX(1); allW(2); allW(3); allX(2); allW(4); allW(5);
    bf16x8 af[2][4], bf[2][7];
    waitv<28>();
    OPAQUE(epv);
    ep = ((u64)rfl(epv[1]) << 32) | rfl(epv[0]);
    BAR();
    STAMP(1);
#pragma unroll
    for (int d = 0; d < 4; ++d) { const unsigned a = bw + 16384u + 512u * (d >> 1) + 8u * (d & 1); af[0][d] = lds_tr2(a, a + 256u); }
#pragma unroll
    for (int q = 0; q < 7; ++q) bf[0][q] = xfrag(bx + 24576u, bx + 24576u + 2u * REG, q);
    LGKM0();
    unsigned sl = 16384u, xb = 0, cn = chunk(3);
    unsigned aw = bw + 16384u + 4096u, ax = bx + 24576u + 512u, ax2 = ax + 2u * REG;
    OPAQUE(aw); OPAQUE(ax); OPAQUE(ax2);
    srd_t sT = sW; unsigned so = cn * 524288u, hs = 262144u;
#pragma unroll 1
    for (unsigned i = 0; i < 10; ++i) {
      if (i >= 7u) {
        const unsigned j = i - 7u;
        if (early) {
#pragma unroll
          for (int k = 0; k < 4; ++k) v1[k] = j == 1u ? vv[k & 1] : vk[k];
          sT = j == 1u ? sV : sK; so = kvs(j >> 1); hs = 4096u;
        } else {
#pragma unroll
          for (int k = 0; k < 4; ++k) v1[k] = vw[k];
          so = (2u * chunk(j >> 1) + (j & 1u)) * 262144u; hs = 131072u;
        }
      }
      const unsigned sln = sl == 16384u ? 0u : sl + 8192u, xbn = (xb + 4096u) & 12288u;
      sfor<2>([&](auto E) {
        constexpr int e = E;
        bf16x4 lo;
        unsigned awn = 0, axn = 0;
        sfor<28>([&](auto T) {
          constexpr int t = T, d = t / 7, q = t % 7;
          mfv(qa[t], af[e][d], bf[e][q]);
          GAP();
          if constexpr (t == 0) { if constexpr (e == 0) waitv<24>(); else waitv<18>(); }
          if constexpr (t == 1) { if constexpr (e == 1) BAR(); setm0(ring + sl + e * 4096u); }
          if constexpr (t % 3 == 2 && t / 3 < 8) {
            constexpr int j = t / 3, dd = j >> 1; const unsigned a = aw + 512u * (dd >> 1) + 8u * (dd & 1);
            if constexpr ((j & 1) == 0) lo = lds_tr(a); else af[e ^ 1][dd] = cat(lo, lds_tr(a + 256u));
          }
          if constexpr (t % 3 == 0 && t >= 3 && t <= 21) bf[e ^ 1][t / 3 - 1] = xfrag(ax, ax2, t / 3 - 1);
          if constexpr (t == 4 || t == 7 || t == 13 || t == 16) { constexpr int p = t == 4 ? 0 : t == 7 ? 1 : t == 13 ? 2 : 3; dma<0, 1024 * p>(v1[p], sT, so); }
          if constexpr (t == 19) setm0(mx0 + ((xb + 12288u) & 12288u));
          if constexpr (t == 22 || t == 25) { constexpr int p = 2 * e + (t == 25); if (i < 9u) dma<0, 1024 * p>(vx[p], sX, cn * 128u); }
          if constexpr (t == 10) { awn = e == 0 ? bw + sln : aw + 4096u; OPAQUE(awn); }
          if constexpr (t == 24) { axn = e == 0 ? bx + 24576u + xbn : ax + 512u; OPAQUE(axn); }
          if constexpr (t == 26) { ax2 = axn + 2u * REG; OPAQUE(ax2); if constexpr (e == 0) { so += hs; SPIN(so); } }
          if constexpr (t == 27) { LGKM0(); aw = awn; ax = axn; }
          GAP();
        });
      });
      sl = sln; xb = xbn; cn = cn == 9u ? 0u : cn + 1u; so = cn * 524288u;
    }
    NOPS(7);
    GAP();
    sfor<14>([&](auto T) { constexpr int k = T / 7, q = T % 7; qf[k][q] = pack2(qa[14 * k + q], qa[14 * k + 7 + q], QSCALE); OPAQUE(qf[k][q]); });
    waitv<8>();
    if (!early) {
      sfor<14>([&](auto T) { *(LAS bf16x8*)(size_t)(hb + (T / 7) * 2u * REG + (T % 7) * 1024u) = qf[T / 7][T % 7]; });
      if (w == 3u && l < 8u) lds_st(fl + l * 4u, 0u);
    }
    LGKM0(); BAR();
  }
  STAMP(2);
  if (early) {
    sfor<14>([&](auto T) { qf[2 + T / 7][T % 7] = lds_b128(hb + (T / 7) * 2u * REG + (T % 7) * 1024u); });
    LGKM0(); BAR();
    sfor<8>([&](auto P) { if constexpr ((P & 3) == 0) { setm0(ring + 24576u + P * 1024u); NOPS(0); } dma<1, 1024 * (P & 3)>(vv[P & 1], sV, kvs(1u) + (P >> 2) * 4096u); });
  } else {
    BAR();
    unsigned vx[8];
#pragma unroll
    for (int i = 0; i < 8; ++i) vx[i] = xoff(64u * (w & 1u) + 8u * i + drow, i & 3);
    const unsigned mx0 = ldsb + (w & 1u) * 2u * REG + 32768u;
    const unsigned myf = fl + 16u + (w & 1u) * 4u, otf = fl + 16u + ((w & 1u) ^ 1u) * 4u;
    unsigned bw = ring + 2048u * g4 + 64u * q4 + 16u * p4;
    OPAQUE(bw);
    sfor<8>([&](auto P) { if constexpr ((P & 3) == 0) { setm0(ring + 24576u + P * 1024u); NOPS(0); } dma<0, 1024 * (P & 3)>(vw[P & 3], sW, (2u * chunk(1) + 1u) * 262144u + (P >> 2) * 131072u); });
    sfor<56>([&](auto T) { acc_zero<T>(); });
    bf16x8 af[2][8], bf[2][7];
#pragma unroll
    for (int d = 0; d < 8; ++d) { const unsigned a = bw + 512u * (d >> 1) + 8u * (d & 1); af[0][d] = lds_tr2(a, a + 256u); if (d < 7) bf[0][d] = xfrag(bx + 32768u, bx + 32768u + 2u * REG, d); }
    LGKM0();
    unsigned par = 0, cn = chunk(2), rn = 1;
    unsigned aw = bw + 8192u, ax = bx + 32768u + 512u, ax2 = ax + 2u * REG, rv = 1;
    OPAQUE(aw); OPAQUE(ax); OPAQUE(ax2); OPAQUE(rv);
    auto round = [&](auto TAIL) {
      constexpr bool tail = TAIL;
      unsigned so = tail ? kvs(par) : cn * 524288u, so2 = 0;
      sfor<2>([&](auto E) {
        constexpr int e = E;
        bf16x4 lo;
        unsigned awn = 0, axn = 0, pv = 0; u64 bad = 0;
        sfor<56>([&](auto T) {
          constexpr int t = T, d = t / 7, q = t % 7;
          mfa<t>(af[e][d], bf[e][q]);
          if constexpr (t == 0) { if constexpr (e == 0) waitv<16>(); else waitv<8>(); }
          if constexpr (e == 1) {
            if constexpr (t == 2) lds_st(myf, rv);
            if constexpr (t == 8) pv = lds_ld(otf);
            if constexpr (t == 14) { bad = __builtin_amdgcn_ballot_w64(pv < rn); SPIN(bad); }
            if constexpr (t == 17) { GAP(); if (__builtin_expect(bad != 0ull, 0)) wait_ge(otf, rn); asm volatile("" ::: "memory"); }
          }
          if constexpr (t % 3 == 1 && t / 3 < 16) {
            constexpr int j = t / 3, dd = j >> 1; const unsigned a = aw + 512u * (dd >> 1) + 8u * (dd & 1);
            if constexpr ((j & 1) == 0) lo = lds_tr(a); else af[e ^ 1][dd] = cat(lo, lds_tr(a + 256u));
          }
          if constexpr (t % 3 == 2 && t >= 20 && t <= 44 && t != 26 && t != 38) { constexpr int qq = (t - 20) / 3 - (t > 26) - (t > 38); bf[e ^ 1][qq] = xfrag(ax, ax2, qq); }
          constexpr int np = (e == 1 && !tail) ? 16 : 8;
          constexpr auto gap_of = [](int p) { return 3 + 3 * ((p * 16) / np); };
          sfor<np>([&](auto P) {
            constexpr int p = P;
            if constexpr ((p & 3) == 0 && t == gap_of(p) - 1) setm0(p >= 8 ? mx0 + par * 4096u + ((p - 8) >> 2) * REG : ring + par * 16384u + e * 8192u + p * 1024u);
            if constexpr (t == gap_of(p)) {
              if constexpr (HQ < 63) waitv<HQ - 1>();
              if constexpr (p >= 14) { if (w == 2u) dma<0, 1024 * (p & 3)>(vx[p & 7], sX, cn * 128u); }
              else if constexpr (p >= 8) dma<0, 1024 * (p & 3)>(vx[p & 7], sX, cn * 128u);
              else if constexpr (!tail) dma<0, 1024 * (p & 3)>(vw[p & 3], sW, p < 4 ? so : so2);
              else if constexpr (e == 0) dma<1, 1024 * (p & 3)>(vk[p & 3], sK, p < 4 ? so : so2);
              else dma<1, 1024 * (p & 3)>(vv[p & 1], sV, p < 4 ? so : so2);
            }
          });
          if constexpr (t == 5) { so2 = so + (tail ? 4096u : 131072u); SPIN(so2); }
          if constexpr (t == 47) { awn = e == 0 ? bw + (par ^ 1u) * 16384u : aw + 8192u; OPAQUE(awn); }
          if constexpr (t == 50) { axn = e == 0 ? bx + 32768u + (par ^ 1u) * 4096u : ax + 512u; OPAQUE(axn); }
          if constexpr (t == 52) { ax2 = axn + 2u * REG; OPAQUE(ax2); }
          if constexpr (t == 53 && e == 1) { rv += 1u; OPAQUE(rv); }
          if constexpr (t == 55) { LGKM0(); aw = awn; ax = axn; if constexpr (e == 0 && !tail) { so += 262144u; SPIN(so); } }
          GAP();
        });
      });
      par ^= 1u; cn = cn == 9u ? 0u : cn + 1u; rn += 1u;
    };
#pragma unroll 1
    for (unsigned i = 0; i < 8; ++i) round(std::false_type{});
#pragma unroll 1
    for (unsigned i = 8; i < 10; ++i) round(std::true_type{});
    lds_st(myf, 11u);
    NOPS(7);
    sfor<28>([&](auto T) { constexpr int k = T / 7, q = T % 7; qf[k][q] = pack2(acc_rd<14 * k + q>(), acc_rd<14 * k + 7 + q>(), QSCALE); OPAQUE(qf[k][q]); });
  }
  STAMP(3);
  float lsum[7];
  {
    bf16x8 ones = __builtin_bit_cast(bf16x8, u32x4{0x3f803f80u, 0x3f803f80u, 0x3f803f80u, 0x3f803f80u});
    OPAQUE(ones);
    unsigned nsafe = FORCE_SAFE ? 16u : 1u;
#pragma unroll 1
    for (;;) {
      const unsigned l = lane_id(), g4 = l >> 4, q15 = l & 15u, q4 = (l >> 2) & 3u, p4 = l & 3u;
      unsigned ak = ring + 2048u * (q15 >> 3) + 64u * (q15 & 7u) + 16u * (g4 ^ (2u * (q15 >> 3)));
      unsigned av = ring + 8192u + 2048u * (g4 >> 1) + 64u * (4u * (g4 & 1u) + q4) + 16u * ((p4 >> 1) ^ (2u * (g4 & 1u))) + 8u * (p4 & 1u), av1 = av ^ 32u;
      OPAQUE(ak); OPAQUE(av); OPAQUE(av1);
      bf16x8 kf[2][4], vf[8], pp;
      f32x4 sp0, sp1, nm[7];
      waitv<24>();
#pragma unroll
      for (int e = 0; e < 8; ++e) kf[e >> 2][e & 3] = lds_b128(ak + 4096u * (e >> 2) + 512u * (e & 3));
      ak += 16384u;
      auto check = [&](auto Q, u64 big, float mx) {
        constexpr int q = Q;
        if (__builtin_expect(big != 0ull, 0)) {
          const float d = fmaxf(col_max(mx), 0.f), al = __builtin_amdgcn_exp2f(-d);
          NOPS(7);
          sfor<9>([&](auto T) { acc_wr<7 * T + q>(acc_rd<7 * T + q>() * al); });
          nm[q] -= d; sp0 -= d; sp1 -= d;
          NOPS(1);
        }
      };
      auto setref = [&](auto Q, float mx) { constexpr int q = Q; const float d = col_max(mx); nm[q] = f32x4{-d, -d, -d, -d}; OPAQUE(nm[q]); sp0 -= d; sp1 -= d; };
      unsigned so = kvs(2u), mk = ring, dl = 16384u;
      auto step = [&](unsigned s, auto MODE) {
        constexpr bool safe = MODE != 0, first = MODE == 2;
        const int nrec = s < 14u ? (int)(KV_BYTES + 4096u) : 0;
        srd_t sKs = __builtin_amdgcn_make_buffer_rsrc((char*)Kp - 4096, (short)0, nrec, 0x00020000);
        srd_t sVs = __builtin_amdgcn_make_buffer_rsrc((char*)Vp - 4096, (short)0, nrec, 0x00020000);
        unsigned so2 = 0; bf16x4 lo;
        sfor<7>([&](auto I) {
          constexpr int i = I, qs = (i + 6) % 7, qp = (i + 5) % 7;
          f32x4 s0, s1; float mx; u64 big; u32x4 pn;
          sfor<17>([&](auto G) {
            constexpr int g = G;
            constexpr AttGap ag = att_gap(safe, i, g);
            constexpr bool rev = !safe && i == 1;
            constexpr int m = rev ? (g < 9 ? g + 8 : g - 9) : g;
            if constexpr (g == 0 && i == 6) waitv<8 + att_pieces_before(safe, 6, 0)>();
            if constexpr (g == (safe ? 8 : 0) && i == 1) waitv<16 + att_pieces_before(safe, 1, g)>();
            if constexpr (m < 2 && first) (m ? s1 : s0) = mfv00(kf[m][0], qf[0][i]);
            else if constexpr (m < 2) (m ? s1 : s0) = mfv0(kf[m][0], qf[0][i], nm[i]);
            else if constexpr (m < 8) mfv((m & 1) ? s1 : s0, kf[m & 1][m >> 1], qf[m >> 1][i]);
            else if constexpr (first && i < 2) acc_zero<7 * (m - 8) + qp>();
            else if constexpr (first) mfa0<7 * (m - 8) + qp>(m < 16 ? vf[(m - 8) & 7] : ones, pp);
            else mfa<7 * (m - 8) + qp>(m < 16 ? vf[(m - 8) & 7] : ones, pp);
            GAP();
            if constexpr (ag.kr >= 0) kf[ag.kr & 1][ag.kr >> 1] = lds_b128(ak + 4096u * (ag.kr & 1) + 512u * (ag.kr >> 1));
            if constexpr (ag.tr >= 0) { constexpr int d = ag.tr >> 1; const unsigned a = ((d & 1) ? av1 : av) + 512u * (d >> 1);
              if constexpr ((ag.tr & 1) == 0) lo = lds_tr(a); else vf[d] = cat(lo, lds_tr(a + 4096u)); }
            if constexpr (!(first && i == 0)) {
              if constexpr (ag.c == 'x') mx = max5(sp0[0], sp0[1], sp0[2], sp0[3], sp1[0]);
              if constexpr (ag.c == 'y') mx = max5(mx, sp1[1], sp1[2], sp1[3], sp1[3]);
              if constexpr (ag.c == 'z' && first) setref(IC<qs>{}, mx);
              if constexpr (ag.c == 'z' && !first) { big = __builtin_amdgcn_ballot_w64(mx > THR); SPIN(big); }
              if constexpr (ag.c == 'w' && !first) check(IC<qs>{}, big, mx);
              if constexpr (ag.ex >= 0 && ag.ex < 4) { sp0[ag.ex] = __builtin_amdgcn_exp2f(sp0[ag.ex]); OPAQUE(sp0[ag.ex]); }
              if constexpr (ag.ex >= 4 && ag.ex < 8) { sp1[ag.ex - 4] = __builtin_amdgcn_exp2f(sp1[ag.ex - 4]); OPAQUE(sp1[ag.ex - 4]); }
              if constexpr (ag.ex >= 8) { s0[ag.ex - 8] = __builtin_amdgcn_exp2f(s0[ag.ex - 8]); OPAQUE(s0[ag.ex - 8]); }
              if constexpr (ag.cv & 1) { pn[0] = cvtpk(sp0[0], sp0[1]); OPAQUE(pn[0]); }
              if constexpr (ag.cv & 2) { pn[1] = cvtpk(sp0[2], sp0[3]); OPAQUE(pn[1]); }
              if constexpr (ag.cv & 4) { pn[2] = cvtpk(sp1[0], sp1[1]); OPAQUE(pn[2]); }
              if constexpr (ag.cv & 8) { pn[3] = cvtpk(sp1[2], sp1[3]); OPAQUE(pn[3]); }
            }
            if constexpr (ag.pc >= 0) { constexpr int p = ag.pc;
              if constexpr (p < 8) dma<1, 1024 * (p & 3)>(vk[p & 3], sKs, p < 4 ? so : so2); else dma<1, 1024 * (p & 3)>(vv[p & 1], sVs, p < 12 ? so : so2); }
            if constexpr (ag.m0 >= 0) setm0(mk + ag.m0 * 1024u);
            if constexpr (ag.bk == 1) { so2 = so + 4096u; SPIN(so2); }
            if constexpr (ag.bk == 2) { av += dl; OPAQUE(av); av1 += dl; OPAQUE(av1); }
            if constexpr (ag.bk == 3) { ak += dl; OPAQUE(ak); }
            if constexpr (i == 6 && g == 16) LGKM0();
            GAP();
          });
          if constexpr (!(first && i == 0)) pp = __builtin_bit_cast(bf16x8, pn);
          sp0 = s0; sp1 = s1;
        });
        so = kvb | ((so + 8192u) & 0x1ffffu); mk += dl; dl = 0u - dl;
      };
      ak -= 16384u;
      step(0u, IC<2>{});
      { float mx = max5(sp0[0], sp0[1], sp0[2], sp0[3], sp1[0]); mx = max5(mx, sp1[1], sp1[2], sp1[3], sp1[3]); setref(IC<6>{}, mx); GAP(); }
      unsigned s = 1;
#pragma unroll 1
      for (; s < nsafe; ++s) step(s, IC<1>{});
#pragma unroll 1
      for (; s < 16u; ++s) step(s, IC<0>{});
      { float mx = max5(sp0[0], sp0[1], sp0[2], sp0[3], sp1[0]); mx = max5(mx, sp1[1], sp1[2], sp1[3], sp1[3]);
        check(IC<6>{}, __builtin_amdgcn_ballot_w64(mx > THR), mx); }
      NOPS(1);
      sfor<9>([&](auto D) {
        mfa<7 * D + 5>(D < 8 ? vf[D & 7] : ones, pp);
        GAP();
        constexpr int d = D;
        if constexpr (d < 4) { sp0[d] = __builtin_amdgcn_exp2f(sp0[d]); OPAQUE(sp0[d]); } else if constexpr (d < 8) { sp1[d - 4] = __builtin_amdgcn_exp2f(sp1[d - 4]); OPAQUE(sp1[d - 4]); }
        GAP();
      });
      pp = pack2(sp0, sp1, 1.f); OPAQUE(pp);
      NOPS(1);
      mfa<62>(ones, pp);
      sfor<8>([&](auto D) { mfa<7 * D + 6>(vf[D], pp); });
      bool bad = false;
      sfor<7>([&](auto Q) { lsum[Q] = acc_rd1<56 + Q>(); bad |= !(lsum[Q] < 1.0995116e12f); });
      if (__builtin_expect(nsafe == 16u || __builtin_amdgcn_ballot_w64(bad) == 0ull, 1)) break;
      nsafe = 16u;
      waitv<0>();
      sfor<32>([&](auto P) { constexpr int p = P; if constexpr ((p & 3) == 0) { setm0(ring + p * 1024u); NOPS(0); }
        if constexpr ((p >> 3) & 1) dma<1, 1024 * (p & 3)>(vv[p & 1], sV, kvs(p >> 4) + ((p >> 2) & 1) * 4096u);
        else dma<1, 1024 * (p & 3)>(vk[p & 3], sK, kvs(p >> 4) + ((p >> 2) & 1) * 4096u); });
    }
  }
  STAMP(4);
  {
    const unsigned l = lane_id(), g4 = l >> 4, q15 = l & 15u, q4 = (l >> 2) & 3u, p4 = l & 3u, drow = (l >> 2) & 7u;
    srd_t sO = __builtin_amdgcn_make_buffer_rsrc((char*)Wo - 4096, (short)0, (int)(W_BYTES + 4096u), 0x00020000);
    srd_t sP = __builtin_amdgcn_make_buffer_rsrc((char*)Pws + F_BYTES, (short)0, (int)P_BYTES, 0x00020000);
    unsigned vo[4];
#pragma unroll
    for (int i = 0; i < 4; ++i) vo[i] = (hg * 512u + 8u * i + drow) * 1280u + (w & 1u) * 640u + 64u * (l >> 5) + 16u * p4 + 4096u - 1024u * i;
    const unsigned slot0 = ring + 28672u;
    const unsigned rot8 = (rot * 13u) >> 4, sg0 = early ? 0u : 8u;
    auto cchunk = [&](unsigned sg) { return (unsigned)(0x1023443210ull >> (4u * sg)) & 7u; };
    auto sbase = [&](unsigned sg) { return ((0x7e0u >> sg) & 1u) * 327680u + cchunk(sg) * 128u; };
    auto allO = [&](unsigned u) { setm0(slot0 + (u % 3u) * 4096u); NOPS(0); sfor<4>([&](auto P) { dma<0, 1024 * P>(vo[P], sO, sbase(sg0) + ((u + rot8) & 7u) * 40960u); }); };
    waitv<0>();
    wait_ge(fl + 16u + (l & 1u) * 4u, 11u);
    allO(0); allO(1);
    sfor<7>([&](auto Q) {
      constexpr int q = Q;
      const float inv = __builtin_amdgcn_rcpf(lsum[q]);
      sfor<4>([&](auto K) { constexpr int k = K; *(LAS bf16x8*)(size_t)(ring + l * 16u + (k * 7 + q) * 1024u) = pack2(acc_rd<14 * k + q>(), acc_rd<14 * k + 7 + q>(), inv); });
    });
    unsigned ba = slot0 + 1024u * (g4 >> 1) + 64u * (4u * (g4 & 1u) + q4) + 16u * p4, bb = ldsb + l * 16u;
    OPAQUE(ba); OPAQUE(bb);
    bf16x8 af[2][4], bf[2][7];
    if (early) { lds_st(fl + w * 4u, 1u); wait_ge(fl + (l & 1u) * 4u, 1u); }
    else { DBGX(STAMP(5);) LGKM0(); BAR(); DBGX(STAMP(6);) }
    allO(2);
    waitv<8>();
#pragma unroll
    for (int n = 0; n < 4; ++n) { const unsigned a = ba + 8u * (n & 1) + 512u * (n >> 1); af[0][n] = lds_tr2(a, a + 2048u); }
    LGKM0(); asm volatile("" ::: "memory");
    allO(3);
    unsigned k1 = (1u + rot8) & 7u, o3 = ((4u + rot8) & 7u) * 40960u, sl = 4096u;
    unsigned aa = ba + 4096u;
    OPAQUE(aa);
    unsigned bs = bb; bool pfw = false;
    auto firstB = [&]() { const unsigned ab = bs + rot8 * 7168u + (rot8 & 4u) * 3072u;
#pragma unroll
      for (int q = 0; q < 7; ++q) bf[0][q] = lds_b128(ab + 1024u * q); };
    f32x4 oa[28];
    sfor<28>([&](auto T) { f32x4 u; asm volatile("" : "=v"(u)); oa[T] = u; });
    auto unpack = [&](auto R) {
      const unsigned ll = lane_id(), one = (ll >> 4) == ((ll >> 2) & 3u) ? (ll & 1u ? 0x3f800000u : 0x00003f80u) : 0u, lo = (ll & 2u) ? 0u : one, hi = (ll & 2u) ? one : 0u;
      bf16x8 sel[2] = {__builtin_bit_cast(bf16x8, u32x4{lo, hi, 0u, 0u}), __builtin_bit_cast(bf16x8, u32x4{0u, 0u, lo, hi})};
      OPAQUE(sel[0]); OPAQUE(sel[1]); NOPS(1);
      sfor<28>([&](auto T) { mfv0a<R + T / 2>(oa[T], sel[T & 1]); });
    };
    auto packed = [&](auto T) { return pack2(oa[2 * T], oa[2 * T + 1], 1.f); };
    auto gput = [&](unsigned idx, u64 val) {
      asm volatile("s_mov_b64 exec, 1\n\tglobal_store_dwordx2 %0, %1, %2 sc1\n\ts_mov_b64 exec, -1" ::"v"(idx * 8u), "v"(val), "s"(gfl) : "memory"); };
    auto pairU = [&](unsigned i, unsigned sc, unsigned sn, auto ZERO, auto AFTER, auto THROT) {
      {
        sfor<2>([&](auto E) {
          constexpr int e = E;
          unsigned ab = 0, ab2 = 0, so = 0; bf16x4 lo;
          sfor<28>([&](auto T) {
            constexpr int t = T, q = t / 4, n = t % 4;
            if constexpr (ZERO && e == 0) mfv00t(oa[t], af[e][n], bf[e][q]); else mfv(oa[t], af[e][n], bf[e][q]);
            GAP();
            if constexpr (t == 0) {
              if constexpr (AFTER) asm volatile("s_cmp_lt_u32 %0, %1\n\ts_cbranch_scc1 1f\n\ts_waitcnt vmcnt(8)\n1:\n\ts_waitcnt vmcnt(22)" ::"s"(i), "i"(2 - e) : "scc", "memory"); else waitv<8>();
            }
            if constexpr (t >= 1 && t <= 8) { constexpr int j = t - 1; const unsigned a = aa + 4u * (j & 2) + 512u * (j >> 2);
              if constexpr ((j & 1) == 0) lo = lds_tr(a); else af[e ^ 1][j >> 1] = cat(lo, lds_tr(a + 2048u)); }
            if constexpr (t == 9) { ab2 = (k1 & 4u) * 3072u; SPIN(ab2); }
            if constexpr (t == 10) { ab = k1 * 7168u + ab2; SPIN(ab); }
            if constexpr (t == 11) { ab += bs; OPAQUE(ab); setm0(slot0 + sl); }
            if constexpr (t == 12) { so = (i >> 1) * sn; SPIN(so); }
            if constexpr (t == 13) { so += o3 + sc; SPIN(so); LGKM0(); asm volatile("" ::: "memory"); }
            if constexpr ((t == 14 || t == 17 || t == 20 || t == 23) && HO < 63 && THROT) waitv<HO - 1>();
            if constexpr (t == 14 || t == 17 || t == 20 || t == 23) dma<0, 1024 * ((t - 14) / 3)>(vo[(t - 14) / 3], sO, so);
            if constexpr (t >= 15 && t <= 24 && (t - 14) % 3 != 0) { constexpr int qq = (t - 15) - (t - 14) / 3; bf[e ^ 1][qq] = lds_b128(ab + 1024u * qq); }
            if constexpr (t == 25) { sl = sl == 8192u ? 0u : sl + 4096u; SPIN(sl); }
            if constexpr (t == 26) { aa = ba + sl; OPAQUE(aa); k1 = (k1 + 1u) & 7u; SPIN(k1); }
            if constexpr (t == 27) { o3 = o3 == 286720u ? 0u : o3 + 40960u; SPIN(o3); }
            GAP();
          });
        });
      }
    };
    if (early) {
      firstB(); LGKM0();
      pfw = true;
#pragma unroll 1
      for (unsigned sg = 0; sg < 5u; ++sg) {
        const unsigned sc = sbase(sg), sn = sbase(sg + 1u) - sc;
        pairU(0u, sc, sn, std::true_type{}, std::false_type{}, std::true_type{});
#pragma unroll 1
        for (unsigned i = 1; i < 4; ++i) pairU(i, sc, sn, std::false_type{}, std::false_type{}, std::true_type{});
        NOPS(7); GAP();
        if (pfw && sg == 2u) {
          constexpr unsigned NBX = 128 / NHX, PER = 2560u / NBX / 2u;
          unsigned dummy = 0;
#pragma unroll
          for (unsigned k = 0; k < (PER + 63u) / 64u; ++k) {
            const unsigned li = k * 64u + lane_id();
            asm volatile("buffer_load_dword %0, %1, %2, 0 offen" : "+v"(dummy) : "v"(li < PER ? (hg * 512u + 256u) * 1280u + (((b % NBX) * 2u + w) * PER + li) * 128u + 4096u : NONE), "s"(sO) : "memory");
          }
          waitv<0>(); asm volatile("" :: "v"(dummy));
        }
        sfor<4>([&](auto C) { if (sg == C) sfor<14>([&](auto T) { acc_wr<14 * C + T>(__builtin_bit_cast(f32x4, packed(T))); }); });
      }
      lds_st(fl + w * 4u, 2u); wait_ge(fl + (lane_id() & 1u) * 4u, 2u);
      const unsigned a = bb + w * REG;
      sfor<28>([&](auto T) { acc_lds_wr<T, T * 1024>(a); });
      DBGX(STAMP(5);)
      asm volatile("s_waitcnt lgkmcnt(0)" ::: "memory"); BAR();
      DBGX(STAMP(6);)
    }
    unsigned w2 = w; SPIN(w2);
    if (w2 >= 2u) {
      const unsigned a = bb + (w & 1u) * REG;
      sfor<28>([&](auto T) { acc_lds_rd<28 + T, T * 1024>(a); });
      asm volatile("s_waitcnt lgkmcnt(0)" ::: "memory");
      unpack(IC<28>{});
    }
    bs = bb + 2u * REG; OPAQUE(bs);
    firstB();
    LGKM0();
    auto lastseg = [&](unsigned sg, auto AFTER) {
      const unsigned sc = sbase(sg), sn = sbase(sg + 1u) - sc;
#pragma unroll 1
      for (unsigned i = 0; i < 4; ++i) {
        pairU(i, sc, sn, std::false_type{}, AFTER, std::false_type{});
      }
      NOPS(7); GAP();
      const unsigned po = ((b * 10u + (w & 1u) * 5u + cchunk(sg)) * 8u + hg) * P_UNIT + lane_id() * 16u;
      sfor<14>([&](auto T) { __builtin_amdgcn_raw_buffer_store_b128(__builtin_bit_cast(u32x4, packed(T)), sP, po + T * 1024u, 0, 16); });
      GAP();
    };
    lastseg(early ? 5u : 8u, std::false_type{});
    unpack(IC<42>{});
    lastseg(early ? 6u : 9u, std::true_type{});
    if (early) {
      unpack(IC<28>{});
      lastseg(7u, std::true_type{});
    }
    waitv<0>();
    DBGX(STAMP(7);)
    BAR();
    const unsigned ll = lane_id();
    if (w == 0u) {
      gput(hg, ep + 1ull);
      unsigned sp = 0;
      while (__builtin_amdgcn_ballot_w64(__hip_atomic_load(gfl + (ll & 7u), __ATOMIC_RELAXED, __HIP_MEMORY_SCOPE_AGENT) != ep + 1ull) != 0ull && ++sp < SPIN_CAP) __builtin_amdgcn_s_sleep(2);
      if (hg == 0u) gput(32u, ep + 1ull);
    }
    BAR();
    srd_t sOut = __builtin_amdgcn_make_buffer_rsrc(Out, (short)0, (int)X_BYTES, 0x00020000);
    u32x4 v[3][2][8];
    unsigned oo[3];
#pragma unroll
    for (int j = 0; j < 3; ++j) {
      const unsigned u = hg + 8u * (w + 4u * j), ch = (u * 37u) >> 8, q = u - 7u * ch;
      const unsigned pi = u < 70u ? (b * 10u + ch) * 8u * P_UNIT + q * 2048u + ll * 16u : NONE;
      oo[j] = u < 70u ? (b * 112u + q * 16u + (ll & 15u)) * 1280u + ch * 128u + (ll >> 4) * 16u : NONE;
      if (j < 2 || w == 0u) {
#pragma unroll
        for (int k = 0; k < 8; ++k) { v[j][0][k] = __builtin_amdgcn_raw_buffer_load_b128(sP, pi + k * P_UNIT, 0, 16); v[j][1][k] = __builtin_amdgcn_raw_buffer_load_b128(sP, pi + k * P_UNIT + 1024u, 0, 16); }
      }
    }
#pragma unroll
    for (int j = 0; j < 3; ++j) {
      if (j < 2 || w == 0u) {
#pragma unroll
        for (int r = 0; r < 2; ++r) {
          u32x4 o;
#pragma unroll
          for (int e = 0; e < 4; ++e) {
            float lo = __uint_as_float(v[j][r][0][e] << 16), hi = __uint_as_float(v[j][r][0][e] & 0xffff0000u);
#pragma unroll
            for (int k = 1; k < 8; ++k) { lo += __uint_as_float(v[j][r][k][e] << 16); hi += __uint_as_float(v[j][r][k][e] & 0xffff0000u); }
            o[e] = cvtpk(lo, hi);
          }
          __builtin_amdgcn_raw_buffer_store_b128(o, sOut, oo[j] + r * 64u, 0, 0);
        }
      }
    }
  }
}

extern "C" void kernel_launch(void* const* d_in, const int* in_sizes, int n_in, void* d_out, int out_size, void* d_ws, size_t ws_size,
                              hipStream_t stream) {
  (void)in_sizes; (void)n_in; (void)out_size; (void)ws_size;
  (void)hipFuncSetAttribute((const void*)fused_k, hipFuncAttributeMaxDynamicSharedMemorySize, (int)LDS_ALL);
  fused_k<<<256, 256, LDS_ALL, stream>>>(d_in[0], d_in[1], d_in[2], d_in[3], d_in[4], d_ws, d_out);
}
```

```cpp
#include <hip/hip_runtime.h>
#include <hip/hip_bf16.h>
#include <utility>

typedef __bf16 bf16x8 __attribute__((ext_vector_type(8)));
typedef __bf16 bf16x4 __attribute__((ext_vector_type(4)));
typedef __bf16 bf16x2 __attribute__((ext_vector_type(2)));
typedef float f32x4 __attribute__((ext_vector_type(4)));
typedef float f32x2 __attribute__((ext_vector_type(2)));
typedef unsigned u32x4 __attribute__((ext_vector_type(4)));
typedef unsigned u32x2 __attribute__((ext_vector_type(2)));
typedef unsigned long long u64;
typedef __amdgpu_buffer_rsrc_t srd_t;
#define LAS __attribute__((address_space(3)))
#define DI __device__ __forceinline__
#define GAP() __builtin_amdgcn_sched_barrier(0)
#define OPAQUE(x) asm volatile("" : "+v"(x))
#define SPIN(x) asm volatile("" : "+s"(x))
#define BAR() do { asm volatile("" ::: "memory"); __builtin_amdgcn_s_barrier(); asm volatile("" ::: "memory"); } while (0)

constexpr unsigned KV_BYTES = 134217728u, X_BYTES = 3584u * 1280u, W_BYTES = 640u * 8192u;
constexpr unsigned P_UNIT = 14336u, P_BYTES = 320u * 8u * P_UNIT;
constexpr unsigned F_BYTES = 32u * 512u;
constexpr int CODE_BYTES = 52608;
constexpr unsigned REG = 40960u, LDS_ALL = 163840u, FLAGS = 3u * REG + 38912u;
constexpr unsigned SPIN_CAP = 1u << 22;
constexpr float QSCALE = 0.12751743082f;
constexpr float THR = 8.0f;
constexpr unsigned NONE = 0x80000000u;

#define STAMP(i)
#define DBGX(x)

DI unsigned lane_id() { unsigned r; asm volatile("v_mbcnt_lo_u32_b32 %0, -1, 0\n\tv_mbcnt_hi_u32_b32 %0, -1, %0" : "=v"(r)); return r; }
DI unsigned rfl(unsigned x) { return __builtin_amdgcn_readfirstlane(x); }
DI unsigned cvtpk(float lo, float hi) { f32x2 v = {lo, hi}; bf16x2 b = __builtin_convertvector(v, bf16x2); return __builtin_bit_cast(unsigned, b); }
template <int N> DI void waitv() { asm volatile("s_waitcnt vmcnt(%c0)" ::"i"(N) : "memory"); }
DI void setm0(unsigned v) { asm volatile("s_mov_b32 m0, %0" ::"s"(v) : "m0", "memory"); }
template <int NT, int OFF> DI void dma(unsigned v, srd_t srd, unsigned soff) {
  if constexpr (NT) asm volatile("buffer_load_dwordx4 %0, %1, %2 offen offset:%c3 nt lds" ::"v"(v), "s"(srd), "s"(soff), "i"(OFF) : "memory");
  else asm volatile("buffer_load_dwordx4 %0, %1, %2 offen offset:%c3 lds" ::"v"(v), "s"(srd), "s"(soff), "i"(OFF) : "memory");
}
#define LGKM0() __builtin_amdgcn_s_waitcnt(0xC07F)
DI bf16x8 lds_b128(unsigned a) { return *(const LAS bf16x8*)(size_t)a; }
DI bf16x4 lds_tr(unsigned a) { return __builtin_amdgcn_ds_read_tr16_b64_v4bf16((LAS bf16x4*)(size_t)a); }
DI bf16x8 cat(bf16x4 lo, bf16x4 hi) { return __builtin_shufflevector(lo, hi, 0, 1, 2, 3, 4, 5, 6, 7); }
DI bf16x8 lds_tr2(unsigned a0, unsigned a1) {
  bf16x4 lo = __builtin_amdgcn_ds_read_tr16_b64_v4bf16((LAS bf16x4*)(size_t)a0);
  bf16x4 hi = __builtin_amdgcn_ds_read_tr16_b64_v4bf16((LAS bf16x4*)(size_t)a1);
  return __builtin_shufflevector(lo, hi, 0, 1, 2, 3, 4, 5, 6, 7);
}
DI bf16x8 pack2(const f32x4& a, const f32x4& b, float s) {
  u32x4 w = {cvtpk(a[0] * s, a[1] * s), cvtpk(a[2] * s, a[3] * s), cvtpk(b[0] * s, b[1] * s), cvtpk(b[2] * s, b[3] * s)};
  return __builtin_bit_cast(bf16x8, w);
}
DI float col_max(float v) {
  auto r = __builtin_amdgcn_permlane16_swap(__float_as_uint(v), __float_as_uint(v), false, false);
  v = fmaxf(__uint_as_float(r[0]), __uint_as_float(r[1]));
  auto s = __builtin_amdgcn_permlane32_swap(__float_as_uint(v), __float_as_uint(v), false, false);
  return fmaxf(__uint_as_float(s[0]), __uint_as_float(s[1]));
}
DI float col_sum(float v) {
  auto r = __builtin_amdgcn_permlane16_swap(__float_as_uint(v), __float_as_uint(v), false, false);
  v = __uint_as_float(r[0]) + __uint_as_float(r[1]);
  auto s = __builtin_amdgcn_permlane32_swap(__float_as_uint(v), __float_as_uint(v), false, false);
  return __uint_as_float(s[0]) + __uint_as_float(s[1]);
}

template <class F, int... Is> DI void sfor_(F&& f, std::integer_sequence<int, Is...>) { (f(std::integral_constant<int, Is>{}), ...); }
template <int N, class F> DI void sfor(F&& f) { sfor_(f, std::make_integer_sequence<int, N>{}); }
template <int T> DI void mfa(bf16x8 a, bf16x8 b) { asm volatile("v_mfma_f32_16x16x32_bf16 a[%c2:%c3], %0, %1, a[%c2:%c3]" ::"v"(a), "v"(b), "i"(4 * T + 4), "i"(4 * T + 7)); }
template <int T> DI void mfa0(bf16x8 a, bf16x8 b) { asm volatile("v_mfma_f32_16x16x32_bf16 a[%c2:%c3], %0, %1, 0" ::"v"(a), "v"(b), "i"(4 * T + 4), "i"(4 * T + 7)); }
DI float max5(float a, float b, float c, float d, float e) { float r; asm volatile("v_max3_f32 %0, %1, %2, %3\n\tv_max3_f32 %0, %0, %4, %5" : "=&v"(r) : "v"(a), "v"(b), "v"(c), "v"(d), "v"(e)); return r; }
template <int T, int R> DI void mfa0a(bf16x8 a) { asm volatile("v_mfma_f32_16x16x32_bf16 a[%c1:%c2], %0, a[%c3:%c4], 0" ::"v"(a), "i"(4 * T + 4), "i"(4 * T + 7), "i"(4 * R + 4), "i"(4 * R + 7)); }
DI unsigned lds_ld(unsigned a) { return *(volatile LAS unsigned*)(size_t)a; }
DI void lds_st(unsigned a, unsigned v) { *(volatile LAS unsigned*)(size_t)a = v; }
DI void wait_ge(unsigned a, unsigned need) {
  unsigned sp = 0;
  while (__builtin_amdgcn_ballot_w64(lds_ld(a) < need) != 0ull && ++sp < SPIN_CAP) __builtin_amdgcn_s_sleep(1);
  asm volatile("" ::: "memory");
}
DI void mfv(f32x4& d, bf16x8 a, bf16x8 b) { asm volatile("v_mfma_f32_16x16x32_bf16 %0, %1, %2, %0" : "+v"(d) : "v"(a), "v"(b)); }
DI f32x4 mfv0(bf16x8 a, bf16x8 b, f32x4 c) { f32x4 d; asm volatile("v_mfma_f32_16x16x32_bf16 %0, %1, %2, %3" : "=&v"(d) : "v"(a), "v"(b), "v"(c)); return d; }
DI f32x4 mfv00(bf16x8 a, bf16x8 b) { f32x4 d; asm volatile("v_mfma_f32_16x16x32_bf16 %0, %1, %2, 0" : "=&v"(d) : "v"(a), "v"(b)); return d; }
template <int R> DI void mfv0a(f32x4& d, bf16x8 a) { asm volatile("v_mfma_f32_16x16x32_bf16 %0, %1, a[%c2:%c3], 0" : "+v"(d) : "v"(a), "i"(4 * R + 4), "i"(4 * R + 7)); }
DI void mfv00t(f32x4& d, bf16x8 a, bf16x8 b) { asm volatile("v_mfma_f32_16x16x32_bf16 %0, %1, %2, 0" : "+v"(d) : "v"(a), "v"(b)); }
template <int T> DI float acc_rd1() { float x; asm volatile("v_accvgpr_read_b32 %0, a%c1" : "=v"(x) : "i"(4 * T + 4)); return x; }
template <int T, int OFF> DI void acc_lds_wr(unsigned a) { asm volatile("ds_write_b128 %0, a[%c1:%c2] offset:%c3" ::"v"(a), "i"(4 * T + 4), "i"(4 * T + 7), "i"(OFF) : "memory"); }
template <int T, int OFF> DI void acc_lds_rd(unsigned a) { asm volatile("ds_read_b128 a[%c1:%c2], %0 offset:%c3" ::"v"(a), "i"(4 * T + 4), "i"(4 * T + 7), "i"(OFF) : "memory"); }
template <int T> DI f32x4 acc_rd() {
  float x0, x1, x2, x3;
  asm volatile("v_accvgpr_read_b32 %0, a%c4\n\tv_accvgpr_read_b32 %1, a%c5\n\tv_accvgpr_read_b32 %2, a%c6\n\tv_accvgpr_read_b32 %3, a%c7"
               : "=v"(x0), "=v"(x1), "=v"(x2), "=v"(x3) : "i"(4 * T + 4), "i"(4 * T + 5), "i"(4 * T + 6), "i"(4 * T + 7));
  return f32x4{x0, x1, x2, x3};
}
template <int T> DI void acc_wr(f32x4 v) {
  asm volatile("v_accvgpr_write_b32 a%c4, %0\n\tv_accvgpr_write_b32 a%c5, %1\n\tv_accvgpr_write_b32 a%c6, %2\n\tv_accvgpr_write_b32 a%c7, %3"
               ::"v"(v[0]), "v"(v[1]), "v"(v[2]), "v"(v[3]), "i"(4 * T + 4), "i"(4 * T + 5), "i"(4 * T + 6), "i"(4 * T + 7));
}
template <int T> DI void acc_zero() {
  asm volatile("v_accvgpr_write_b32 a%c0, 0\n\tv_accvgpr_write_b32 a%c1, 0\n\tv_accvgpr_write_b32 a%c2, 0\n\tv_accvgpr_write_b32 a%c3, 0"
               ::"i"(4 * T + 4), "i"(4 * T + 5), "i"(4 * T + 6), "i"(4 * T + 7));
}
#define NOPS(n) asm volatile("s_nop " #n)
template <int V> using IC = std::integral_constant<int, V>;

#define HQ 10
#define HO 8
#define KROTM 15u
#define FORCE_SAFE false
constexpr const char* ATT_FAST[7] = {"m0123p4567CDabcdp", "4tt5tt6tt7ttCptDt", "ttt0t1tt2345p67CD", "m0p12p34B5p67pCDm", "p01p23A45p67.pCDm", "p0123p4567CDpab.c", "3kk4kk5kk6kk7pCD."};
constexpr const char* ATT_SAFE[7] = {"xyzmw01234567CDpp", "xyz.w01234567CD..", "xyzpw01234567CDpm", "xyzpw01234567CDpp", "xyzpw01234567CDpp", "xyzpw01234567CDpp", "xyzpw01234567CDpp"};
struct AttGap { int ex, cv, tr, kr, pc, m0, bk; char c; };
constexpr AttGap att_gap(bool safe, int i, int g) {
  const char* const* tb = safe ? ATT_SAFE : ATT_FAST;
  AttGap r{-1, 0, -1, -1, -1, -1, 0, tb[i][g]};
  int nt = 0, nk = 0, np = 0;
  for (int k = 0; k < 17 * i + g; ++k) { const char c = tb[k / 17][k % 17]; nt += c == 't'; nk += c == 'k'; np += c == 'p'; }
  const char c = r.c;
  if (c >= '0' && c <= '7') r.ex = c - '0';
  if (c >= 'a' && c <= 'd') r.ex = 8 + c - 'a';
  if (c == 'C') r.cv = 3;
  if (c == 'D') r.cv = 12;
  if (c == 't') r.tr = nt;
  if (c == 'k') r.kr = nk;
  if (c == 'p') r.pc = np;
  if (c == 'm') { r.m0 = np; if (np == 0) r.bk = 1; }
  if (c == 'B') r.bk = 2;
  if (c == 'A') r.bk = 3;
  if (safe) {
    if (i == 1 && g >= 8) r.tr = g - 8;
    if (i == 2 && g < 7) r.tr = g + 9;
    if (i == 6 && g < 8) r.kr = g;
    if (i == 4 && g == 14) r.m0 = 8;
    if (i == 5 && g == 15) r.m0 = 12;
    if (i == 3 && g == 4) r.bk = 2;
    if (i == 1 && g == 3) r.bk = 3;
  }
  return r;
}
constexpr int att_pieces_before(bool safe, int i, int g) { int n = 0; for (int k = 0; k < 17 * i + g; ++k) n += att_gap(safe, k / 17, k % 17).pc >= 0; return n; }
static_assert(att_pieces_before(true, 7, 0) == 16 && att_pieces_before(false, 7, 0) == 16, "pieces");

#define NHX 8
DI void map_block(unsigned g, unsigned& b, unsigned& hg, unsigned& sh) {
  constexpr unsigned HG = NHX / 4, NXH = 32 / NHX, NBX = 32 / HG;
  const unsigned xcd = g & 7u, sl = g >> 3, j = sl % HG, bi = sl / HG;
  b = (xcd / NXH) * NBX + bi; hg = (xcd % NXH) * HG + j;
  sh = ((bi * 10u) / NBX + (j * 10u) / HG) % 10u;
}

__global__ __launch_bounds__(256, 1) void fused_k(const void* __restrict__ Xp, const void* __restrict__ Kp, const void* __restrict__ Vp,
                                                  const void* __restrict__ Wq, const void* __restrict__ Wo, void* __restrict__ Pws,
                                                  void* __restrict__ Out) {
  extern __shared__ __attribute__((aligned(16))) char lds[];
  const unsigned ldsb = (unsigned)(size_t)lds;
  const unsigned l = threadIdx.x & 63u, w = rfl(threadIdx.x >> 6);
  const bool early = w < 2u;
  unsigned b, hg, rot;
  map_block(blockIdx.x, b, hg, rot);
  const unsigned h = 4u * hg + w;
  const unsigned g4 = l >> 4, q15 = l & 15u, q4 = (l >> 2) & 3u, p4 = l & 3u;
  DBGX(unsigned seg[8]; unsigned segr[8];)
  STAMP(0);
  {
    u64 pc; asm volatile("s_getpc_b64 %0" : "=s"(pc));
    srd_t sC = __builtin_amdgcn_make_buffer_rsrc((void*)(pc & ~255ull), (short)0, CODE_BYTES, 0x00020000);
    setm0(ldsb + w * REG + 32768u); NOPS(0);
    asm volatile("buffer_load_dword %0, %1, 0 offen lds" ::"v"((((blockIdx.x >> 3) * 4u + w) * 4u + (l & 3u)) * 128u), "s"(sC) : "memory");
  }
  u64* const gfl = (u64*)Pws + b * 64u;
  u32x2 epv;
  asm volatile("global_load_dwordx2 %0, %1, %2 offset:256 sc1" : "=v"(epv) : "v"(0u), "s"(gfl) : "memory");
  srd_t sK = __builtin_amdgcn_make_buffer_rsrc((char*)Kp - 4096, (short)0, (int)(KV_BYTES + 4096u), 0x00020000);
  srd_t sV = __builtin_amdgcn_make_buffer_rsrc((char*)Vp - 4096, (short)0, (int)(KV_BYTES + 4096u), 0x00020000);
  srd_t sX = __builtin_amdgcn_make_buffer_rsrc((char*)Xp - 4096, (short)0, (int)(X_BYTES + 4096u), 0x00020000);
  srd_t sW = __builtin_amdgcn_make_buffer_rsrc((char*)Wq - 4096, (short)0, (int)(W_BYTES + 4096u), 0x00020000);
  const unsigned ring = ldsb + w * REG;
  const unsigned fl = ldsb + FLAGS;
  const unsigned drow = (l >> 2) & 7u, dsub = l >> 5;
  unsigned kh = (blockIdx.x * 4u + w) * 2654435761u; kh ^= kh >> 15; kh *= 2246822519u; kh ^= kh >> 13;
  const unsigned kvb = (b * 32u + h) * 131072u, krot = (kh & KROTM) * 8192u;
  auto kvs = [&](unsigned s) { return kvb + ((krot + s * 8192u) & 0x1ffffu); };
  unsigned vk[4], vv[2];
#pragma unroll
  for (int i = 0; i < 4; ++i) vk[i] = drow * 256u + 64u * dsub + 16u * (p4 ^ (i & 2u)) + ((i & 1) ? 3200u : 4096u);
#pragma unroll
  for (int i = 0; i < 2; ++i) vv[i] = drow * 256u + 64u * dsub + 16u * (p4 ^ (2u * (drow >> 2))) + (i ? 3200u : 4096u);
  auto chunk = [&](unsigned i) { unsigned c = i + rot; return c >= 10u ? c - 10u : c; };
  auto xoff = [&](unsigned rx, int i) { return rx < 112u ? (b * 112u + rx) * 1280u + 64u * dsub + 16u * (p4 ^ (2u * (i & 1))) + 4096u - 1024u * i : NONE; };
  unsigned bx = ldsb + 1024u * (q15 >> 3) + 64u * (q15 & 7u) + 16u * (g4 ^ (2u * (q15 >> 3)));
  OPAQUE(bx);
  auto xfrag = [&](unsigned a0, unsigned a1, int q) { return lds_b128((q < 4 ? a0 : a1) + ((q >> 1) & 1) * REG + (q & 1) * 2048u); };
  unsigned vw[4];
#pragma unroll
  for (int i = 0; i < 4; ++i) vw[i] = (8u * (i >> 1) + drow) * 8192u + 128u * (i & 1) + 64u * dsub + 16u * p4 + h * 256u + 4096u - 1024u * i;

  asm volatile("" ::: "a255");
  bf16x8 qf[4][7];
  u64 ep;
  const unsigned hb = ldsb + (w & 1u) * REG + 24576u + l * 16u;
  {
    unsigned v1[4], vx[4];
#pragma unroll
    for (int i = 0; i < 4; ++i) {
      v1[i] = (8u * i + drow) * 8192u + 64u * dsub + 16u * p4 + (4u * hg + (w & 1u)) * 256u + (w >> 1) * 128u + 4096u - 1024u * i;
      vx[i] = xoff(32u * w + 8u * i + drow, i);
    }
    const unsigned mx0 = ring + 24576u;
    auto allW = [&](unsigned s) { setm0(ring + (((s >> 1) + 2u) % 3u) * 8192u + (s & 1u) * 4096u); NOPS(0);
      sfor<4>([&](auto P) { dma<0, 1024 * P>(v1[P], sW, (2u * chunk(s >> 1) + (s & 1u)) * 262144u); }); };
    auto allX = [&](unsigned i) { setm0(mx0 + i * 4096u); NOPS(0); sfor<4>([&](auto P) { dma<0, 1024 * P>(vx[P], sX, chunk(i) * 128u); }); };
    unsigned bw = ring + 1024u * g4 + 64u * q4 + 16u * p4;
    OPAQUE(bw);
    f32x4 qa[28];
    allX(0); allW(0); allW(1);
    sfor<28>([&](auto T) { qa[T] = f32x4{}; OPAQUE(qa[T]); });
    allX(1); allW(2); allW(3); allX(2); allW(4); allW(5);
    bf16x8 af[2][4], bf[2][7];
    waitv<28>();
    OPAQUE(epv);
    ep = ((u64)rfl(epv[1]) << 32) | rfl(epv[0]);
    BAR();
    STAMP(1);
#pragma unroll
    for (int d = 0; d < 4; ++d) { const unsigned a = bw + 16384u + 512u * (d >> 1) + 8u * (d & 1); af[0][d] = lds_tr2(a, a + 256u); }
#pragma unroll
    for (int q = 0; q < 7; ++q) bf[0][q] = xfrag(bx + 24576u, bx + 24576u + 2u * REG, q);
    LGKM0();
    unsigned sl = 16384u, xb = 0, cn = chunk(3);
    unsigned aw = bw + 16384u + 4096u, ax = bx + 24576u + 512u, ax2 = ax + 2u * REG;
    OPAQUE(aw); OPAQUE(ax); OPAQUE(ax2);
    srd_t sT = sW; unsigned so = cn * 524288u, hs = 262144u;
#pragma unroll 1
    for (unsigned i = 0; i < 10; ++i) {
      if (i >= 7u) {
        const unsigned j = i - 7u;
        if (early) {
#pragma unroll
          for (int k = 0; k < 4; ++k) v1[k] = j == 1u ? vv[k & 1] : vk[k];
          sT = j == 1u ? sV : sK; so = kvs(j >> 1); hs = 4096u;
        } else {
#pragma unroll
          for (int k = 0; k < 4; ++k) v1[k] = vw[k];
          so = (2u * chunk(j >> 1) + (j & 1u)) * 262144u; hs = 131072u;
        }
      }
      const unsigned sln = sl == 16384u ? 0u : sl + 8192u, xbn = (xb + 4096u) & 12288u;
      sfor<2>([&](auto E) {
        constexpr int e = E;
        bf16x4 lo;
        unsigned awn = 0, axn = 0;
        sfor<28>([&](auto T) {
          constexpr int t = T, d = t / 7, q = t % 7;
          mfv(qa[t], af[e][d], bf[e][q]);
          GAP();
          if constexpr (t == 0) { if constexpr (e == 0) waitv<24>(); else waitv<18>(); }
          if constexpr (t == 1) { if constexpr (e == 1) BAR(); setm0(ring + sl + e * 4096u); }
          if constexpr (t % 3 == 2 && t / 3 < 8) {
            constexpr int j = t / 3, dd = j >> 1; const unsigned a = aw + 512u * (dd >> 1) + 8u * (dd & 1);
            if constexpr ((j & 1) == 0) lo = lds_tr(a); else af[e ^ 1][dd] = cat(lo, lds_tr(a + 256u));
          }
          if constexpr (t % 3 == 0 && t >= 3 && t <= 21) bf[e ^ 1][t / 3 - 1] = xfrag(ax, ax2, t / 3 - 1);
          if constexpr (t == 4 || t == 7 || t == 13 || t == 16) { constexpr int p = t == 4 ? 0 : t == 7 ? 1 : t == 13 ? 2 : 3; dma<0, 1024 * p>(v1[p], sT, so); }
          if constexpr (t == 19) setm0(mx0 + ((xb + 12288u) & 12288u));
          if constexpr (t == 22 || t == 25) { constexpr int p = 2 * e + (t == 25); if (i < 9u) dma<0, 1024 * p>(vx[p], sX, cn * 128u); }
          if constexpr (t == 10) { awn = e == 0 ? bw + sln : aw + 4096u; OPAQUE(awn); }
          if constexpr (t == 24) { axn = e == 0 ? bx + 24576u + xbn : ax + 512u; OPAQUE(axn); }
          if constexpr (t == 26) { ax2 = axn + 2u * REG; OPAQUE(ax2); if constexpr (e == 0) { so += hs; SPIN(so); } }
          if constexpr (t == 27) { LGKM0(); aw = awn; ax = axn; }
          GAP();
        });
      });
      sl = sln; xb = xbn; cn = cn == 9u ? 0u : cn + 1u; so = cn * 524288u;
    }
    NOPS(7);
    GAP();
    sfor<14>([&](auto T) { constexpr int k = T / 7, q = T % 7; qf[k][q] = pack2(qa[14 * k + q], qa[14 * k + 7 + q], QSCALE); OPAQUE(qf[k][q]); });
    waitv<8>();
    if (!early) {
      sfor<14>([&](auto T) { *(LAS bf16x8*)(size_t)(hb + (T / 7) * 2u * REG + (T % 7) * 1024u) = qf[T / 7][T % 7]; });
      if (w == 3u && l < 8u) lds_st(fl + l * 4u, 0u);
    }
    LGKM0(); BAR();
  }
  STAMP(2);
  if (early) {
    sfor<14>([&](auto T) { qf[2 + T / 7][T % 7] = lds_b128(hb + (T / 7) * 2u * REG + (T % 7) * 1024u); });
    LGKM0(); BAR();
    sfor<8>([&](auto P) { if constexpr ((P & 3) == 0) { setm0(ring + 24576u + P * 1024u); NOPS(0); } dma<1, 1024 * (P & 3)>(vv[P & 1], sV, kvs(1u) + (P >> 2) * 4096u); });
  } else {
    BAR();
    unsigned vx[8];
#pragma unroll
    for (int i = 0; i < 8; ++i) vx[i] = xoff(64u * (w & 1u) + 8u * i + drow, i & 3);
    const unsigned mx0 = ldsb + (w & 1u) * 2u * REG + 32768u;
    const unsigned myf = fl + 16u + (w & 1u) * 4u, otf = fl + 16u + ((w & 1u) ^ 1u) * 4u;
    unsigned bw = ring + 2048u * g4 + 64u * q4 + 16u * p4;
    OPAQUE(bw);
    sfor<8>([&](auto P) { if constexpr ((P & 3) == 0) { setm0(ring + 24576u + P * 1024u); NOPS(0); } dma<0, 1024 * (P & 3)>(vw[P & 3], sW, (2u * chunk(1) + 1u) * 262144u + (P >> 2) * 131072u); });
    sfor<56>([&](auto T) { acc_zero<T>(); });
    bf16x8 af[2][8], bf[2][7];
#pragma unroll
    for (int d = 0; d < 8; ++d) { const unsigned a = bw + 512u * (d >> 1) + 8u * (d & 1); af[0][d] = lds_tr2(a, a + 256u); if (d < 7) bf[0][d] = xfrag(bx + 32768u, bx + 32768u + 2u * REG, d); }
    LGKM0();
    unsigned par = 0, cn = chunk(2), rn = 1;
    unsigned aw = bw + 8192u, ax = bx + 32768u + 512u, ax2 = ax + 2u * REG, rv = 1;
    OPAQUE(aw); OPAQUE(ax); OPAQUE(ax2); OPAQUE(rv);
    auto round = [&](auto TAIL) {
      constexpr bool tail = TAIL;
      unsigned so = tail ? kvs(par) : cn * 524288u, so2 = 0;
      sfor<2>([&](auto E) {
        constexpr int e = E;
        bf16x4 lo;
        unsigned awn = 0, axn = 0, pv = 0; u64 bad = 0;
        sfor<56>([&](auto T) {
          constexpr int t = T, d = t / 7, q = t % 7;
          mfa<t>(af[e][d], bf[e][q]);
          if constexpr (t == 0) { if constexpr (e == 0) waitv<16>(); else waitv<8>(); }
          if constexpr (e == 1) {
            if constexpr (t == 2) lds_st(myf, rv);
            if constexpr (t == 8) pv = lds_ld(otf);
            if constexpr (t == 14) { bad = __builtin_amdgcn_ballot_w64(pv < rn); SPIN(bad); }
            if constexpr (t == 17) { GAP(); if (__builtin_expect(bad != 0ull, 0)) wait_ge(otf, rn); asm volatile("" ::: "memory"); }
          }
          if constexpr (t % 3 == 1 && t / 3 < 16) {
            constexpr int j = t / 3, dd = j >> 1; const unsigned a = aw + 512u * (dd >> 1) + 8u * (dd & 1);
            if constexpr ((j & 1) == 0) lo = lds_tr(a); else af[e ^ 1][dd] = cat(lo, lds_tr(a + 256u));
          }
          if constexpr (t % 3 == 2 && t >= 20 && t <= 44 && t != 26 && t != 38) { constexpr int qq = (t - 20) / 3 - (t > 26) - (t > 38); bf[e ^ 1][qq] = xfrag(ax, ax2, qq); }
          constexpr int np = (e == 1 && !tail) ? 16 : 8;
          constexpr auto gap_of = [](int p) { return 3 + 3 * ((p * 16) / np); };
          sfor<np>([&](auto P) {
            constexpr int p = P;
            if constexpr ((p & 3) == 0 && t == gap_of(p) - 1) setm0(p >= 8 ? mx0 + par * 4096u + ((p - 8) >> 2) * REG : ring + par * 16384u + e * 8192u + p * 1024u);
            if constexpr (t == gap_of(p)) {
              if constexpr (HQ < 63) waitv<HQ - 1>();
              if constexpr (p >= 14) { if (w == 2u) dma<0, 1024 * (p & 3)>(vx[p & 7], sX, cn * 128u); }
              else if constexpr (p >= 8) dma<0, 1024 * (p & 3)>(vx[p & 7], sX, cn * 128u);
              else if constexpr (!tail) dma<0, 1024 * (p & 3)>(vw[p & 3], sW, p < 4 ? so : so2);
              else if constexpr (e == 0) dma<1, 1024 * (p & 3)>(vk[p & 3], sK, p < 4 ? so : so2);
              else dma<1, 1024 * (p & 3)>(vv[p & 1], sV, p < 4 ? so : so2);
            }
          });
          if constexpr (t == 5) { so2 = so + (tail ? 4096u : 131072u); SPIN(so2); }
          if constexpr (t == 47) { awn = e == 0 ? bw + (par ^ 1u) * 16384u : aw + 8192u; OPAQUE(awn); }
          if constexpr (t == 50) { axn = e == 0 ? bx + 32768u + (par ^ 1u) * 4096u : ax + 512u; OPAQUE(axn); }
          if constexpr (t == 52) { ax2 = axn + 2u * REG; OPAQUE(ax2); }
          if constexpr (t == 53 && e == 1) { rv += 1u; OPAQUE(rv); }
          if constexpr (t == 55) { LGKM0(); aw = awn; ax = axn; if constexpr (e == 0 && !tail) { so += 262144u; SPIN(so); } }
          GAP();
        });
      });
      par ^= 1u; cn = cn == 9u ? 0u : cn + 1u; rn += 1u;
    };
#pragma unroll 1
    for (unsigned i = 0; i < 8; ++i) round(std::false_type{});
#pragma unroll 1
    for (unsigned i = 8; i < 10; ++i) round(std::true_type{});
    lds_st(myf, 11u);
    NOPS(7);
    sfor<28>([&](auto T) { constexpr int k = T / 7, q = T % 7; qf[k][q] = pack2(acc_rd<14 * k + q>(), acc_rd<14 * k + 7 + q>(), QSCALE); OPAQUE(qf[k][q]); });
  }
  STAMP(3);
  float lsum[7];
  {
    bf16x8 ones = __builtin_bit_cast(bf16x8, u32x4{0x3f803f80u, 0x3f803f80u, 0x3f803f80u, 0x3f803f80u});
    OPAQUE(ones);
    unsigned nsafe = FORCE_SAFE ? 16u : 1u;
#pragma unroll 1
    for (;;) {
      const unsigned l = lane_id(), g4 = l >> 4, q15 = l & 15u, q4 = (l >> 2) & 3u, p4 = l & 3u;
      unsigned ak = ring + 2048u * (q15 >> 3) + 64u * (q15 & 7u) + 16u * (g4 ^ (2u * (q15 >> 3)));
      unsigned av = ring + 8192u + 2048u * (g4 >> 1) + 64u * (4u * (g4 & 1u) + q4) + 16u * ((p4 >> 1) ^ (2u * (g4 & 1u))) + 8u * (p4 & 1u), av1 = av ^ 32u;
      OPAQUE(ak); OPAQUE(av); OPAQUE(av1);
      bf16x8 kf[2][4], vf[8], pp;
      f32x4 sp0, sp1, nm[7];
      waitv<24>();
#pragma unroll
      for (int e = 0; e < 8; ++e) kf[e >> 2][e & 3] = lds_b128(ak + 4096u * (e >> 2) + 512u * (e & 3));
      ak += 16384u;
      auto check = [&](auto Q, u64 big, float mx) {
        constexpr int q = Q;
        if (__builtin_expect(big != 0ull, 0)) {
          const float d = fmaxf(col_max(mx), 0.f), al = __builtin_amdgcn_exp2f(-d);
          NOPS(7);
          sfor<9>([&](auto T) { acc_wr<7 * T + q>(acc_rd<7 * T + q>() * al); });
          nm[q] -= d; sp0 -= d; sp1 -= d;
          NOPS(1);
        }
      };
      auto setref = [&](auto Q, float mx) { constexpr int q = Q; const float d = col_max(mx); nm[q] = f32x4{-d, -d, -d, -d}; OPAQUE(nm[q]); sp0 -= d; sp1 -= d; };
      unsigned so = kvs(2u), mk = ring, dl = 16384u;
      auto step = [&](unsigned s, auto MODE) {
        constexpr bool safe = MODE != 0, first = MODE == 2;
        const int nrec = s < 14u ? (int)(KV_BYTES + 4096u) : 0;
        srd_t sKs = __builtin_amdgcn_make_buffer_rsrc((char*)Kp - 4096, (short)0, nrec, 0x00020000);
        srd_t sVs = __builtin_amdgcn_make_buffer_rsrc((char*)Vp - 4096, (short)0, nrec, 0x00020000);
        unsigned so2 = 0; bf16x4 lo;
        sfor<7>([&](auto I) {
          constexpr int i = I, qs = (i + 6) % 7, qp = (i + 5) % 7;
          f32x4 s0, s1; float mx; u64 big; u32x4 pn;
          sfor<17>([&](auto G) {
            constexpr int g = G;
            constexpr AttGap ag = att_gap(safe, i, g);
            constexpr bool rev = !safe && i == 1;
            constexpr int m = rev ? (g < 9 ? g + 8 : g - 9) : g;
            if constexpr (g == 0 && i == 6) waitv<8 + att_pieces_before(safe, 6, 0)>();
            if constexpr (g == (safe ? 8 : 0) && i == 1) waitv<16 + att_pieces_before(safe, 1, g)>();
            if constexpr (m < 2 && first) (m ? s1 : s0) = mfv00(kf[m][0], qf[0][i]);
            else if constexpr (m < 2) (m ? s1 : s0) = mfv0(kf[m][0], qf[0][i], nm[i]);
            else if constexpr (m < 8) mfv((m & 1) ? s1 : s0, kf[m & 1][m >> 1], qf[m >> 1][i]);
            else if constexpr (first && i < 2) acc_zero<7 * (m - 8) + qp>();
            else if constexpr (first) mfa0<7 * (m - 8) + qp>(m < 16 ? vf[(m - 8) & 7] : ones, pp);
            else mfa<7 * (m - 8) + qp>(m < 16 ? vf[(m - 8) & 7] : ones, pp);
            GAP();
            if constexpr (ag.kr >= 0) kf[ag.kr & 1][ag.kr >> 1] = lds_b128(ak + 4096u * (ag.kr & 1) + 512u * (ag.kr >> 1));
            if constexpr (ag.tr >= 0) { constexpr int d = ag.tr >> 1; const unsigned a = ((d & 1) ? av1 : av) + 512u * (d >> 1);
              if constexpr ((ag.tr & 1) == 0) lo = lds_tr(a); else vf[d] = cat(lo, lds_tr(a + 4096u)); }
            if constexpr (!(first && i == 0)) {
              if constexpr (ag.c == 'x') mx = max5(sp0[0], sp0[1], sp0[2], sp0[3], sp1[0]);
              if constexpr (ag.c == 'y') mx = max5(mx, sp1[1], sp1[2], sp1[3], sp1[3]);
              if constexpr (ag.c == 'z' && first) setref(IC<qs>{}, mx);
              if constexpr (ag.c == 'z' && !first) { big = __builtin_amdgcn_ballot_w64(mx > THR); SPIN(big); }
              if constexpr (ag.c == 'w' && !first) check(IC<qs>{}, big, mx);
              if constexpr (ag.ex >= 0 && ag.ex < 4) { sp0[ag.ex] = __builtin_amdgcn_exp2f(sp0[ag.ex]); OPAQUE(sp0[ag.ex]); }
              if constexpr (ag.ex >= 4 && ag.ex < 8) { sp1[ag.ex - 4] = __builtin_amdgcn_exp2f(sp1[ag.ex - 4]); OPAQUE(sp1[ag.ex - 4]); }
              if constexpr (ag.ex >= 8) { s0[ag.ex - 8] = __builtin_amdgcn_exp2f(s0[ag.ex - 8]); OPAQUE(s0[ag.ex - 8]); }
              if constexpr (ag.cv & 1) { pn[0] = cvtpk(sp0[0], sp0[1]); OPAQUE(pn[0]); }
              if constexpr (ag.cv & 2) { pn[1] = cvtpk(sp0[2], sp0[3]); OPAQUE(pn[1]); }
              if constexpr (ag.cv & 4) { pn[2] = cvtpk(sp1[0], sp1[1]); OPAQUE(pn[2]); }
              if constexpr (ag.cv & 8) { pn[3] = cvtpk(sp1[2], sp1[3]); OPAQUE(pn[3]); }
            }
            if constexpr (ag.pc >= 0) { constexpr int p = ag.pc;
              if constexpr (p < 8) dma<1, 1024 * (p & 3)>(vk[p & 3], sKs, p < 4 ? so : so2); else dma<1, 1024 * (p & 3)>(vv[p & 1], sVs, p < 12 ? so : so2); }
            if constexpr (ag.m0 >= 0) setm0(mk + ag.m0 * 1024u);
            if constexpr (ag.bk == 1) { so2 = so + 4096u; SPIN(so2); }
            if constexpr (ag.bk == 2) { av += dl; OPAQUE(av); av1 += dl; OPAQUE(av1); }
            if constexpr (ag.bk == 3) { ak += dl; OPAQUE(ak); }
            if constexpr (i == 6 && g == 16) LGKM0();
            GAP();
          });
          if constexpr (!(first && i == 0)) pp = __builtin_bit_cast(bf16x8, pn);
          sp0 = s0; sp1 = s1;
        });
        so = kvb | ((so + 8192u) & 0x1ffffu); mk += dl; dl = 0u - dl;
      };
      ak -= 16384u;
      step(0u, IC<2>{});
      { float mx = max5(sp0[0], sp0[1], sp0[2], sp0[3], sp1[0]); mx = max5(mx, sp1[1], sp1[2], sp1[3], sp1[3]); setref(IC<6>{}, mx); GAP(); }
      unsigned s = 1;
#pragma unroll 1
      for (; s < nsafe; ++s) step(s, IC<1>{});
#pragma unroll 1
      for (; s < 16u; ++s) step(s, IC<0>{});
      { float mx = max5(sp0[0], sp0[1], sp0[2], sp0[3], sp1[0]); mx = max5(mx, sp1[1], sp1[2], sp1[3], sp1[3]);
        check(IC<6>{}, __builtin_amdgcn_ballot_w64(mx > THR), mx); }
      NOPS(1);
      sfor<9>([&](auto D) {
        mfa<7 * D + 5>(D < 8 ? vf[D & 7] : ones, pp);
        GAP();
        constexpr int d = D;
        if constexpr (d < 4) { sp0[d] = __builtin_amdgcn_exp2f(sp0[d]); OPAQUE(sp0[d]); } else if constexpr (d < 8) { sp1[d - 4] = __builtin_amdgcn_exp2f(sp1[d - 4]); OPAQUE(sp1[d - 4]); }
        GAP();
      });
      pp = pack2(sp0, sp1, 1.f); OPAQUE(pp);
      NOPS(1);
      mfa<62>(ones, pp);
      sfor<8>([&](auto D) { mfa<7 * D + 6>(vf[D], pp); });
      bool bad = false;
      sfor<7>([&](auto Q) { lsum[Q] = acc_rd1<56 + Q>(); bad |= !(lsum[Q] < 1.0995116e12f); });
      if (__builtin_expect(nsafe == 16u || __builtin_amdgcn_ballot_w64(bad) == 0ull, 1)) break;
      nsafe = 16u;
      waitv<0>();
      sfor<32>([&](auto P) { constexpr int p = P; if constexpr ((p & 3) == 0) { setm0(ring + p * 1024u); NOPS(0); }
        if constexpr ((p >> 3) & 1) dma<1, 1024 * (p & 3)>(vv[p & 1], sV, kvs(p >> 4) + ((p >> 2) & 1) * 4096u);
        else dma<1, 1024 * (p & 3)>(vk[p & 3], sK, kvs(p >> 4) + ((p >> 2) & 1) * 4096u); });
    }
  }
  STAMP(4);
  {
    const unsigned l = lane_id(), g4 = l >> 4, q15 = l & 15u, q4 = (l >> 2) & 3u, p4 = l & 3u, drow = (l >> 2) & 7u;
    srd_t sO = __builtin_amdgcn_make_buffer_rsrc((char*)Wo - 4096, (short)0, (int)(W_BYTES + 4096u), 0x00020000);
    srd_t sP = __builtin_amdgcn_make_buffer_rsrc((char*)Pws + F_BYTES, (short)0, (int)P_BYTES, 0x00020000);
    unsigned vo[4];
#pragma unroll
    for (int i = 0; i < 4; ++i) vo[i] = (hg * 512u + 8u * i + drow) * 1280u + (w & 1u) * 640u + 64u * (l >> 5) + 16u * p4 + 4096u - 1024u * i;
    const unsigned slot0 = ring + 28672u;
    const unsigned rot8 = (rot * 13u) >> 4, sg0 = early ? 0u : 8u;
    auto cchunk = [&](unsigned sg) { return (unsigned)(0x1023443210ull >> (4u * sg)) & 7u; };
    auto sbase = [&](unsigned sg) { return ((0x7e0u >> sg) & 1u) * 327680u + cchunk(sg) * 128u; };
    auto allO = [&](unsigned u) { setm0(slot0 + (u % 3u) * 4096u); NOPS(0); sfor<4>([&](auto P) { dma<0, 1024 * P>(vo[P], sO, sbase(sg0) + ((u + rot8) & 7u) * 40960u); }); };
    waitv<0>();
    wait_ge(fl + 16u + (l & 1u) * 4u, 11u);
    allO(0); allO(1);
    sfor<7>([&](auto Q) {
      constexpr int q = Q;
      const float inv = __builtin_amdgcn_rcpf(lsum[q]);
      sfor<4>([&](auto K) { constexpr int k = K; *(LAS bf16x8*)(size_t)(ring + l * 16u + (k * 7 + q) * 1024u) = pack2(acc_rd<14 * k + q>(), acc_rd<14 * k + 7 + q>(), inv); });
    });
    unsigned ba = slot0 + 1024u * (g4 >> 1) + 64u * (4u * (g4 & 1u) + q4) + 16u * p4, bb = ldsb + l * 16u;
    OPAQUE(ba); OPAQUE(bb);
    bf16x8 af[2][4], bf[2][7];
    if (early) { lds_st(fl + w * 4u, 1u); wait_ge(fl + (l & 1u) * 4u, 1u); }
    else { DBGX(STAMP(5);) LGKM0(); BAR(); DBGX(STAMP(6);) }
    allO(2);
    waitv<8>();
#pragma unroll
    for (int n = 0; n < 4; ++n) { const unsigned a = ba + 8u * (n & 1) + 512u * (n >> 1); af[0][n] = lds_tr2(a, a + 2048u); }
    LGKM0(); asm volatile("" ::: "memory");
    allO(3);
    unsigned k1 = (1u + rot8) & 7u, o3 = ((4u + rot8) & 7u) * 40960u, sl = 4096u;
    unsigned aa = ba + 4096u;
    OPAQUE(aa);
    unsigned bs = bb; bool pfw = false;
    auto firstB = [&]() { const unsigned ab = bs + rot8 * 7168u + (rot8 & 4u) * 3072u;
#pragma unroll
      for (int q = 0; q < 7; ++q) bf[0][q] = lds_b128(ab + 1024u * q); };
    f32x4 oa[28];
    sfor<28>([&](auto T) { f32x4 u; asm volatile("" : "=v"(u)); oa[T] = u; });
    auto unpack = [&](auto R) {
      const unsigned ll = lane_id(), one = (ll >> 4) == ((ll >> 2) & 3u) ? (ll & 1u ? 0x3f800000u : 0x00003f80u) : 0u, lo = (ll & 2u) ? 0u : one, hi = (ll & 2u) ? one : 0u;
      bf16x8 sel[2] = {__builtin_bit_cast(bf16x8, u32x4{lo, hi, 0u, 0u}), __builtin_bit_cast(bf16x8, u32x4{0u, 0u, lo, hi})};
      OPAQUE(sel[0]); OPAQUE(sel[1]); NOPS(1);
      sfor<28>([&](auto T) { mfv0a<R + T / 2>(oa[T], sel[T & 1]); });
    };
    auto packed = [&](auto T) { return pack2(oa[2 * T], oa[2 * T + 1], 1.f); };
    auto gput = [&](unsigned idx, u64 val) {
      asm volatile("s_mov_b64 exec, 1\n\tglobal_store_dwordx2 %0, %1, %2 sc1\n\ts_mov_b64 exec, -1" ::"v"(idx * 8u), "v"(val), "s"(gfl) : "memory"); };
    auto pairU = [&](unsigned i, unsigned sc, unsigned sn, auto ZERO, auto AFTER, auto THROT) {
      {
        sfor<2>([&](auto E) {
          constexpr int e = E;
          unsigned ab = 0, ab2 = 0, so = 0; bf16x4 lo;
          sfor<28>([&](auto T) {
            constexpr int t = T, q = t / 4, n = t % 4;
            if constexpr (ZERO && e == 0) mfv00t(oa[t], af[e][n], bf[e][q]); else mfv(oa[t], af[e][n], bf[e][q]);
            GAP();
            if constexpr (t == 0) {
              if constexpr (AFTER) asm volatile("s_cmp_lt_u32 %0, %1\n\ts_cbranch_scc1 1f\n\ts_waitcnt vmcnt(8)\n1:\n\ts_waitcnt vmcnt(22)" ::"s"(i), "i"(2 - e) : "scc", "memory"); else waitv<8>();
            }
            if constexpr (t >= 1 && t <= 8) { constexpr int j = t - 1; const unsigned a = aa + 4u * (j & 2) + 512u * (j >> 2);
              if constexpr ((j & 1) == 0) lo = lds_tr(a); else af[e ^ 1][j >> 1] = cat(lo, lds_tr(a + 2048u)); }
            if constexpr (t == 9) { ab2 = (k1 & 4u) * 3072u; SPIN(ab2); }
            if constexpr (t == 10) { ab = k1 * 7168u + ab2; SPIN(ab); }
            if constexpr (t == 11) { ab += bs; OPAQUE(ab); setm0(slot0 + sl); }
            if constexpr (t == 12) { so = (i >> 1) * sn; SPIN(so); }
            if constexpr (t == 13) { so += o3 + sc; SPIN(so); LGKM0(); asm volatile("" ::: "memory"); }
            if constexpr ((t == 14 || t == 17 || t == 20 || t == 23) && HO < 63 && THROT) waitv<HO - 1>();
            if constexpr (t == 14 || t == 17 || t == 20 || t == 23) dma<0, 1024 * ((t - 14) / 3)>(vo[(t - 14) / 3], sO, so);
            if constexpr (t >= 15 && t <= 24 && (t - 14) % 3 != 0) { constexpr int qq = (t - 15) - (t - 14) / 3; bf[e ^ 1][qq] = lds_b128(ab + 1024u * qq); }
            if constexpr (t == 25) { sl = sl == 8192u ? 0u : sl + 4096u; SPIN(sl); }
            if constexpr (t == 26) { aa = ba + sl; OPAQUE(aa); k1 = (k1 + 1u) & 7u; SPIN(k1); }
            if constexpr (t == 27) { o3 = o3 == 286720u ? 0u : o3 + 40960u; SPIN(o3); }
            GAP();
          });
        });
      }
    };
    if (early) {
      firstB(); LGKM0();
      pfw = true;
#pragma unroll 1
      for (unsigned sg = 0; sg < 5u; ++sg) {
        const unsigned sc = sbase(sg), sn = sbase(sg + 1u) - sc;
        pairU(0u, sc, sn, std::true_type{}, std::false_type{}, std::true_type{});
#pragma unroll 1
        for (unsigned i = 1; i < 4; ++i) pairU(i, sc, sn, std::false_type{}, std::false_type{}, std::true_type{});
        NOPS(7); GAP();
        if (pfw && sg == 2u) {
          constexpr unsigned NBX = 128 / NHX, PER = 2560u / NBX / 2u;
          unsigned dummy = 0;
#pragma unroll
          for (unsigned k = 0; k < (PER + 63u) / 64u; ++k) {
            const unsigned li = k * 64u + lane_id();
            asm volatile("buffer_load_dword %0, %1, %2, 0 offen" : "+v"(dummy) : "v"(li < PER ? (hg * 512u + 256u) * 1280u + (((b % NBX) * 2u + w) * PER + li) * 128u + 4096u : NONE), "s"(sO) : "memory");
          }
          waitv<0>(); asm volatile("" :: "v"(dummy));
        }
        sfor<4>([&](auto C) { if (sg == C) sfor<14>([&](auto T) { acc_wr<14 * C + T>(__builtin_bit_cast(f32x4, packed(T))); }); });
      }
      lds_st(fl + w * 4u, 2u); wait_ge(fl + (lane_id() & 1u) * 4u, 2u);
      const unsigned a = bb + w * REG;
      sfor<28>([&](auto T) { acc_lds_wr<T, T * 1024>(a); });
      DBGX(STAMP(5);)
      asm volatile("s_waitcnt lgkmcnt(0)" ::: "memory"); BAR();
      DBGX(STAMP(6);)
    }
    unsigned w2 = w; SPIN(w2);
    if (w2 >= 2u) {
      const unsigned a = bb + (w & 1u) * REG;
      sfor<28>([&](auto T) { acc_lds_rd<28 + T, T * 1024>(a); });
      asm volatile("s_waitcnt lgkmcnt(0)" ::: "memory");
      unpack(IC<28>{});
    }
    bs = bb + 2u * REG; OPAQUE(bs);
    firstB();
    LGKM0();
    auto lastseg = [&](unsigned sg, auto AFTER) {
      const unsigned sc = sbase(sg), sn = sbase(sg + 1u) - sc;
#pragma unroll 1
      for (unsigned i = 0; i < 4; ++i) {
        pairU(i, sc, sn, std::false_type{}, AFTER, std::false_type{});
      }
      NOPS(7); GAP();
      const unsigned po = ((b * 10u + (w & 1u) * 5u + cchunk(sg)) * 8u + hg) * P_UNIT + lane_id() * 16u;
      sfor<14>([&](auto T) { __builtin_amdgcn_raw_buffer_store_b128(__builtin_bit_cast(u32x4, packed(T)), sP, po + T * 1024u, 0, 16); });
      GAP();
    };
    lastseg(early ? 5u : 8u, std::false_type{});
    unpack(IC<42>{});
    lastseg(early ? 6u : 9u, std::true_type{});
    if (early) {
      unpack(IC<28>{});
      lastseg(7u, std::true_type{});
    }
    waitv<0>();
    DBGX(STAMP(7);)
    BAR();
    const unsigned ll = lane_id();
    if (w == 0u) {
      gput(hg, ep + 1ull);
      unsigned sp = 0;
      while (__builtin_amdgcn_ballot_w64(__hip_atomic_load(gfl + (ll & 7u), __ATOMIC_RELAXED, __HIP_MEMORY_SCOPE_AGENT) != ep + 1ull) != 0ull && ++sp < SPIN_CAP) __builtin_amdgcn_s_sleep(2);
      if (hg == 0u) gput(32u, ep + 1ull);
    }
    BAR();
    srd_t sOut = __builtin_amdgcn_make_buffer_rsrc(Out, (short)0, (int)X_BYTES, 0x00020000);
    u32x4 v[3][2][8];
    unsigned oo[3];
#pragma unroll
    for (int j = 0; j < 3; ++j) {
      const unsigned u = hg + 8u * (w + 4u * j), ch = (u * 37u) >> 8, q = u - 7u * ch;
      const unsigned pi = u < 70u ? (b * 10u + ch) * 8u * P_UNIT + q * 2048u + ll * 16u : NONE;
      oo[j] = u < 70u ? (b * 112u + q * 16u + (ll & 15u)) * 1280u + ch * 128u + (ll >> 4) * 16u : NONE;
      if (j < 2 || w == 0u) {
#pragma unroll
        for (int k = 0; k < 8; ++k) { v[j][0][k] = __builtin_amdgcn_raw_buffer_load_b128(sP, pi + k * P_UNIT, 0, 16); v[j][1][k] = __builtin_amdgcn_raw_buffer_load_b128(sP, pi + k * P_UNIT + 1024u, 0, 16); }
      }
    }
#pragma unroll
    for (int j = 0; j < 3; ++j) {
      if (j < 2 || w == 0u) {
#pragma unroll
        for (int r = 0; r < 2; ++r) {
          u32x4 o;
#pragma unroll
          for (int e = 0; e < 4; ++e) {
            float lo = __uint_as_float(v[j][r][0][e] << 16), hi = __uint_as_float(v[j][r][0][e] & 0xffff0000u);
#pragma unroll
            for (int k = 1; k < 8; ++k) { lo += __uint_as_float(v[j][r][k][e] << 16); hi += __uint_as_float(v[j][r][k][e] & 0xffff0000u); }
            o[e] = cvtpk(lo, hi);
          }
          __builtin_amdgcn_raw_buffer_store_b128(o, sOut, oo[j] + r * 64u, 0, 0);
        }
      }
    }
  }
}

template <int WHICH>
__global__ __launch_bounds__(256) void probe_k(const void* X, const void* Wq, const void* Wo, void* ws, void* out, unsigned* sink) {
  const unsigned t = blockIdx.x * 256u + threadIdx.x;
  unsigned a = 0;
  if (WHICH & 1) { if (t < 35840u) a ^= ((const unsigned*)X)[t * 32u]; if (t < 40960u) { a ^= ((const unsigned*)Wq)[t * 32u]; a ^= ((const unsigned*)Wo)[t * 32u]; } }
  if (WHICH & 2) { for (unsigned i = 0; i < 5; ++i) { const unsigned li = i * 65536u + t; if (li < 287000u) a ^= ((const unsigned*)ws)[li * 32u]; } if (t < 35840u) a ^= ((const unsigned*)out)[t * 32u]; }
  if (WHICH & 4) {
    const unsigned u = (blockIdx.x >> 3) * 256u + threadIdx.x;
    if (u < 1120u) a ^= ((const unsigned*)X)[u * 1024u] ^ ((const unsigned*)out)[u * 1024u];
    if (u < 1280u) a ^= ((const unsigned*)Wq)[u * 1024u] ^ ((const unsigned*)Wo)[u * 1024u];
    a ^= ((const unsigned*)ws)[u * 1024u]; if (u < 800u) a ^= ((const unsigned*)ws)[(8192u + u) * 1024u];
  }
  if (WHICH & 8) { u64 pc; asm volatile("s_getpc_b64 %0" : "=s"(pc)); const unsigned u = (blockIdx.x >> 3) * 256u + threadIdx.x; if (u < 400u) a ^= ((const unsigned*)((pc & ~255ull) - 52480ull))[u * 32u]; }
  asm volatile("" :: "v"(a));
}
extern "C" void kernel_launch(void* const* d_in, const int* in_sizes, int n_in, void* d_out, int out_size, void* d_ws, size_t ws_size,
                              hipStream_t stream) {
  (void)in_sizes; (void)n_in; (void)out_size; (void)ws_size;
  (void)hipFuncSetAttribute((const void*)fused_k, hipFuncAttributeMaxDynamicSharedMemorySize, (int)LDS_ALL);
  probe_k<0><<<256, 256, 0, stream>>>(d_in[0], d_in[3], d_in[4], d_ws, d_out, (unsigned*)d_ws);
  fused_k<<<256, 256, LDS_ALL, stream>>>(d_in[0], d_in[1], d_in[2], d_in[3], d_in[4], d_ws, d_out);
}
```

```cpp
#include <hip/hip_runtime.h>
#include <hip/hip_bf16.h>
#include <utility>

typedef __bf16 bf16x8 __attribute__((ext_vector_type(8)));
typedef __bf16 bf16x4 __attribute__((ext_vector_type(4)));
typedef __bf16 bf16x2 __attribute__((ext_vector_type(2)));
typedef float f32x4 __attribute__((ext_vector_type(4)));
typedef float f32x2 __attribute__((ext_vector_type(2)));
typedef unsigned u32x4 __attribute__((ext_vector_type(4)));
typedef unsigned u32x2 __attribute__((ext_vector_type(2)));
typedef unsigned long long u64;
typedef __amdgpu_buffer_rsrc_t srd_t;
#define LAS __attribute__((address_space(3)))
#define DI __device__ __forceinline__
#define GAP() __builtin_amdgcn_sched_barrier(0)
#define OPAQUE(x) asm volatile("" : "+v"(x))
#define SPIN(x) asm volatile("" : "+s"(x))
#define BAR() do { asm volatile("" ::: "memory"); __builtin_amdgcn_s_barrier(); asm volatile("" ::: "memory"); } while (0)

constexpr unsigned KV_BYTES = 134217728u, X_BYTES = 3584u * 1280u, W_BYTES = 640u * 8192u;
constexpr unsigned P_UNIT = 14336u, P_BYTES = 320u * 8u * P_UNIT;
constexpr unsigned F_BYTES = 32u * 512u;
constexpr int CODE_BYTES = 55040;
constexpr unsigned REG = 40960u, LDS_ALL = 163840u, FLAGS = 3u * REG + 38912u;
constexpr unsigned SPIN_CAP = 1u << 22;
constexpr float QSCALE = 0.12751743082f;
constexpr float THR = 8.0f;
constexpr unsigned NONE = 0x80000000u;

#define STAMP(i)
#define DBGX(x)

DI unsigned lane_id() { unsigned r; asm volatile("v_mbcnt_lo_u32_b32 %0, -1, 0\n\tv_mbcnt_hi_u32_b32 %0, -1, %0" : "=v"(r)); return r; }
DI unsigned rfl(unsigned x) { return __builtin_amdgcn_readfirstlane(x); }
DI unsigned cvtpk(float lo, float hi) { f32x2 v = {lo, hi}; bf16x2 b = __builtin_convertvector(v, bf16x2); return __builtin_bit_cast(unsigned, b); }
template <int N> DI void waitv() { asm volatile("s_waitcnt vmcnt(%c0)" ::"i"(N) : "memory"); }
DI void setm0(unsigned v) { asm volatile("s_mov_b32 m0, %0" ::"s"(v) : "m0", "memory"); }
template <int NT, int OFF> DI void dma(unsigned v, srd_t srd, unsigned soff) {
  if constexpr (NT) asm volatile("buffer_load_dwordx4 %0, %1, %2 offen offset:%c3 nt lds" ::"v"(v), "s"(srd), "s"(soff), "i"(OFF) : "memory");
  else asm volatile("buffer_load_dwordx4 %0, %1, %2 offen offset:%c3 lds" ::"v"(v), "s"(srd), "s"(soff), "i"(OFF) : "memory");
}
#define LGKM0() __builtin_amdgcn_s_waitcnt(0xC07F)
DI bf16x8 lds_b128(unsigned a) { return *(const LAS bf16x8*)(size_t)a; }
DI bf16x4 lds_tr(unsigned a) { return __builtin_amdgcn_ds_read_tr16_b64_v4bf16((LAS bf16x4*)(size_t)a); }
DI bf16x8 cat(bf16x4 lo, bf16x4 hi) { return __builtin_shufflevector(lo, hi, 0, 1, 2, 3, 4, 5, 6, 7); }
DI bf16x8 lds_tr2(unsigned a0, unsigned a1) {
  bf16x4 lo = __builtin_amdgcn_ds_read_tr16_b64_v4bf16((LAS bf16x4*)(size_t)a0);
  bf16x4 hi = __builtin_amdgcn_ds_read_tr16_b64_v4bf16((LAS bf16x4*)(size_t)a1);
  return __builtin_shufflevector(lo, hi, 0, 1, 2, 3, 4, 5, 6, 7);
}
DI bf16x8 pack2(const f32x4& a, const f32x4& b, float s) {
  u32x4 w = {cvtpk(a[0] * s, a[1] * s), cvtpk(a[2] * s, a[3] * s), cvtpk(b[0] * s, b[1] * s), cvtpk(b[2] * s, b[3] * s)};
  return __builtin_bit_cast(bf16x8, w);
}
DI u32x4 regroup(u32x4 v) {
  auto a = __builtin_amdgcn_permlane32_swap(v[0], v[2], false, false), b = __builtin_amdgcn_permlane32_swap(v[1], v[3], false, false);
  auto c = __builtin_amdgcn_permlane16_swap(a[0], a[1], false, false), d = __builtin_amdgcn_permlane16_swap(b[0], b[1], false, false);
  return u32x4{c[0], d[0], c[1], d[1]};
}
DI float col_max(float v) {
  auto r = __builtin_amdgcn_permlane16_swap(__float_as_uint(v), __float_as_uint(v), false, false);
  v = fmaxf(__uint_as_float(r[0]), __uint_as_float(r[1]));
  auto s = __builtin_amdgcn_permlane32_swap(__float_as_uint(v), __float_as_uint(v), false, false);
  return fmaxf(__uint_as_float(s[0]), __uint_as_float(s[1]));
}
DI float col_sum(float v) {
  auto r = __builtin_amdgcn_permlane16_swap(__float_as_uint(v), __float_as_uint(v), false, false);
  v = __uint_as_float(r[0]) + __uint_as_float(r[1]);
  auto s = __builtin_amdgcn_permlane32_swap(__float_as_uint(v), __float_as_uint(v), false, false);
  return __uint_as_float(s[0]) + __uint_as_float(s[1]);
}

template <class F, int... Is> DI void sfor_(F&& f, std::integer_sequence<int, Is...>) { (f(std::integral_constant<int, Is>{}), ...); }
template <int N, class F> DI void sfor(F&& f) { sfor_(f, std::make_integer_sequence<int, N>{}); }
template <int T> DI void mfa(bf16x8 a, bf16x8 b) { asm volatile("v_mfma_f32_16x16x32_bf16 a[%c2:%c3], %0, %1, a[%c2:%c3]" ::"v"(a), "v"(b), "i"(4 * T + 4), "i"(4 * T + 7)); }
template <int T> DI void mfa0(bf16x8 a, bf16x8 b) { asm volatile("v_mfma_f32_16x16x32_bf16 a[%c2:%c3], %0, %1, 0" ::"v"(a), "v"(b), "i"(4 * T + 4), "i"(4 * T + 7)); }
DI float max5(float a, float b, float c, float d, float e) { float r; asm volatile("v_max3_f32 %0, %1, %2, %3\n\tv_max3_f32 %0, %0, %4, %5" : "=&v"(r) : "v"(a), "v"(b), "v"(c), "v"(d), "v"(e)); return r; }
template <int T, int R> DI void mfa0a(bf16x8 a) { asm volatile("v_mfma_f32_16x16x32_bf16 a[%c1:%c2], %0, a[%c3:%c4], 0" ::"v"(a), "i"(4 * T + 4), "i"(4 * T + 7), "i"(4 * R + 4), "i"(4 * R + 7)); }
DI unsigned lds_ld(unsigned a) { return *(volatile LAS unsigned*)(size_t)a; }
DI void lds_st(unsigned a, unsigned v) { *(volatile LAS unsigned*)(size_t)a = v; }
DI void wait_ge(unsigned a, unsigned need) {
  unsigned sp = 0;
  while (__builtin_amdgcn_ballot_w64(lds_ld(a) < need) != 0ull && ++sp < SPIN_CAP) __builtin_amdgcn_s_sleep(1);
  asm volatile("" ::: "memory");
}
DI void mfv(f32x4& d, bf16x8 a, bf16x8 b) { asm volatile("v_mfma_f32_16x16x32_bf16 %0, %1, %2, %0" : "+v"(d) : "v"(a), "v"(b)); }
DI f32x4 mfv0(bf16x8 a, bf16x8 b, f32x4 c) { f32x4 d; asm volatile("v_mfma_f32_16x16x32_bf16 %0, %1, %2, %3" : "=&v"(d) : "v"(a), "v"(b), "v"(c)); return d; }
DI f32x4 mfv00(bf16x8 a, bf16x8 b) { f32x4 d; asm volatile("v_mfma_f32_16x16x32_bf16 %0, %1, %2, 0" : "=&v"(d) : "v"(a), "v"(b)); return d; }
template <int R> DI void mfv0a(f32x4& d, bf16x8 a) { asm volatile("v_mfma_f32_16x16x32_bf16 %0, %1, a[%c2:%c3], 0" : "+v"(d) : "v"(a), "i"(4 * R + 4), "i"(4 * R + 7)); }
DI void mfv00t(f32x4& d, bf16x8 a, bf16x8 b) { asm volatile("v_mfma_f32_16x16x32_bf16 %0, %1, %2, 0" : "+v"(d) : "v"(a), "v"(b)); }
template <int T> DI float acc_rd1() { float x; asm volatile("v_accvgpr_read_b32 %0, a%c1" : "=v"(x) : "i"(4 * T + 4)); return x; }
template <int T, int OFF> DI void acc_lds_wr(unsigned a) { asm volatile("ds_write_b128 %0, a[%c1:%c2] offset:%c3" ::"v"(a), "i"(4 * T + 4), "i"(4 * T + 7), "i"(OFF) : "memory"); }
template <int T, int OFF> DI void acc_lds_rd(unsigned a) { asm volatile("ds_read_b128 a[%c1:%c2], %0 offset:%c3" ::"v"(a), "i"(4 * T + 4), "i"(4 * T + 7), "i"(OFF) : "memory"); }
template <int T> DI f32x4 acc_rd() {
  float x0, x1, x2, x3;
  asm volatile("v_accvgpr_read_b32 %0, a%c4\n\tv_accvgpr_read_b32 %1, a%c5\n\tv_accvgpr_read_b32 %2, a%c6\n\tv_accvgpr_read_b32 %3, a%c7"
               : "=v"(x0), "=v"(x1), "=v"(x2), "=v"(x3) : "i"(4 * T + 4), "i"(4 * T + 5), "i"(4 * T + 6), "i"(4 * T + 7));
  return f32x4{x0, x1, x2, x3};
}
template <int T> DI void acc_wr(f32x4 v) {
  asm volatile("v_accvgpr_write_b32 a%c4, %0\n\tv_accvgpr_write_b32 a%c5, %1\n\tv_accvgpr_write_b32 a%c6, %2\n\tv_accvgpr_write_b32 a%c7, %3"
               ::"v"(v[0]), "v"(v[1]), "v"(v[2]), "v"(v[3]), "i"(4 * T + 4), "i"(4 * T + 5), "i"(4 * T + 6), "i"(4 * T + 7));
}
template <int T> DI void acc_zero() {
  asm volatile("v_accvgpr_write_b32 a%c0, 0\n\tv_accvgpr_write_b32 a%c1, 0\n\tv_accvgpr_write_b32 a%c2, 0\n\tv_accvgpr_write_b32 a%c3, 0"
               ::"i"(4 * T + 4), "i"(4 * T + 5), "i"(4 * T + 6), "i"(4 * T + 7));
}
#define NOPS(n) asm volatile("s_nop " #n)
template <int V> using IC = std::integral_constant<int, V>;

#define HQ 14
#define HO 12
#define ATN 10
#define KROTM 15u
#define FORCE_SAFE false
constexpr const char* ATT_FAST[7] = {"m0123p4567CDabcdp", "4tt5tt6tt7ttCptDt", "ttt0t1tt2345p67CD", "m0p12p34B5p67pCDm", "p01p23A45p67.pCDm", "p0123p4567CDpab.c", "3kk4kk5kk6kk7pCD."};
constexpr const char* ATT_SAFE[7] = {"xyzmw01234567CDpp", "xyz.w01234567CD..", "xyzpw01234567CDpm", "xyzpw01234567CDpp", "xyzpw01234567CDpp", "xyzpw01234567CDpp", "xyzpw01234567CDpp"};
struct AttGap { int ex, cv, tr, kr, pc, m0, bk; char c; };
constexpr AttGap att_gap(bool safe, int i, int g) {
  const char* const* tb = safe ? ATT_SAFE : ATT_FAST;
  AttGap r{-1, 0, -1, -1, -1, -1, 0, tb[i][g]};
  int nt = 0, nk = 0, np = 0;
  for (int k = 0; k < 17 * i + g; ++k) { const char c = tb[k / 17][k % 17]; nt += c == 't'; nk += c == 'k'; np += c == 'p'; }
  const char c = r.c;
  if (c >= '0' && c <= '7') r.ex = c - '0';
  if (c >= 'a' && c <= 'd') r.ex = 8 + c - 'a';
  if (c == 'C') r.cv = 3;
  if (c == 'D') r.cv = 12;
  if (c == 't') r.tr = nt;
  if (c == 'k') r.kr = nk;
  if (c == 'p') r.pc = np;
  if (c == 'm') { r.m0 = np; if (np == 0) r.bk = 1; }
  if (c == 'B') r.bk = 2;
  if (c == 'A') r.bk = 3;
  if (safe) {
    if (i == 1 && g >= 8) r.tr = g - 8;
    if (i == 2 && g < 7) r.tr = g + 9;
    if (i == 6 && g < 8) r.kr = g;
    if (i == 4 && g == 14) r.m0 = 8;
    if (i == 5 && g == 15) r.m0 = 12;
    if (i == 3 && g == 4) r.bk = 2;
    if (i == 1 && g == 3) r.bk = 3;
  }
  return r;
}
constexpr int att_pieces_before(bool safe, int i, int g) { int n = 0; for (int k = 0; k < 17 * i + g; ++k) n += att_gap(safe, k / 17, k % 17).pc >= 0; return n; }
static_assert(att_pieces_before(true, 7, 0) == 16 && att_pieces_before(false, 7, 0) == 16, "pieces");

#define NHX 8
DI void map_block(unsigned g, unsigned& b, unsigned& hg, unsigned& sh) {
  constexpr unsigned HG = NHX / 4, NXH = 32 / NHX, NBX = 32 / HG;
  const unsigned xcd = g & 7u, sl = g >> 3, j = sl % HG, bi = sl / HG;
  b = (xcd / NXH) * NBX + bi; hg = (xcd % NXH) * HG + j;
  sh = ((bi * 10u) / NBX + (j * 10u) / HG) % 10u;
}

__global__ __launch_bounds__(256, 1) void fused_k(const void* __restrict__ Xp, const void* __restrict__ Kp, const void* __restrict__ Vp,
                                                  const void* __restrict__ Wq, const void* __restrict__ Wo, void* __restrict__ Pws,
                                                  void* __restrict__ Out) {
  extern __shared__ __attribute__((aligned(16))) char lds[];
  const unsigned ldsb = (unsigned)(size_t)lds;
  const unsigned l = threadIdx.x & 63u, w = rfl(threadIdx.x >> 6);
  const bool early = w < 2u;
  unsigned b, hg, rot;
  map_block(blockIdx.x, b, hg, rot);
  const unsigned h = 4u * hg + w;
  const unsigned g4 = l >> 4, q15 = l & 15u, q4 = (l >> 2) & 3u, p4 = l & 3u;
  DBGX(unsigned seg[8]; unsigned segr[8];)
  STAMP(0);
  {
    u64 pc; asm volatile("s_getpc_b64 %0" : "=s"(pc));
    srd_t sC = __builtin_amdgcn_make_buffer_rsrc((void*)(pc & ~255ull), (short)0, CODE_BYTES, 0x00020000);
    setm0(ldsb + w * REG + 32768u); NOPS(0);
    asm volatile("buffer_load_dword %0, %1, 0 offen lds" ::"v"((((blockIdx.x >> 3) * 4u + w) * 4u + (l & 3u)) * 128u), "s"(sC) : "memory");
  }
  u64* const gfl = (u64*)Pws + b * 64u;
  u32x2 epv;
  asm volatile("global_load_dwordx2 %0, %1, %2 offset:256 sc1" : "=v"(epv) : "v"(0u), "s"(gfl) : "memory");
  srd_t sK = __builtin_amdgcn_make_buffer_rsrc((char*)Kp - 4096, (short)0, (int)(KV_BYTES + 4096u), 0x00020000);
  srd_t sV = __builtin_amdgcn_make_buffer_rsrc((char*)Vp - 4096, (short)0, (int)(KV_BYTES + 4096u), 0x00020000);
  srd_t sX = __builtin_amdgcn_make_buffer_rsrc((char*)Xp - 4096, (short)0, (int)(X_BYTES + 4096u), 0x00020000);
  srd_t sW = __builtin_amdgcn_make_buffer_rsrc((char*)Wq - 4096, (short)0, (int)(W_BYTES + 4096u), 0x00020000);
  const unsigned ring = ldsb + w * REG;
  const unsigned fl = ldsb + FLAGS;
  const unsigned drow = (l >> 2) & 7u, dsub = l >> 5;
  unsigned kh = (blockIdx.x * 4u + w) * 2654435761u; kh ^= kh >> 15; kh *= 2246822519u; kh ^= kh >> 13;
  const unsigned kvb = (b * 32u + h) * 131072u, krot = (kh & KROTM) * 8192u;
  auto kvs = [&](unsigned s) { return kvb + ((krot + s * 8192u) & 0x1ffffu); };
  unsigned vk[4], vv[2];
#pragma unroll
  for (int i = 0; i < 4; ++i) vk[i] = drow * 256u + 64u * dsub + 16u * (p4 ^ (i & 2u)) + ((i & 1) ? 3200u : 4096u);
#pragma unroll
  for (int i = 0; i < 2; ++i) vv[i] = drow * 256u + 64u * dsub + 16u * (p4 ^ (2u * (drow >> 2))) + (i ? 3200u : 4096u);
  auto chunk = [&](unsigned i) { unsigned c = i + rot; return c >= 10u ? c - 10u : c; };
  auto xoff = [&](unsigned rx, int i) { return rx < 112u ? (b * 112u + rx) * 1280u + 64u * dsub + 16u * (p4 ^ (2u * (i & 1))) + 4096u - 1024u * i : NONE; };
  unsigned bx = ldsb + 1024u * (q15 >> 3) + 64u * (q15 & 7u) + 16u * (g4 ^ (2u * (q15 >> 3)));
  OPAQUE(bx);
  auto xfrag = [&](unsigned a0, unsigned a1, int q) { return lds_b128((q < 4 ? a0 : a1) + ((q >> 1) & 1) * REG + (q & 1) * 2048u); };
  unsigned vw[4];
#pragma unroll
  for (int i = 0; i < 4; ++i) vw[i] = (8u * (i >> 1) + drow) * 8192u + 128u * (i & 1) + 64u * dsub + 16u * p4 + h * 256u + 4096u - 1024u * i;

  asm volatile("" ::: "a255");
  bf16x8 qf[4][7];
  u64 ep;
  const unsigned hb = ldsb + (w & 1u) * REG + 24576u + l * 16u;
  {
    unsigned v1[4], vx[4];
#pragma unroll
    for (int i = 0; i < 4; ++i) {
      v1[i] = (8u * i + drow) * 8192u + 64u * dsub + 16u * (p4 ^ (2u * (i & 1))) + (4u * hg + (w & 1u)) * 256u + (w >> 1) * 128u + 4096u - 1024u * i;
      vx[i] = xoff(32u * w + 8u * i + drow, i);
    }
    const unsigned mx0 = ring + 24576u;
    auto allW = [&](unsigned s) { setm0(ring + (((s >> 1) + 2u) % 3u) * 8192u + (s & 1u) * 4096u); NOPS(0);
      sfor<4>([&](auto P) { dma<0, 1024 * P>(v1[P], sW, (2u * chunk(s >> 1) + (s & 1u)) * 262144u); }); };
    auto allX = [&](unsigned i) { setm0(mx0 + ((i + 2u) & 3u) * 4096u); NOPS(0); sfor<4>([&](auto P) { dma<0, 1024 * P>(vx[P], sX, chunk(i) * 128u); }); };
    unsigned bw = ring + 1024u * g4 + 64u * q4 + 16u * ((p4 >> 1) ^ (2u * (g4 & 1u))) + 8u * (p4 & 1u);
    OPAQUE(bw);
    f32x4 qa[28];
    allX(0); allW(0); allW(1);
    sfor<28>([&](auto T) { qa[T] = f32x4{}; OPAQUE(qa[T]); });
    allX(1); allW(2); allW(3); allX(2); allW(4); allW(5);
    bf16x8 af[2][4], bf[2][7];
    waitv<28>();
    OPAQUE(epv);
    ep = ((u64)rfl(epv[1]) << 32) | rfl(epv[0]);
    BAR();
    STAMP(1);
#pragma unroll
    for (int d = 0; d < 4; ++d) { const unsigned a = ((bw + 16384u) ^ (32u * (d & 1))) + 512u * (d >> 1); af[0][d] = lds_tr2(a, a + 256u); }
#pragma unroll
    for (int q = 0; q < 7; ++q) bf[0][q] = xfrag(bx + 32768u, bx + 32768u + 2u * REG, q);
    LGKM0();
    unsigned sl = 16384u, xb = 8192u, mx = mx0 + 4096u, cx = rfl(chunk(3)) * 128u;
    unsigned aw = bw + 16384u + 4096u, aw1 = aw ^ 32u, ax = bx + 32768u + 512u, ax2 = ax + 2u * REG;
    OPAQUE(aw); OPAQUE(aw1); OPAQUE(ax); OPAQUE(ax2);
    srd_t sT = sW; unsigned so = 0, hs = 0;
    auto round = [&](auto TAIL) {
      constexpr bool tail = TAIL;
      sfor<2>([&](auto E) {
        constexpr int e = E;
        bf16x4 lo;
        sfor<28>([&](auto T) {
          constexpr int t = T, d = t / 7, q = t % 7;
          mfv(qa[t], af[e][d], bf[e][q]);
          GAP();
          if constexpr (t == 0) { if constexpr (e == 0) waitv<(tail ? 16 : 24)>(); else { waitv<(tail ? 12 : 18)>(); BAR(); } }
          constexpr const char* plan = "sttmbtptbtbptbtbptbpxbqAqBC.";
          constexpr char c = plan[t];
          constexpr auto before = [](char k, int n) { int r = 0; for (int i = 0; i < n; ++i) r += plan[i] == k; return r; };
          if constexpr (c == 's' && !tail) { so = e == 0 ? cx * 4096u : so + 262144u; SPIN(so); }
          if constexpr (c == 's' && tail && e == 1) { so += hs; SPIN(so); }
          if constexpr (c == 'm') setm0(ring + sl + e * 4096u);
          if constexpr (c == 't') { constexpr int j = before('t', t); const unsigned a = ((j & 2) ? aw1 : aw) + 512u * (j >> 2);
            if constexpr ((j & 1) == 0) lo = lds_tr(a); else af[e ^ 1][j >> 1] = cat(lo, lds_tr(a + 256u)); }
          if constexpr (c == 'b') { constexpr int qq = before('b', t); bf[e ^ 1][qq] = xfrag(ax, ax2, qq); }
          if constexpr (c == 'p') { constexpr int p = before('p', t); if constexpr (tail) dma<1, 1024 * p>(v1[p], sT, so); else dma<0, 1024 * p>(v1[p], sW, so); }
          if constexpr (c == 'x') { if constexpr (!tail) setm0(mx); if constexpr (e == 1) { sl = sl == 16384u ? 0u : sl + 8192u; SPIN(sl); } }
          if constexpr (c == 'q' && !tail) { constexpr int p = 2 * e + before('q', t); dma<0, 1024 * p>(vx[p], sX, cx); }
          if constexpr (c == 'A') { if constexpr (e == 0) { unsigned sn = sl == 16384u ? 0u : sl + 8192u; SPIN(sn); aw = bw + sn; } else aw += 4096u; OPAQUE(aw); }
          if constexpr (c == 'B') { aw1 = aw ^ 32u; OPAQUE(aw1); if constexpr (e == 0) { xb = (xb + 4096u) & 12288u; SPIN(xb); } else { cx = cx == 1152u ? 0u : cx + 128u; SPIN(cx); } }
          if constexpr (c == 'C') { if constexpr (e == 0) ax = bx + 24576u + xb; else ax += 512u; OPAQUE(ax); ax2 = ax + 2u * REG; OPAQUE(ax2); }
          if constexpr (t == 27) { LGKM0(); if constexpr (e == 1) { mx = mx0 + ((xb + 12288u) & 12288u); SPIN(mx); } }
          GAP();
        });
      });
    };
#pragma unroll 1
    for (unsigned i = 0; i < 7; ++i) round(std::false_type{});
#pragma unroll 1
    for (unsigned j = 0; j < 3; ++j) {
      if (early) {
#pragma unroll
        for (int k = 0; k < 4; ++k) v1[k] = j == 1u ? vv[k & 1] : vk[k];
        sT = j == 1u ? sV : sK; so = kvs(j >> 1); hs = 4096u;
      } else {
#pragma unroll
        for (int k = 0; k < 4; ++k) v1[k] = vw[k];
        so = (2u * chunk(8u + (j >> 1)) + (j & 1u)) * 262144u; hs = 131072u;
      }
      round(std::true_type{});
    }
    NOPS(7);
    GAP();
    sfor<14>([&](auto T) { constexpr int k = T / 7, q = T % 7; qf[k][q] = __builtin_bit_cast(bf16x8, regroup(__builtin_bit_cast(u32x4, pack2(qa[14 * k + q], qa[14 * k + 7 + q], QSCALE)))); OPAQUE(qf[k][q]); });
    if (!early) {
      sfor<14>([&](auto T) { *(LAS bf16x8*)(size_t)(hb + (T / 7) * 2u * REG + (T % 7) * 1024u) = qf[T / 7][T % 7]; });
      if (w == 3u && l < 8u) lds_st(fl + l * 4u, 0u);
    }
    LGKM0(); BAR();
  }
  STAMP(2);
  if (early) {
    sfor<14>([&](auto T) { qf[2 + T / 7][T % 7] = lds_b128(hb + (T / 7) * 2u * REG + (T % 7) * 1024u); });
    LGKM0(); BAR();
    sfor<8>([&](auto P) { if constexpr ((P & 3) == 0) { setm0(ring + 24576u + P * 1024u); NOPS(0); } dma<1, 1024 * (P & 3)>(vv[P & 1], sV, kvs(1u) + (P >> 2) * 4096u); });
  } else {
    BAR();
    unsigned vx[8];
#pragma unroll
    for (int i = 0; i < 8; ++i) vx[i] = xoff(64u * (w & 1u) + 8u * i + drow, i & 3);
    const unsigned mx0 = ldsb + (w & 1u) * 2u * REG + 32768u;
    const unsigned myf = fl + 16u + (w & 1u) * 4u, otf = fl + 16u + ((w & 1u) ^ 1u) * 4u;
    unsigned bw = ring + 2048u * g4 + 64u * q4 + 16u * p4;
    OPAQUE(bw);
    sfor<8>([&](auto P) { if constexpr ((P & 3) == 0) { setm0(ring + 24576u + P * 1024u); NOPS(0); } dma<0, 1024 * (P & 3)>(vw[P & 3], sW, (2u * chunk(9) + 1u) * 262144u + (P >> 2) * 131072u); });
    sfor<56>([&](auto T) { acc_zero<T>(); });
    waitv<24>();
    bf16x8 af[2][8], bf[2][7];
#pragma unroll
    for (int d = 0; d < 8; ++d) { const unsigned a = bw + 512u * (d >> 1) + 8u * (d & 1); af[0][d] = lds_tr2(a, a + 256u); if (d < 7) bf[0][d] = xfrag(bx + 32768u, bx + 32768u + 2u * REG, d); }
    LGKM0();
    unsigned par = 0, cn = chunk(0), rn = 1;
    unsigned aw = bw + 8192u, ax = bx + 32768u + 512u, ax2 = ax + 2u * REG, rv = 1;
    OPAQUE(aw); OPAQUE(ax); OPAQUE(ax2); OPAQUE(rv);
    auto round = [&](auto TAIL) {
      constexpr bool tail = TAIL;
      unsigned so = tail ? kvs(par) : cn * 524288u, so2 = 0;
      sfor<2>([&](auto E) {
        constexpr int e = E;
        bf16x4 lo;
        unsigned awn = 0, axn = 0, pv = 0; u64 bad = 0;
        sfor<56>([&](auto T) {
          constexpr int t = T, d = t / 7, q = t % 7;
          mfa<t>(af[e][d], bf[e][q]);
          if constexpr (t == 0) { if constexpr (e == 0) waitv<16>(); else waitv<8>(); }
          if constexpr (e == 1) {
            if constexpr (t == 2) lds_st(myf, rv);
            if constexpr (t == 8) pv = lds_ld(otf);
            if constexpr (t == 14) { bad = __builtin_amdgcn_ballot_w64(pv < rn); SPIN(bad); }
            if constexpr (t == 17) { GAP(); if (__builtin_expect(bad != 0ull, 0)) wait_ge(otf, rn); asm volatile("" ::: "memory"); }
          }
          if constexpr (t % 3 == 1 && t / 3 < 16) {
            constexpr int j = t / 3, dd = j >> 1; const unsigned a = aw + 512u * (dd >> 1) + 8u * (dd & 1);
            if constexpr ((j & 1) == 0) lo = lds_tr(a); else af[e ^ 1][dd] = cat(lo, lds_tr(a + 256u));
          }
          if constexpr (t % 3 == 2 && t >= 20 && t <= 44 && t != 26 && t != 38) { constexpr int qq = (t - 20) / 3 - (t > 26) - (t > 38); bf[e ^ 1][qq] = xfrag(ax, ax2, qq); }
          constexpr int np = (e == 1 && !tail) ? 16 : 8;
          constexpr auto gap_of = [](int p) { return 3 + 3 * ((p * 16) / np); };
          sfor<np>([&](auto P) {
            constexpr int p = P;
            if constexpr ((p & 3) == 0 && t == gap_of(p) - 1) setm0(p >= 8 ? mx0 + par * 4096u + ((p - 8) >> 2) * REG : ring + par * 16384u + e * 8192u + p * 1024u);
            if constexpr (t == gap_of(p)) {
              if constexpr (HQ < 63) waitv<HQ - 1>();
              if constexpr (p >= 14) { if (w == 2u) dma<0, 1024 * (p & 3)>(vx[p & 7], sX, cn * 128u); }
              else if constexpr (p >= 8) dma<0, 1024 * (p & 3)>(vx[p & 7], sX, cn * 128u);
              else if constexpr (!tail) dma<0, 1024 * (p & 3)>(vw[p & 3], sW, p < 4 ? so : so2);
              else if constexpr (e == 0) dma<1, 1024 * (p & 3)>(vk[p & 3], sK, p < 4 ? so : so2);
              else dma<1, 1024 * (p & 3)>(vv[p & 1], sV, p < 4 ? so : so2);
            }
          });
          if constexpr (t == 5) { so2 = so + (tail ? 4096u : 131072u); SPIN(so2); }
          if constexpr (t == 47) { awn = e == 0 ? bw + (par ^ 1u) * 16384u : aw + 8192u; OPAQUE(awn); }
          if constexpr (t == 50) { axn = e == 0 ? bx + 32768u + (par ^ 1u) * 4096u : ax + 512u; OPAQUE(axn); }
          if constexpr (t == 52) { ax2 = axn + 2u * REG; OPAQUE(ax2); }
          if constexpr (t == 53 && e == 1) { rv += 1u; OPAQUE(rv); }
          if constexpr (t == 55) { LGKM0(); aw = awn; ax = axn; if constexpr (e == 0 && !tail) { so += 262144u; SPIN(so); } }
          GAP();
        });
      });
      par ^= 1u; cn = cn == 9u ? 0u : cn + 1u; rn += 1u;
    };
#pragma unroll 1
    for (unsigned i = 0; i < 8; ++i) round(std::false_type{});
#pragma unroll 1
    for (unsigned i = 8; i < 10; ++i) round(std::true_type{});
    lds_st(myf, 11u);
    NOPS(7);
    sfor<28>([&](auto T) { constexpr int k = T / 7, q = T % 7; qf[k][q] = pack2(acc_rd<14 * k + q>(), acc_rd<14 * k + 7 + q>(), QSCALE); OPAQUE(qf[k][q]); });
  }
  STAMP(3);
  float lsum[7];
  {
    bf16x8 ones = __builtin_bit_cast(bf16x8, u32x4{0x3f803f80u, 0x3f803f80u, 0x3f803f80u, 0x3f803f80u});
    OPAQUE(ones);
    unsigned nsafe = FORCE_SAFE ? 16u : 1u;
#pragma unroll 1
    for (;;) {
      const unsigned l = lane_id(), g4 = l >> 4, q15 = l & 15u, q4 = (l >> 2) & 3u, p4 = l & 3u;
      unsigned ak = ring + 2048u * (q15 >> 3) + 64u * (q15 & 7u) + 16u * (g4 ^ (2u * (q15 >> 3)));
      unsigned av = ring + 8192u + 2048u * (g4 >> 1) + 64u * (4u * (g4 & 1u) + q4) + 16u * ((p4 >> 1) ^ (2u * (g4 & 1u))) + 8u * (p4 & 1u), av1 = av ^ 32u;
      OPAQUE(ak); OPAQUE(av); OPAQUE(av1);
      bf16x8 kf[2][4], vf[8], pp;
      f32x4 sp0, sp1, nm[7];
      waitv<24>();
#pragma unroll
      for (int e = 0; e < 8; ++e) kf[e >> 2][e & 3] = lds_b128(ak + 4096u * (e >> 2) + 512u * (e & 3));
      ak += 16384u;
      auto check = [&](auto Q, u64 big, float mx) {
        constexpr int q = Q;
        if (__builtin_expect(big != 0ull, 0)) {
          const float d = fmaxf(col_max(mx), 0.f), al = __builtin_amdgcn_exp2f(-d);
          NOPS(7);
          sfor<9>([&](auto T) { acc_wr<7 * T + q>(acc_rd<7 * T + q>() * al); });
          nm[q] -= d; sp0 -= d; sp1 -= d;
          NOPS(1);
        }
      };
      auto setref = [&](auto Q, float mx) { constexpr int q = Q; const float d = col_max(mx); nm[q] = f32x4{-d, -d, -d, -d}; OPAQUE(nm[q]); sp0 -= d; sp1 -= d; };
      unsigned so = kvs(2u), mk = ring, dl = 16384u;
      auto step = [&](unsigned s, auto MODE) {
        constexpr bool safe = MODE != 0, first = MODE == 2;
        const int nrec = s < 14u ? (int)(KV_BYTES + 4096u) : 0;
        srd_t sKs = __builtin_amdgcn_make_buffer_rsrc((char*)Kp - 4096, (short)0, nrec, 0x00020000);
        srd_t sVs = __builtin_amdgcn_make_buffer_rsrc((char*)Vp - 4096, (short)0, nrec, 0x00020000);
        unsigned so2 = 0; bf16x4 lo;
        sfor<7>([&](auto I) {
          constexpr int i = I, qs = (i + 6) % 7, qp = (i + 5) % 7;
          f32x4 s0, s1; float mx; u64 big; u32x4 pn;
          sfor<17>([&](auto G) {
            constexpr int g = G;
            constexpr AttGap ag = att_gap(safe, i, g);
            constexpr bool rev = !safe && i == 1;
            constexpr int m = rev ? (g < 9 ? g + 8 : g - 9) : g;
            if constexpr (g == 0 && i == 6) waitv<8 + att_pieces_before(safe, 6, 0)>();
            if constexpr (g == (safe ? 8 : 0) && i == 1) waitv<16 + att_pieces_before(safe, 1, g)>();
            if constexpr (m < 2 && first) (m ? s1 : s0) = mfv00(kf[m][0], qf[0][i]);
            else if constexpr (m < 2) (m ? s1 : s0) = mfv0(kf[m][0], qf[0][i], nm[i]);
            else if constexpr (m < 8) mfv((m & 1) ? s1 : s0, kf[m & 1][m >> 1], qf[m >> 1][i]);
            else if constexpr (first && i < 2) acc_zero<7 * (m - 8) + qp>();
            else if constexpr (first) mfa0<7 * (m - 8) + qp>(m < 16 ? vf[(m - 8) & 7] : ones, pp);
            else mfa<7 * (m - 8) + qp>(m < 16 ? vf[(m - 8) & 7] : ones, pp);
            GAP();
            if constexpr (ag.kr >= 0) kf[ag.kr & 1][ag.kr >> 1] = lds_b128(ak + 4096u * (ag.kr & 1) + 512u * (ag.kr >> 1));
            if constexpr (ag.tr >= 0) { constexpr int d = ag.tr >> 1; const unsigned a = ((d & 1) ? av1 : av) + 512u * (d >> 1);
              if constexpr ((ag.tr & 1) == 0) lo = lds_tr(a); else vf[d] = cat(lo, lds_tr(a + 4096u)); }
            if constexpr (!(first && i == 0)) {
              if constexpr (ag.c == 'x') mx = max5(sp0[0], sp0[1], sp0[2], sp0[3], sp1[0]);
              if constexpr (ag.c == 'y') mx = max5(mx, sp1[1], sp1[2], sp1[3], sp1[3]);
              if constexpr (ag.c == 'z' && first) setref(IC<qs>{}, mx);
              if constexpr (ag.c == 'z' && !first) { big = __builtin_amdgcn_ballot_w64(mx > THR); SPIN(big); }
              if constexpr (ag.c == 'w' && !first) check(IC<qs>{}, big, mx);
              if constexpr (ag.ex >= 0 && ag.ex < 4) { sp0[ag.ex] = __builtin_amdgcn_exp2f(sp0[ag.ex]); OPAQUE(sp0[ag.ex]); }
              if constexpr (ag.ex >= 4 && ag.ex < 8) { sp1[ag.ex - 4] = __builtin_amdgcn_exp2f(sp1[ag.ex - 4]); OPAQUE(sp1[ag.ex - 4]); }
              if constexpr (ag.ex >= 8) { s0[ag.ex - 8] = __builtin_amdgcn_exp2f(s0[ag.ex - 8]); OPAQUE(s0[ag.ex - 8]); }
              if constexpr (ag.cv & 1) { pn[0] = cvtpk(sp0[0], sp0[1]); OPAQUE(pn[0]); }
              if constexpr (ag.cv & 2) { pn[1] = cvtpk(sp0[2], sp0[3]); OPAQUE(pn[1]); }
              if constexpr (ag.cv & 4) { pn[2] = cvtpk(sp1[0], sp1[1]); OPAQUE(pn[2]); }
              if constexpr (ag.cv & 8) { pn[3] = cvtpk(sp1[2], sp1[3]); OPAQUE(pn[3]); }
            }
            if constexpr (ag.pc >= 0) { constexpr int p = ag.pc;
              if constexpr (p < 8) dma<1, 1024 * (p & 3)>(vk[p & 3], sKs, p < 4 ? so : so2); else dma<1, 1024 * (p & 3)>(vv[p & 1], sVs, p < 12 ? so : so2); }
            if constexpr (ag.m0 >= 0) setm0(mk + ag.m0 * 1024u);
            if constexpr (ag.bk == 1) { so2 = so + 4096u; SPIN(so2); }
            if constexpr (ag.bk == 2) { av += dl; OPAQUE(av); av1 += dl; OPAQUE(av1); }
            if constexpr (ag.bk == 3) { ak += dl; OPAQUE(ak); }
            if constexpr (i == 6 && g == 16) LGKM0();
            GAP();
          });
          if constexpr (ATN >= 0 && !safe) asm volatile("s_nop %c0" ::"i"(ATN));
          if constexpr (!(first && i == 0)) pp = __builtin_bit_cast(bf16x8, pn);
          sp0 = s0; sp1 = s1;
        });
        so = kvb | ((so + 8192u) & 0x1ffffu); mk += dl; dl = 0u - dl;
      };
      ak -= 16384u;
      step(0u, IC<2>{});
      { float mx = max5(sp0[0], sp0[1], sp0[2], sp0[3], sp1[0]); mx = max5(mx, sp1[1], sp1[2], sp1[3], sp1[3]); setref(IC<6>{}, mx); GAP(); }
      unsigned s = 1;
#pragma unroll 1
      for (; s < nsafe; ++s) step(s, IC<1>{});
#pragma unroll 1
      for (; s < 16u; ++s) step(s, IC<0>{});
      { float mx = max5(sp0[0], sp0[1], sp0[2], sp0[3], sp1[0]); mx = max5(mx, sp1[1], sp1[2], sp1[3], sp1[3]);
        check(IC<6>{}, __builtin_amdgcn_ballot_w64(mx > THR), mx); }
      NOPS(1);
      sfor<9>([&](auto D) {
        mfa<7 * D + 5>(D < 8 ? vf[D & 7] : ones, pp);
        GAP();
        constexpr int d = D;
        if constexpr (d < 4) { sp0[d] = __builtin_amdgcn_exp2f(sp0[d]); OPAQUE(sp0[d]); } else if constexpr (d < 8) { sp1[d - 4] = __builtin_amdgcn_exp2f(sp1[d - 4]); OPAQUE(sp1[d - 4]); }
        GAP();
      });
      pp = pack2(sp0, sp1, 1.f); OPAQUE(pp);
      NOPS(1);
      mfa<62>(ones, pp);
      sfor<8>([&](auto D) { mfa<7 * D + 6>(vf[D], pp); });
      bool bad = false;
      sfor<7>([&](auto Q) { lsum[Q] = acc_rd1<56 + Q>(); bad |= !(lsum[Q] < 1.0995116e12f); });
      if (__builtin_expect(nsafe == 16u || __builtin_amdgcn_ballot_w64(bad) == 0ull, 1)) break;
      nsafe = 16u;
      waitv<0>();
      sfor<32>([&](auto P) { constexpr int p = P; if constexpr ((p & 3) == 0) { setm0(ring + p * 1024u); NOPS(0); }
        if constexpr ((p >> 3) & 1) dma<1, 1024 * (p & 3)>(vv[p & 1], sV, kvs(p >> 4) + ((p >> 2) & 1) * 4096u);
        else dma<1, 1024 * (p & 3)>(vk[p & 3], sK, kvs(p >> 4) + ((p >> 2) & 1) * 4096u); });
    }
  }
  STAMP(4);
  {
    const unsigned l = lane_id(), g4 = l >> 4, q15 = l & 15u, q4 = (l >> 2) & 3u, p4 = l & 3u, drow = (l >> 2) & 7u;
    srd_t sO = __builtin_amdgcn_make_buffer_rsrc((char*)Wo - 4096, (short)0, (int)(W_BYTES + 4096u), 0x00020000);
    srd_t sP = __builtin_amdgcn_make_buffer_rsrc((char*)Pws + F_BYTES, (short)0, (int)P_BYTES, 0x00020000);
    unsigned vo[4];
#pragma unroll
    for (int i = 0; i < 4; ++i) vo[i] = (hg * 512u + 8u * i + drow) * 1280u + (w & 1u) * 640u + 64u * (l >> 5) + 16u * (p4 ^ (2u * (drow >> 2))) + 4096u - 1024u * i;
    const unsigned slot0 = ring + 28672u;
    const unsigned rot8 = (rot * 13u) >> 4, sg0 = early ? 0u : 8u;
    auto cchunk = [&](unsigned sg) { return (unsigned)(0x1023443210ull >> (4u * sg)) & 7u; };
    auto sbase = [&](unsigned sg) { return ((0x7e0u >> sg) & 1u) * 327680u + cchunk(sg) * 128u; };
    auto allO = [&](unsigned u) { setm0(slot0 + (u % 3u) * 4096u); NOPS(0); sfor<4>([&](auto P) { dma<0, 1024 * P>(vo[P], sO, sbase(sg0) + ((u + rot8) & 7u) * 40960u); }); };
    waitv<0>();
    wait_ge(fl + 16u + (l & 1u) * 4u, 11u);
    allO(0); allO(1);
    sfor<7>([&](auto Q) {
      constexpr int q = Q;
      const float inv = __builtin_amdgcn_rcpf(lsum[q]);
      sfor<4>([&](auto K) { constexpr int k = K; *(LAS bf16x8*)(size_t)(ring + l * 16u + (k * 7 + q) * 1024u) = pack2(acc_rd<14 * k + q>(), acc_rd<14 * k + 7 + q>(), inv); });
    });
    unsigned ba = slot0 + 1024u * (g4 >> 1) + 64u * (4u * (g4 & 1u) + q4) + 16u * ((p4 >> 1) ^ (2u * (g4 & 1u))) + 8u * (p4 & 1u), bb = ldsb + l * 16u;
    OPAQUE(ba); OPAQUE(bb);
    bf16x8 af[2][4], bf[2][7];
    if (early) { lds_st(fl + w * 4u, 1u); wait_ge(fl + (l & 1u) * 4u, 1u); }
    else { DBGX(STAMP(5);) LGKM0(); BAR(); DBGX(STAMP(6);) }
    allO(2);
    waitv<8>();
#pragma unroll
    for (int n = 0; n < 4; ++n) { const unsigned a = (ba ^ (32u * (n & 1))) + 512u * (n >> 1); af[0][n] = lds_tr2(a, a + 2048u); }
    LGKM0(); asm volatile("" ::: "memory");
    allO(3);
    unsigned k1 = (1u + rot8) & 7u, o3 = ((4u + rot8) & 7u) * 40960u, sl = 4096u;
    unsigned aa = ba + 4096u, aa1 = aa ^ 32u;
    OPAQUE(aa); OPAQUE(aa1);
    unsigned bs = bb; bool pfw = false;
    auto firstB = [&]() { const unsigned ab = bs + rot8 * 7168u + (rot8 & 4u) * 3072u;
#pragma unroll
      for (int q = 0; q < 7; ++q) bf[0][q] = lds_b128(ab + 1024u * q); };
    f32x4 oa[28];
    sfor<28>([&](auto T) { f32x4 u; asm volatile("" : "=v"(u)); oa[T] = u; });
    auto unpack = [&](auto R) {
      const unsigned ll = lane_id(), one = (ll >> 4) == ((ll >> 2) & 3u) ? (ll & 1u ? 0x3f800000u : 0x00003f80u) : 0u, lo = (ll & 2u) ? 0u : one, hi = (ll & 2u) ? one : 0u;
      bf16x8 sel[2] = {__builtin_bit_cast(bf16x8, u32x4{lo, hi, 0u, 0u}), __builtin_bit_cast(bf16x8, u32x4{0u, 0u, lo, hi})};
      OPAQUE(sel[0]); OPAQUE(sel[1]); NOPS(1);
      sfor<28>([&](auto T) { mfv0a<R + T / 2>(oa[T], sel[T & 1]); });
    };
    auto packed = [&](auto T) { return pack2(oa[2 * T], oa[2 * T + 1], 1.f); };
    auto gput = [&](unsigned idx, u64 val) {
      asm volatile("s_mov_b64 exec, 1\n\tglobal_store_dwordx2 %0, %1, %2 sc1\n\ts_mov_b64 exec, -1" ::"v"(idx * 8u), "v"(val), "s"(gfl) : "memory"); };
    auto pairU = [&](unsigned i, unsigned sc, unsigned sn, auto ZERO, auto AFTER, auto THROT) {
      {
        sfor<2>([&](auto E) {
          constexpr int e = E;
          unsigned ab = 0, ab2 = 0, so = 0; bf16x4 lo;
          sfor<28>([&](auto T) {
            constexpr int t = T, q = t / 4, n = t % 4;
            if constexpr (ZERO && e == 0) mfv00t(oa[t], af[e][n], bf[e][q]); else mfv(oa[t], af[e][n], bf[e][q]);
            GAP();
            if constexpr (t == 1) { aa1 = aa ^ 32u; OPAQUE(aa1); }
            if constexpr (t == 0) {
              if constexpr (AFTER) asm volatile("s_cmp_lt_u32 %0, %1\n\ts_cbranch_scc1 1f\n\ts_waitcnt vmcnt(8)\n1:\n\ts_waitcnt vmcnt(22)" ::"s"(i), "i"(2 - e) : "scc", "memory"); else waitv<8>();
            }
            if constexpr (t >= 1 && t <= 8) { constexpr int j = t - 1; const unsigned a = ((j & 2) ? aa1 : aa) + 512u * (j >> 2);
              if constexpr ((j & 1) == 0) lo = lds_tr(a); else af[e ^ 1][j >> 1] = cat(lo, lds_tr(a + 2048u)); }
            if constexpr (t == 9) { ab2 = (k1 & 4u) * 3072u; SPIN(ab2); }
            if constexpr (t == 10) { ab = k1 * 7168u + ab2; SPIN(ab); }
            if constexpr (t == 11) { ab += bs; OPAQUE(ab); setm0(slot0 + sl); }
            if constexpr (t == 12) { so = (i >> 1) * sn; SPIN(so); }
            if constexpr (t == 13) { so += o3 + sc; SPIN(so); LGKM0(); asm volatile("" ::: "memory"); }
            if constexpr ((t == 14 || t == 17 || t == 20 || t == 23) && HO < 63 && THROT) waitv<HO - 1>();
            if constexpr (t == 14 || t == 17 || t == 20 || t == 23) dma<0, 1024 * ((t - 14) / 3)>(vo[(t - 14) / 3], sO, so);
            if constexpr (t >= 15 && t <= 24 && (t - 14) % 3 != 0) { constexpr int qq = (t - 15) - (t - 14) / 3; bf[e ^ 1][qq] = lds_b128(ab + 1024u * qq); }
            if constexpr (t == 25) { sl = sl == 8192u ? 0u : sl + 4096u; SPIN(sl); }
            if constexpr (t == 26) { aa = ba + sl; OPAQUE(aa); k1 = (k1 + 1u) & 7u; SPIN(k1); }
            if constexpr (t == 27) { o3 = o3 == 286720u ? 0u : o3 + 40960u; SPIN(o3); }
            GAP();
          });
        });
      }
    };
    if (early) {
      firstB(); LGKM0();
      pfw = true;
#pragma unroll 1
      for (unsigned sg = 0; sg < 5u; ++sg) {
        const unsigned sc = sbase(sg), sn = sbase(sg + 1u) - sc;
        pairU(0u, sc, sn, std::true_type{}, std::false_type{}, std::true_type{});
#pragma unroll 1
        for (unsigned i = 1; i < 4; ++i) pairU(i, sc, sn, std::false_type{}, std::false_type{}, std::true_type{});
        NOPS(7); GAP();
        if (pfw && sg == 2u) {
          constexpr unsigned NBX = 128 / NHX, PER = 2560u / NBX / 2u;
          unsigned dummy = 0;
#pragma unroll
          for (unsigned k = 0; k < (PER + 63u) / 64u; ++k) {
            const unsigned li = k * 64u + lane_id();
            asm volatile("buffer_load_dword %0, %1, %2, 0 offen" : "+v"(dummy) : "v"(li < PER ? (hg * 512u + 256u) * 1280u + (((b % NBX) * 2u + w) * PER + li) * 128u + 4096u : NONE), "s"(sO) : "memory");
          }
          waitv<0>(); asm volatile("" :: "v"(dummy));
        }
        sfor<4>([&](auto C) { if (sg == C) sfor<14>([&](auto T) { acc_wr<14 * C + T>(__builtin_bit_cast(f32x4, packed(T))); }); });
      }
      lds_st(fl + w * 4u, 2u); wait_ge(fl + (lane_id() & 1u) * 4u, 2u);
      const unsigned a = bb + w * REG;
      sfor<28>([&](auto T) { acc_lds_wr<T, T * 1024>(a); });
      DBGX(STAMP(5);)
      asm volatile("s_waitcnt lgkmcnt(0)" ::: "memory"); BAR();
      DBGX(STAMP(6);)
    }
    unsigned w2 = w; SPIN(w2);
    if (w2 >= 2u) {
      const unsigned a = bb + (w & 1u) * REG;
      sfor<28>([&](auto T) { acc_lds_rd<28 + T, T * 1024>(a); });
      asm volatile("s_waitcnt lgkmcnt(0)" ::: "memory");
      unpack(IC<28>{});
    }
    bs = bb + 2u * REG; OPAQUE(bs);
    firstB();
    LGKM0();
    auto lastseg = [&](unsigned sg, auto AFTER) {
      const unsigned sc = sbase(sg), sn = sbase(sg + 1u) - sc;
#pragma unroll 1
      for (unsigned i = 0; i < 4; ++i) {
        pairU(i, sc, sn, std::false_type{}, AFTER, std::false_type{});
      }
      NOPS(7); GAP();
      const unsigned po = ((b * 10u + (w & 1u) * 5u + cchunk(sg)) * 8u + hg) * P_UNIT + lane_id() * 16u;
      sfor<14>([&](auto T) { __builtin_amdgcn_raw_buffer_store_b128(__builtin_bit_cast(u32x4, packed(T)), sP, po + T * 1024u, 0, 16); });
      GAP();
    };
    lastseg(early ? 5u : 8u, std::false_type{});
    unpack(IC<42>{});
    lastseg(early ? 6u : 9u, std::true_type{});
    if (early) {
      unpack(IC<28>{});
      lastseg(7u, std::true_type{});
    }
    waitv<0>();
    DBGX(STAMP(7);)
    BAR();
    const unsigned ll = lane_id();
    if (w == 0u) gput(hg, ep + 1ull);
    srd_t sOut = __builtin_amdgcn_make_buffer_rsrc(Out, (short)0, (int)X_BYTES, 0x00020000);
    u32x4 v[3][2][8];
    unsigned oo[3], pi[3];
#pragma unroll
    for (int j = 0; j < 3; ++j) {
      const unsigned u = hg + 8u * (w + 4u * j), ch = (u * 37u) >> 8, q = u - 7u * ch;
      pi[j] = u < 70u ? (b * 10u + ch) * 8u * P_UNIT + q * 2048u + ll * 16u : NONE;
      oo[j] = u < 70u ? (b * 112u + q * 16u + (ll & 15u)) * 1280u + ch * 128u + (ll >> 4) * 16u : NONE;
    }
    unsigned rdy = 1u << hg, sp = 0;
#pragma unroll
    for (int k = 0; k < 8; ++k) {
      const unsigned src = (hg + k) & 7u;
      while (!((rdy >> src) & 1u) && ++sp < SPIN_CAP) {
        rdy = (unsigned)__builtin_amdgcn_ballot_w64(__hip_atomic_load(gfl + (ll & 7u), __ATOMIC_RELAXED, __HIP_MEMORY_SCOPE_AGENT) == ep + 1ull);
        if (!((rdy >> src) & 1u)) __builtin_amdgcn_s_sleep(2);
      }
      asm volatile("" ::: "memory");
#pragma unroll
      for (int j = 0; j < 3; ++j) { v[j][0][k] = __builtin_amdgcn_raw_buffer_load_b128(sP, pi[j] + src * P_UNIT, 0, 16); v[j][1][k] = __builtin_amdgcn_raw_buffer_load_b128(sP, pi[j] + src * P_UNIT + 1024u, 0, 16); }
    }
    if (hg == 0u && w == 0u) gput(32u, ep + 1ull);
#pragma unroll
    for (int j = 0; j < 3; ++j) {
      if (j < 2 || w == 0u) {
#pragma unroll
        for (int r = 0; r < 2; ++r) {
          u32x4 o;
#pragma unroll
          for (int e = 0; e < 4; ++e) {
            float lo = __uint_as_float(v[j][r][0][e] << 16), hi = __uint_as_float(v[j][r][0][e] & 0xffff0000u);
#pragma unroll
            for (int k = 1; k < 8; ++k) { lo += __uint_as_float(v[j][r][k][e] << 16); hi += __uint_as_float(v[j][r][k][e] & 0xffff0000u); }
            o[e] = cvtpk(lo, hi);
          }
          __builtin_amdgcn_raw_buffer_store_b128(regroup(o), sOut, oo[j] + r * 64u, 0, 0);
        }
      }
    }
  }
}

extern "C" void kernel_launch(void* const* d_in, const int* in_sizes, int n_in, void* d_out, int out_size, void* d_ws, size_t ws_size,
                              hipStream_t stream) {
  (void)in_sizes; (void)n_in; (void)out_size; (void)ws_size;
  (void)hipFuncSetAttribute((const void*)fused_k, hipFuncAttributeMaxDynamicSharedMemorySize, (int)LDS_ALL);
  fused_k<<<256, 256, LDS_ALL, stream>>>(d_in[0], d_in[1], d_in[2], d_in[3], d_in[4], d_ws, d_out);
}
```

```cpp
#include <hip/hip_runtime.h>
#include <hip/hip_bf16.h>
#include <utility>

typedef __bf16 bf16x8 __attribute__((ext_vector_type(8)));
typedef __bf16 bf16x4 __attribute__((ext_vector_type(4)));
typedef __bf16 bf16x2 __attribute__((ext_vector_type(2)));
typedef float f32x4 __attribute__((ext_vector_type(4)));
typedef float f32x2 __attribute__((ext_vector_type(2)));
typedef unsigned u32x4 __attribute__((ext_vector_type(4)));
typedef unsigned u32x2 __attribute__((ext_vector_type(2)));
typedef unsigned long long u64;
typedef __amdgpu_buffer_rsrc_t srd_t;
#define LAS __attribute__((address_space(3)))
#define DI __device__ __forceinline__
#define GAP() __builtin_amdgcn_sched_barrier(0)
#define OPAQUE(x) asm volatile("" : "+v"(x))
#define SPIN(x) asm volatile("" : "+s"(x))
#define BAR() do { asm volatile("" ::: "memory"); __builtin_amdgcn_s_barrier(); asm volatile("" ::: "memory"); } while (0)

constexpr unsigned KV_BYTES = 134217728u, X_BYTES = 3584u * 1280u, W_BYTES = 640u * 8192u;
constexpr unsigned P_UNIT = 14336u, P_BYTES = 320u * 8u * P_UNIT;
constexpr unsigned F_BYTES = 32u * 512u;
constexpr unsigned REG = 40960u, LDS_ALL = 163840u, FLAGS = 3u * REG + 38912u;
constexpr unsigned SPIN_CAP = 1u << 22;
constexpr float QSCALE = 0.12751743082f;
constexpr float THR = 8.0f;
constexpr unsigned NONE = 0x80000000u;

#define STAMP(i)
#define DBGX(x)

DI unsigned lane_id() { unsigned r; asm volatile("v_mbcnt_lo_u32_b32 %0, -1, 0\n\tv_mbcnt_hi_u32_b32 %0, -1, %0" : "=v"(r)); return r; }
DI unsigned rfl(unsigned x) { return __builtin_amdgcn_readfirstlane(x); }
DI unsigned cvtpk(float lo, float hi) { f32x2 v = {lo, hi}; bf16x2 b = __builtin_convertvector(v, bf16x2); return __builtin_bit_cast(unsigned, b); }
template <int N> DI void waitv() { asm volatile("s_waitcnt vmcnt(%c0)" ::"i"(N) : "memory"); }
DI void setm0(unsigned v) { asm volatile("s_mov_b32 m0, %0" ::"s"(v) : "m0", "memory"); }
template <int NT, int OFF> DI void dma(unsigned v, srd_t srd, unsigned soff) {
  if constexpr (NT) asm volatile("buffer_load_dwordx4 %0, %1, %2 offen offset:%c3 nt lds" ::"v"(v), "s"(srd), "s"(soff), "i"(OFF) : "memory");
  else asm volatile("buffer_load_dwordx4 %0, %1, %2 offen offset:%c3 lds" ::"v"(v), "s"(srd), "s"(soff), "i"(OFF) : "memory");
}
#define LGKM0() __builtin_amdgcn_s_waitcnt(0xC07F)
DI bf16x8 lds_b128(unsigned a) { return *(const LAS bf16x8*)(size_t)a; }
DI bf16x4 lds_tr(unsigned a) { return __builtin_amdgcn_ds_read_tr16_b64_v4bf16((LAS bf16x4*)(size_t)a); }
DI bf16x8 cat(bf16x4 lo, bf16x4 hi) { return __builtin_shufflevector(lo, hi, 0, 1, 2, 3, 4, 5, 6, 7); }
DI bf16x8 lds_tr2(unsigned a0, unsigned a1) {
  bf16x4 lo = __builtin_amdgcn_ds_read_tr16_b64_v4bf16((LAS bf16x4*)(size_t)a0);
  bf16x4 hi = __builtin_amdgcn_ds_read_tr16_b64_v4bf16((LAS bf16x4*)(size_t)a1);
  return __builtin_shufflevector(lo, hi, 0, 1, 2, 3, 4, 5, 6, 7);
}
DI bf16x8 pack2(const f32x4& a, const f32x4& b, float s) {
  u32x4 w = {cvtpk(a[0] * s, a[1] * s), cvtpk(a[2] * s, a[3] * s), cvtpk(b[0] * s, b[1] * s), cvtpk(b[2] * s, b[3] * s)};
  return __builtin_bit_cast(bf16x8, w);
}
DI u32x4 regroup(u32x4 v) {
  auto a = __builtin_amdgcn_permlane32_swap(v[0], v[2], false, false), b = __builtin_amdgcn_permlane32_swap(v[1], v[3], false, false);
  auto c = __builtin_amdgcn_permlane16_swap(a[0], a[1], false, false), d = __builtin_amdgcn_permlane16_swap(b[0], b[1], false, false);
  return u32x4{c[0], d[0], c[1], d[1]};
}
DI float col_max(float v) {
  auto r = __builtin_amdgcn_permlane16_swap(__float_as_uint(v), __float_as_uint(v), false, false);
  v = fmaxf(__uint_as_float(r[0]), __uint_as_float(r[1]));
  auto s = __builtin_amdgcn_permlane32_swap(__float_as_uint(v), __float_as_uint(v), false, false);
  return fmaxf(__uint_as_float(s[0]), __uint_as_float(s[1]));
}
DI float col_sum(float v) {
  auto r = __builtin_amdgcn_permlane16_swap(__float_as_uint(v), __float_as_uint(v), false, false);
  v = __uint_as_float(r[0]) + __uint_as_float(r[1]);
  auto s = __builtin_amdgcn_permlane32_swap(__float_as_uint(v), __float_as_uint(v), false, false);
  return __uint_as_float(s[0]) + __uint_as_float(s[1]);
}

template <class F, int... Is> DI void sfor_(F&& f, std::integer_sequence<int, Is...>) { (f(std::integral_constant<int, Is>{}), ...); }
template <int N, class F> DI void sfor(F&& f) { sfor_(f, std::make_integer_sequence<int, N>{}); }
template <int T> DI void mfa(bf16x8 a, bf16x8 b) { asm volatile("v_mfma_f32_16x16x32_bf16 a[%c2:%c3], %0, %1, a[%c2:%c3]" ::"v"(a), "v"(b), "i"(4 * T + 4), "i"(4 * T + 7)); }
template <int T> DI void mfa0(bf16x8 a, bf16x8 b) { asm volatile("v_mfma_f32_16x16x32_bf16 a[%c2:%c3], %0, %1, 0" ::"v"(a), "v"(b), "i"(4 * T + 4), "i"(4 * T + 7)); }
DI float max5(float a, float b, float c, float d, float e) { float r; asm volatile("v_max3_f32 %0, %1, %2, %3\n\tv_max3_f32 %0, %0, %4, %5" : "=&v"(r) : "v"(a), "v"(b), "v"(c), "v"(d), "v"(e)); return r; }
template <int T, int R> DI void mfa0a(bf16x8 a) { asm volatile("v_mfma_f32_16x16x32_bf16 a[%c1:%c2], %0, a[%c3:%c4], 0" ::"v"(a), "i"(4 * T + 4), "i"(4 * T + 7), "i"(4 * R + 4), "i"(4 * R + 7)); }
DI unsigned lds_ld(unsigned a) { return *(volatile LAS unsigned*)(size_t)a; }
DI void lds_st(unsigned a, unsigned v) { *(volatile LAS unsigned*)(size_t)a = v; }
DI void wait_ge(unsigned a, unsigned need) {
  unsigned sp = 0;
  while (__builtin_amdgcn_ballot_w64(lds_ld(a) < need) != 0ull && ++sp < SPIN_CAP) __builtin_amdgcn_s_sleep(1);
  asm volatile("" ::: "memory");
}
DI void mfv(f32x4& d, bf16x8 a, bf16x8 b) { asm volatile("v_mfma_f32_16x16x32_bf16 %0, %1, %2, %0" : "+v"(d) : "v"(a), "v"(b)); }
DI f32x4 mfv0(bf16x8 a, bf16x8 b, f32x4 c) { f32x4 d; asm volatile("v_mfma_f32_16x16x32_bf16 %0, %1, %2, %3" : "=&v"(d) : "v"(a), "v"(b), "v"(c)); return d; }
DI f32x4 mfv00(bf16x8 a, bf16x8 b) { f32x4 d; asm volatile("v_mfma_f32_16x16x32_bf16 %0, %1, %2, 0" : "=&v"(d) : "v"(a), "v"(b)); return d; }
template <int R> DI void mfv0a(f32x4& d, bf16x8 a) { asm volatile("v_mfma_f32_16x16x32_bf16 %0, %1, a[%c2:%c3], 0" : "+v"(d) : "v"(a), "i"(4 * R + 4), "i"(4 * R + 7)); }
DI void mfv00t(f32x4& d, bf16x8 a, bf16x8 b) { asm volatile("v_mfma_f32_16x16x32_bf16 %0, %1, %2, 0" : "+v"(d) : "v"(a), "v"(b)); }
template <int T> DI float acc_rd1() { float x; asm volatile("v_accvgpr_read_b32 %0, a%c1" : "=v"(x) : "i"(4 * T + 4)); return x; }
template <int T, int OFF> DI void acc_lds_wr(unsigned a) { asm volatile("ds_write_b128 %0, a[%c1:%c2] offset:%c3" ::"v"(a), "i"(4 * T + 4), "i"(4 * T + 7), "i"(OFF) : "memory"); }
template <int T, int OFF> DI void acc_lds_rd(unsigned a) { asm volatile("ds_read_b128 a[%c1:%c2], %0 offset:%c3" ::"v"(a), "i"(4 * T + 4), "i"(4 * T + 7), "i"(OFF) : "memory"); }
template <int T> DI f32x4 acc_rd() {
  float x0, x1, x2, x3;
  asm volatile("v_accvgpr_read_b32 %0, a%c4\n\tv_accvgpr_read_b32 %1, a%c5\n\tv_accvgpr_read_b32 %2, a%c6\n\tv_accvgpr_read_b32 %3, a%c7"
               : "=v"(x0), "=v"(x1), "=v"(x2), "=v"(x3) : "i"(4 * T + 4), "i"(4 * T + 5), "i"(4 * T + 6), "i"(4 * T + 7));
  return f32x4{x0, x1, x2, x3};
}
template <int T> DI void acc_wr(f32x4 v) {
  asm volatile("v_accvgpr_write_b32 a%c4, %0\n\tv_accvgpr_write_b32 a%c5, %1\n\tv_accvgpr_write_b32 a%c6, %2\n\tv_accvgpr_write_b32 a%c7, %3"
               ::"v"(v[0]), "v"(v[1]), "v"(v[2]), "v"(v[3]), "i"(4 * T + 4), "i"(4 * T + 5), "i"(4 * T + 6), "i"(4 * T + 7));
}
template <int T> DI void acc_zero() {
  asm volatile("v_accvgpr_write_b32 a%c0, 0\n\tv_accvgpr_write_b32 a%c1, 0\n\tv_accvgpr_write_b32 a%c2, 0\n\tv_accvgpr_write_b32 a%c3, 0"
               ::"i"(4 * T + 4), "i"(4 * T + 5), "i"(4 * T + 6), "i"(4 * T + 7));
}
#define NOPS(n) asm volatile("s_nop " #n)
template <int V> using IC = std::integral_constant<int, V>;

#define HQ 14
#define HO 12
#define ATN 12
#define KROTM 15u
#define FORCE_SAFE false
constexpr const char* ATT_FAST[7] = {"m0123p4567CDabcdp", "4tt5tt6tt7ttCptDt", "ttt0t1tt2345p67CD", "m0p12p34B5p67pCDm", "p01p23A45p67.pCDm", "p0123p4567CDpab.c", "3kk4kk5kk6kk7pCD."};
constexpr const char* ATT_SAFE[7] = {"xyzmw01234567CDpp", "xyz.w01234567CD..", "xyzpw01234567CDpm", "xyzpw01234567CDpp", "xyzpw01234567CDpp", "xyzpw01234567CDpp", "xyzpw01234567CDpp"};
struct AttGap { int ex, cv, tr, kr, pc, m0, bk; char c; };
constexpr AttGap att_gap(bool safe, int i, int g) {
  const char* const* tb = safe ? ATT_SAFE : ATT_FAST;
  AttGap r{-1, 0, -1, -1, -1, -1, 0, tb[i][g]};
  int nt = 0, nk = 0, np = 0;
  for (int k = 0; k < 17 * i + g; ++k) { const char c = tb[k / 17][k % 17]; nt += c == 't'; nk += c == 'k'; np += c == 'p'; }
  const char c = r.c;
  if (c >= '0' && c <= '7') r.ex = c - '0';
  if (c >= 'a' && c <= 'd') r.ex = 8 + c - 'a';
  if (c == 'C') r.cv = 3;
  if (c == 'D') r.cv = 12;
  if (c == 't') r.tr = nt;
  if (c == 'k') r.kr = nk;
  if (c == 'p') r.pc = np;
  if (c == 'm') { r.m0 = np; if (np == 0) r.bk = 1; }
  if (c == 'B') r.bk = 2;
  if (c == 'A') r.bk = 3;
  if (safe) {
    if (i == 1 && g >= 8) r.tr = g - 8;
    if (i == 2 && g < 7) r.tr = g + 9;
    if (i == 6 && g < 8) r.kr = g;
    if (i == 4 && g == 14) r.m0 = 8;
    if (i == 5 && g == 15) r.m0 = 12;
    if (i == 3 && g == 4) r.bk = 2;
    if (i == 1 && g == 3) r.bk = 3;
  }
  return r;
}
constexpr int att_pieces_before(bool safe, int i, int g) { int n = 0; for (int k = 0; k < 17 * i + g; ++k) n += att_gap(safe, k / 17, k % 17).pc >= 0; return n; }
static_assert(att_pieces_before(true, 7, 0) == 16 && att_pieces_before(false, 7, 0) == 16, "pieces");

#define NHX 8
DI void map_block(unsigned g, unsigned& b, unsigned& hg, unsigned& sh) {
  constexpr unsigned HG = NHX / 4, NXH = 32 / NHX, NBX = 32 / HG;
  const unsigned xcd = g & 7u, sl = g >> 3, j = sl % HG, bi = sl / HG;
  b = (xcd / NXH) * NBX + bi; hg = (xcd % NXH) * HG + j;
  sh = ((bi * 10u) / NBX + (j * 10u) / HG) % 10u;
}

__global__ __launch_bounds__(256, 1) void fused_k(const void* __restrict__ Xp, const void* __restrict__ Kp, const void* __restrict__ Vp,
                                                  const void* __restrict__ Wq, const void* __restrict__ Wo, void* __restrict__ Pws,
                                                  void* __restrict__ Out) {
  extern __shared__ __attribute__((aligned(16))) char lds[];
  const unsigned ldsb = (unsigned)(size_t)lds;
  const unsigned l = threadIdx.x & 63u, w = rfl(threadIdx.x >> 6);
  const bool early = w < 2u;
  unsigned b, hg, rot;
  map_block(blockIdx.x, b, hg, rot);
  const unsigned h = 4u * hg + w;
  const unsigned g4 = l >> 4, q15 = l & 15u, q4 = (l >> 2) & 3u, p4 = l & 3u;
  DBGX(unsigned seg[8]; unsigned segr[8];)
  STAMP(0);
  {
    u64 pc; int len;
    asm volatile("s_getpc_b64 %0\n.Lgqa_code_begin:\n\ts_mov_b32 %1, .Lgqa_code_end-.Lgqa_code_begin" : "=s"(pc), "=s"(len));
    srd_t sC = __builtin_amdgcn_make_buffer_rsrc((void*)pc, (short)0, len, 0x00020000);
    setm0(ldsb + w * REG + 32768u); NOPS(0);
    asm volatile("buffer_load_dword %0, %1, 0 offen lds" ::"v"((((blockIdx.x >> 3) * 4u + w) * 4u + (l & 3u)) * 128u), "s"(sC) : "memory");
  }
  u64* const gfl = (u64*)Pws + b * 64u;
  u32x2 epv;
  srd_t sK = __builtin_amdgcn_make_buffer_rsrc((char*)Kp - 4096, (short)0, (int)(KV_BYTES + 4096u), 0x00020000);
  srd_t sV = __builtin_amdgcn_make_buffer_rsrc((char*)Vp - 4096, (short)0, (int)(KV_BYTES + 4096u), 0x00020000);
  srd_t sX = __builtin_amdgcn_make_buffer_rsrc((char*)Xp - 4096, (short)0, (int)(X_BYTES + 4096u), 0x00020000);
  srd_t sW = __builtin_amdgcn_make_buffer_rsrc((char*)Wq - 4096, (short)0, (int)(W_BYTES + 4096u), 0x00020000);
  const unsigned ring = ldsb + w * REG;
  const unsigned fl = ldsb + FLAGS;
  const unsigned drow = (l >> 2) & 7u, dsub = l >> 5;
  unsigned kh = (blockIdx.x * 4u + w) * 2654435761u; kh ^= kh >> 15; kh *= 2246822519u; kh ^= kh >> 13;
  const unsigned kvb = (b * 32u + h) * 131072u, krot = (kh & KROTM) * 8192u;
  auto kvs = [&](unsigned s) { return kvb + ((krot + s * 8192u) & 0x1ffffu); };
  unsigned vk[4], vv[2];
#pragma unroll
  for (int i = 0; i < 4; ++i) vk[i] = drow * 256u + 64u * dsub + 16u * (p4 ^ (i & 2u)) + ((i & 1) ? 3200u : 4096u);
#pragma unroll
  for (int i = 0; i < 2; ++i) vv[i] = drow * 256u + 64u * dsub + 16u * (p4 ^ (2u * (drow >> 2))) + (i ? 3200u : 4096u);
  auto chunk = [&](unsigned i) { unsigned c = i + rot; return c >= 10u ? c - 10u : c; };
  auto xoff = [&](unsigned rx, int i) {
    return ((b * 112u + rx) * 1280u + 64u * dsub + 16u * (p4 ^ (2u * (i & 1))) + 4096u - 1024u * i) | (((rx + 16u) >> 7) << 31); };
  unsigned bx = ldsb + 1024u * (q15 >> 3) + 64u * (q15 & 7u) + 16u * (g4 ^ (2u * (q15 >> 3)));
  OPAQUE(bx);
  auto xfrag = [&](unsigned a0, unsigned a1, int q) { return lds_b128((q < 4 ? a0 : a1) + ((q >> 1) & 1) * REG + (q & 1) * 2048u); };
  unsigned vw[4];
#pragma unroll
  for (int i = 0; i < 4; ++i) vw[i] = (8u * (i >> 1) + drow) * 8192u + 128u * (i & 1) + 64u * dsub + 16u * p4 + h * 256u + 4096u - 1024u * i;

  asm volatile("" ::: "a255");
  bf16x8 qf[4][7];
  u64 ep;
  const unsigned hb = ldsb + (w & 1u) * REG + 24576u + l * 16u;
  {
    unsigned v1[4], vx[4];
#pragma unroll
    for (int i = 0; i < 4; ++i) {
      v1[i] = (8u * i + drow) * 8192u + 64u * dsub + 16u * (p4 ^ (2u * (i & 1))) + (4u * hg + (w & 1u)) * 256u + (w >> 1) * 128u + 4096u - 1024u * i;
      vx[i] = xoff(32u * w + 8u * i + drow, i);
    }
    const unsigned mx0 = ring + 24576u;
    auto allW = [&](unsigned s) { setm0(ring + (((s >> 1) + 2u) % 3u) * 8192u + (s & 1u) * 4096u); NOPS(0);
      sfor<4>([&](auto P) { dma<0, 1024 * P>(v1[P], sW, (2u * chunk(s >> 1) + (s & 1u)) * 262144u); }); };
    auto allX = [&](unsigned i) { setm0(mx0 + ((i + 2u) & 3u) * 4096u); NOPS(0); sfor<4>([&](auto P) { dma<0, 1024 * P>(vx[P], sX, chunk(i) * 128u); }); };
    unsigned bw = ring + 1024u * g4 + 64u * q4 + 16u * ((p4 >> 1) ^ (2u * (g4 & 1u))) + 8u * (p4 & 1u);
    OPAQUE(bw);
    f32x4 qa[28];
    allX(0); allW(0); allW(1);
    asm volatile("global_load_dwordx2 %0, %1, %2 offset:256 sc1" : "=v"(epv) : "v"(0u), "s"(gfl) : "memory");
    sfor<28>([&](auto T) { qa[T] = f32x4{}; OPAQUE(qa[T]); });
    allX(1); allW(2); allW(3); allX(2); allW(4); allW(5);
    bf16x8 af[2][4], bf[2][7];
    waitv<29>();
    BAR();
    STAMP(1);
#pragma unroll
    for (int d = 0; d < 4; ++d) { const unsigned a = ((bw + 16384u) ^ (32u * (d & 1))) + 512u * (d >> 1); af[0][d] = lds_tr2(a, a + 256u); }
#pragma unroll
    for (int q = 0; q < 7; ++q) bf[0][q] = xfrag(bx + 32768u, bx + 32768u + 2u * REG, q);
    LGKM0();
    unsigned sl = 16384u, xb = 8192u, mx = mx0 + 4096u, cx = rfl(chunk(3)) * 128u;
    unsigned aw = bw + 16384u + 4096u, aw1 = aw ^ 32u, ax = bx + 32768u + 512u, ax2 = ax + 2u * REG;
    OPAQUE(aw); OPAQUE(aw1); OPAQUE(ax); OPAQUE(ax2);
    srd_t sT = sW; unsigned so = 0, hs = 0;
    auto round = [&](auto TAIL) {
      constexpr bool tail = TAIL;
      sfor<2>([&](auto E) {
        constexpr int e = E;
        bf16x4 lo;
        sfor<28>([&](auto T) {
          constexpr int t = T, d = t / 7, q = t % 7;
          mfv(qa[t], af[e][d], bf[e][q]);
          GAP();
          if constexpr (t == 0) { if constexpr (e == 0) waitv<(tail ? 16 : 24)>(); else { waitv<(tail ? 12 : 18)>(); BAR(); } }
          constexpr const char* plan = "sttmbtptbtbptbtbptbpxbqAqBC.";
          constexpr char c = plan[t];
          constexpr auto before = [](char k, int n) { int r = 0; for (int i = 0; i < n; ++i) r += plan[i] == k; return r; };
          if constexpr (c == 's' && !tail) { so = e == 0 ? cx * 4096u : so + 262144u; SPIN(so); }
          if constexpr (c == 's' && tail && e == 1) { so += hs; SPIN(so); }
          if constexpr (c == 'm') setm0(ring + sl + e * 4096u);
          if constexpr (c == 't') { constexpr int j = before('t', t); const unsigned a = ((j & 2) ? aw1 : aw) + 512u * (j >> 2);
            if constexpr ((j & 1) == 0) lo = lds_tr(a); else af[e ^ 1][j >> 1] = cat(lo, lds_tr(a + 256u)); }
          if constexpr (c == 'b') { constexpr int qq = before('b', t); bf[e ^ 1][qq] = xfrag(ax, ax2, qq); }
          if constexpr (c == 'p') { constexpr int p = before('p', t); if constexpr (tail) dma<1, 1024 * p>(v1[p], sT, so); else dma<0, 1024 * p>(v1[p], sW, so); }
          if constexpr (c == 'x') { if constexpr (!tail) setm0(mx); if constexpr (e == 1) { sl = sl == 16384u ? 0u : sl + 8192u; SPIN(sl); } }
          if constexpr (c == 'q' && !tail) { constexpr int p = 2 * e + before('q', t); dma<0, 1024 * p>(vx[p], sX, cx); }
          if constexpr (c == 'A') { if constexpr (e == 0) { unsigned sn = sl == 16384u ? 0u : sl + 8192u; SPIN(sn); aw = bw + sn; } else aw += 4096u; OPAQUE(aw); }
          if constexpr (c == 'B') { aw1 = aw ^ 32u; OPAQUE(aw1); if constexpr (e == 0) { xb = (xb + 4096u) & 12288u; SPIN(xb); } else { cx = cx == 1152u ? 0u : cx + 128u; SPIN(cx); } }
          if constexpr (c == 'C') { if constexpr (e == 0) ax = bx + 24576u + xb; else ax += 512u; OPAQUE(ax); ax2 = ax + 2u * REG; OPAQUE(ax2); }
          if constexpr (t == 27) { LGKM0(); if constexpr (e == 1) { mx = mx0 + ((xb + 12288u) & 12288u); SPIN(mx); } }
          GAP();
        });
      });
    };
#pragma unroll 1
    for (unsigned i = 0; i < 7; ++i) round(std::false_type{});
#pragma unroll 1
    for (unsigned j = 0; j < 3; ++j) {
      if (early) {
#pragma unroll
        for (int k = 0; k < 4; ++k) v1[k] = j == 1u ? vv[k & 1] : vk[k];
        sT = j == 1u ? sV : sK; so = kvs(j >> 1); hs = 4096u;
      } else {
#pragma unroll
        for (int k = 0; k < 4; ++k) v1[k] = vw[k];
        so = (2u * chunk(8u + (j >> 1)) + (j & 1u)) * 262144u; hs = 131072u;
      }
      round(std::true_type{});
    }
    OPAQUE(epv);
    ep = ((u64)rfl(epv[1]) << 32) | rfl(epv[0]);
    NOPS(7);
    GAP();
    sfor<14>([&](auto T) { constexpr int k = T / 7, q = T % 7; qf[k][q] = __builtin_bit_cast(bf16x8, regroup(__builtin_bit_cast(u32x4, pack2(qa[14 * k + q], qa[14 * k + 7 + q], QSCALE)))); OPAQUE(qf[k][q]); });
    if (!early) {
      sfor<14>([&](auto T) { *(LAS bf16x8*)(size_t)(hb + (T / 7) * 2u * REG + (T % 7) * 1024u) = qf[T / 7][T % 7]; });
      if (w == 3u && l < 8u) lds_st(fl + l * 4u, 0u);
    }
    LGKM0(); BAR();
  }
  STAMP(2);
  if (early) {
    sfor<14>([&](auto T) { qf[2 + T / 7][T % 7] = lds_b128(hb + (T / 7) * 2u * REG + (T % 7) * 1024u); });
    LGKM0(); BAR();
    sfor<8>([&](auto P) { if constexpr ((P & 3) == 0) { setm0(ring + 24576u + P * 1024u); NOPS(0); } dma<1, 1024 * (P & 3)>(vv[P & 1], sV, kvs(1u) + (P >> 2) * 4096u); });
  } else {
    BAR();
    unsigned vx[8];
#pragma unroll
    for (int i = 0; i < 8; ++i) vx[i] = xoff(64u * (w & 1u) + 8u * i + drow, i & 3);
    const unsigned mx0 = ldsb + (w & 1u) * 2u * REG + 32768u;
    const unsigned myf = fl + 16u + (w & 1u) * 4u, otf = fl + 16u + ((w & 1u) ^ 1u) * 4u;
    unsigned bw = ring + 2048u * g4 + 64u * q4 + 16u * p4;
    OPAQUE(bw);
    sfor<8>([&](auto P) { if constexpr ((P & 3) == 0) { setm0(ring + 24576u + P * 1024u); NOPS(0); } dma<0, 1024 * (P & 3)>(vw[P & 3], sW, (2u * chunk(9) + 1u) * 262144u + (P >> 2) * 131072u); });
    sfor<56>([&](auto T) { acc_zero<T>(); });
    waitv<24>();
    bf16x8 af[2][8], bf[2][7];
#pragma unroll
    for (int d = 0; d < 8; ++d) { const unsigned a = bw + 512u * (d >> 1) + 8u * (d & 1); af[0][d] = lds_tr2(a, a + 256u); if (d < 7) bf[0][d] = xfrag(bx + 32768u, bx + 32768u + 2u * REG, d); }
    LGKM0();
    unsigned par = 0, cn = chunk(0), rn = 1;
    unsigned aw = bw + 8192u, ax = bx + 32768u + 512u, ax2 = ax + 2u * REG, rv = 1;
    OPAQUE(aw); OPAQUE(ax); OPAQUE(ax2); OPAQUE(rv);
    auto round = [&](auto TAIL) {
      constexpr bool tail = TAIL;
      unsigned so = tail ? kvs(par) : cn * 524288u, so2 = 0;
      sfor<2>([&](auto E) {
        constexpr int e = E;
        bf16x4 lo;
        unsigned awn = 0, axn = 0, pv = 0; u64 bad = 0;
        sfor<56>([&](auto T) {
          constexpr int t = T, d = t / 7, q = t % 7;
          mfa<t>(af[e][d], bf[e][q]);
          if constexpr (t == 0) { if constexpr (e == 0) waitv<16>(); else waitv<8>(); }
          if constexpr (e == 1) {
            if constexpr (t == 2) lds_st(myf, rv);
            if constexpr (t == 8) pv = lds_ld(otf);
            if constexpr (t == 14) { bad = __builtin_amdgcn_ballot_w64(pv < rn); SPIN(bad); }
            if constexpr (t == 17) { GAP(); if (__builtin_expect(bad != 0ull, 0)) wait_ge(otf, rn); asm volatile("" ::: "memory"); }
          }
          if constexpr (t % 3 == 1 && t / 3 < 16) {
            constexpr int j = t / 3, dd = j >> 1; const unsigned a = aw + 512u * (dd >> 1) + 8u * (dd & 1);
            if constexpr ((j & 1) == 0) lo = lds_tr(a); else af[e ^ 1][dd] = cat(lo, lds_tr(a + 256u));
          }
          if constexpr (t % 3 == 2 && t >= 20 && t <= 44 && t != 26 && t != 38) { constexpr int qq = (t - 20) / 3 - (t > 26) - (t > 38); bf[e ^ 1][qq] = xfrag(ax, ax2, qq); }
          constexpr int np = (e == 1 && !tail) ? 16 : 8;
          constexpr auto gap_of = [](int p) { return 3 + 3 * ((p * 16) / np); };
          sfor<np>([&](auto P) {
            constexpr int p = P;
            if constexpr ((p & 3) == 0 && t == gap_of(p) - 1) setm0(p >= 8 ? mx0 + par * 4096u + ((p - 8) >> 2) * REG : ring + par * 16384u + e * 8192u + p * 1024u);
            if constexpr (t == gap_of(p)) {
              if constexpr (HQ < 63) waitv<HQ - 1>();
              if constexpr (p >= 14) { if (w == 2u) dma<0, 1024 * (p & 3)>(vx[p & 7], sX, cn * 128u); }
              else if constexpr (p >= 8) dma<0, 1024 * (p & 3)>(vx[p & 7], sX, cn * 128u);
              else if constexpr (!tail) dma<0, 1024 * (p & 3)>(vw[p & 3], sW, p < 4 ? so : so2);
              else if constexpr (e == 0) dma<1, 1024 * (p & 3)>(vk[p & 3], sK, p < 4 ? so : so2);
              else dma<1, 1024 * (p & 3)>(vv[p & 1], sV, p < 4 ? so : so2);
            }
          });
          if constexpr (t == 5) { so2 = so + (tail ? 4096u : 131072u); SPIN(so2); }
          if constexpr (t == 47) { awn = e == 0 ? bw + (par ^ 1u) * 16384u : aw + 8192u; OPAQUE(awn); }
          if constexpr (t == 50) { axn = e == 0 ? bx + 32768u + (par ^ 1u) * 4096u : ax + 512u; OPAQUE(axn); }
          if constexpr (t == 52) { ax2 = axn + 2u * REG; OPAQUE(ax2); }
          if constexpr (t == 53 && e == 1) { rv += 1u; OPAQUE(rv); }
          if constexpr (t == 55) { LGKM0(); aw = awn; ax = axn; if constexpr (e == 0 && !tail) { so += 262144u; SPIN(so); } }
          GAP();
        });
      });
      par ^= 1u; cn = cn == 9u ? 0u : cn + 1u; rn += 1u;
    };
#pragma unroll 1
    for (unsigned i = 0; i < 8; ++i) round(std::false_type{});
#pragma unroll 1
    for (unsigned i = 8; i < 10; ++i) round(std::true_type{});
    lds_st(myf, 11u);
    NOPS(7);
    sfor<28>([&](auto T) { constexpr int k = T / 7, q = T % 7; qf[k][q] = pack2(acc_rd<14 * k + q>(), acc_rd<14 * k + 7 + q>(), QSCALE); OPAQUE(qf[k][q]); });
  }
  STAMP(3);
  float lsum[7];
  {
    bf16x8 ones = __builtin_bit_cast(bf16x8, u32x4{0x3f803f80u, 0x3f803f80u, 0x3f803f80u, 0x3f803f80u});
    OPAQUE(ones);
    unsigned nsafe = FORCE_SAFE ? 16u : 1u;
#pragma unroll 1
    for (;;) {
      const unsigned l = lane_id(), g4 = l >> 4, q15 = l & 15u, q4 = (l >> 2) & 3u, p4 = l & 3u;
      unsigned ak = ring + 2048u * (q15 >> 3) + 64u * (q15 & 7u) + 16u * (g4 ^ (2u * (q15 >> 3)));
      unsigned av = ring + 8192u + 2048u * (g4 >> 1) + 64u * (4u * (g4 & 1u) + q4) + 16u * ((p4 >> 1) ^ (2u * (g4 & 1u))) + 8u * (p4 & 1u), av1 = av ^ 32u;
      OPAQUE(ak); OPAQUE(av); OPAQUE(av1);
      bf16x8 kf[2][4], vf[8], pp;
      f32x4 sp0, sp1, nm[7];
      waitv<24>();
#pragma unroll
      for (int e = 0; e < 8; ++e) kf[e >> 2][e & 3] = lds_b128(ak + 4096u * (e >> 2) + 512u * (e & 3));
      ak += 16384u;
      auto check = [&](auto Q, u64 big, float mx) {
        constexpr int q = Q;
        if (__builtin_expect(big != 0ull, 0)) {
          const float d = fmaxf(col_max(mx), 0.f), al = __builtin_amdgcn_exp2f(-d);
          NOPS(7);
          sfor<9>([&](auto T) { acc_wr<7 * T + q>(acc_rd<7 * T + q>() * al); });
          nm[q] -= d; sp0 -= d; sp1 -= d;
          NOPS(1);
        }
      };
      auto setref = [&](auto Q, float mx) { constexpr int q = Q; const float d = col_max(mx); nm[q] = f32x4{-d, -d, -d, -d}; OPAQUE(nm[q]); sp0 -= d; sp1 -= d; };
      unsigned so = kvs(2u), mk = ring, dl = 16384u;
      auto step = [&](unsigned s, auto MODE) {
        constexpr bool safe = MODE != 0, first = MODE == 2;
        const int nrec = s < 14u ? (int)(KV_BYTES + 4096u) : 0;
        srd_t sKs = __builtin_amdgcn_make_buffer_rsrc((char*)Kp - 4096, (short)0, nrec, 0x00020000);
        srd_t sVs = __builtin_amdgcn_make_buffer_rsrc((char*)Vp - 4096, (short)0, nrec, 0x00020000);
        unsigned so2 = 0; bf16x4 lo;
        sfor<7>([&](auto I) {
          constexpr int i = I, qs = (i + 6) % 7, qp = (i + 5) % 7;
          f32x4 s0, s1; float mx; u64 big; u32x4 pn;
          sfor<17>([&](auto G) {
            constexpr int g = G;
            constexpr AttGap ag = att_gap(safe, i, g);
            constexpr bool rev = !safe && i == 1;
            constexpr int m = rev ? (g < 9 ? g + 8 : g - 9) : g;
            if constexpr (g == 0 && i == 6) waitv<8 + att_pieces_before(safe, 6, 0)>();
            if constexpr (g == (safe ? 8 : 0) && i == 1) waitv<16 + att_pieces_before(safe, 1, g)>();
            if constexpr (m < 2 && first) (m ? s1 : s0) = mfv00(kf[m][0], qf[0][i]);
            else if constexpr (m < 2) (m ? s1 : s0) = mfv0(kf[m][0], qf[0][i], nm[i]);
            else if constexpr (m < 8) mfv((m & 1) ? s1 : s0, kf[m & 1][m >> 1], qf[m >> 1][i]);
            else if constexpr (first && i < 2) acc_zero<7 * (m - 8) + qp>();
            else if constexpr (first) mfa0<7 * (m - 8) + qp>(m < 16 ? vf[(m - 8) & 7] : ones, pp);
            else mfa<7 * (m - 8) + qp>(m < 16 ? vf[(m - 8) & 7] : ones, pp);
            GAP();
            if constexpr (ag.kr >= 0) kf[ag.kr & 1][ag.kr >> 1] = lds_b128(ak + 4096u * (ag.kr & 1) + 512u * (ag.kr >> 1));
            if constexpr (ag.tr >= 0) { constexpr int d = ag.tr >> 1; const unsigned a = ((d & 1) ? av1 : av) + 512u * (d >> 1);
              if constexpr ((ag.tr & 1) == 0) lo = lds_tr(a); else vf[d] = cat(lo, lds_tr(a + 4096u)); }
            if constexpr (!(first && i == 0)) {
              if constexpr (ag.c == 'x') mx = max5(sp0[0], sp0[1], sp0[2], sp0[3], sp1[0]);
              if constexpr (ag.c == 'y') mx = max5(mx, sp1[1], sp1[2], sp1[3], sp1[3]);
              if constexpr (ag.c == 'z' && first) setref(IC<qs>{}, mx);
              if constexpr (ag.c == 'z' && !first) { big = __builtin_amdgcn_ballot_w64(mx > THR); SPIN(big); }
              if constexpr (ag.c == 'w' && !first) check(IC<qs>{}, big, mx);
              if constexpr (ag.ex >= 0 && ag.ex < 4) { sp0[ag.ex] = __builtin_amdgcn_exp2f(sp0[ag.ex]); OPAQUE(sp0[ag.ex]); }
              if constexpr (ag.ex >= 4 && ag.ex < 8) { sp1[ag.ex - 4] = __builtin_amdgcn_exp2f(sp1[ag.ex - 4]); OPAQUE(sp1[ag.ex - 4]); }
              if constexpr (ag.ex >= 8) { s0[ag.ex - 8] = __builtin_amdgcn_exp2f(s0[ag.ex - 8]); OPAQUE(s0[ag.ex - 8]); }
              if constexpr (ag.cv & 1) { pn[0] = cvtpk(sp0[0], sp0[1]); OPAQUE(pn[0]); }
              if constexpr (ag.cv & 2) { pn[1] = cvtpk(sp0[2], sp0[3]); OPAQUE(pn[1]); }
              if constexpr (ag.cv & 4) { pn[2] = cvtpk(sp1[0], sp1[1]); OPAQUE(pn[2]); }
              if constexpr (ag.cv & 8) { pn[3] = cvtpk(sp1[2], sp1[3]); OPAQUE(pn[3]); }
            }
            if constexpr (ag.pc >= 0) { constexpr int p = ag.pc;
              if constexpr (p < 8) dma<1, 1024 * (p & 3)>(vk[p & 3], sKs, p < 4 ? so : so2); else dma<1, 1024 * (p & 3)>(vv[p & 1], sVs, p < 12 ? so : so2); }
            if constexpr (ag.m0 >= 0) setm0(mk + ag.m0 * 1024u);
            if constexpr (ag.bk == 1) { so2 = so + 4096u; SPIN(so2); }
            if constexpr (ag.bk == 2) { av += dl; OPAQUE(av); av1 += dl; OPAQUE(av1); }
            if constexpr (ag.bk == 3) { ak += dl; OPAQUE(ak); }
            if constexpr (i == 6 && g == 16) LGKM0();
            GAP();
          });
          if constexpr (ATN >= 0 && !safe) asm volatile("s_nop %c0" ::"i"(ATN));
          if constexpr (!(first && i == 0)) pp = __builtin_bit_cast(bf16x8, pn);
          sp0 = s0; sp1 = s1;
        });
        so = kvb | ((so + 8192u) & 0x1ffffu); mk += dl; dl = 0u - dl;
      };
      ak -= 16384u;
      step(0u, IC<2>{});
      { float mx = max5(sp0[0], sp0[1], sp0[2], sp0[3], sp1[0]); mx = max5(mx, sp1[1], sp1[2], sp1[3], sp1[3]); setref(IC<6>{}, mx); GAP(); }
      unsigned s = 1;
#pragma unroll 1
      for (; s < nsafe; ++s) step(s, IC<1>{});
#pragma unroll 1
      for (; s < 16u; ++s) step(s, IC<0>{});
      { float mx = max5(sp0[0], sp0[1], sp0[2], sp0[3], sp1[0]); mx = max5(mx, sp1[1], sp1[2], sp1[3], sp1[3]);
        check(IC<6>{}, __builtin_amdgcn_ballot_w64(mx > THR), mx); }
      NOPS(1);
      sfor<9>([&](auto D) {
        mfa<7 * D + 5>(D < 8 ? vf[D & 7] : ones, pp);
        GAP();
        constexpr int d = D;
        if constexpr (d < 4) { sp0[d] = __builtin_amdgcn_exp2f(sp0[d]); OPAQUE(sp0[d]); } else if constexpr (d < 8) { sp1[d - 4] = __builtin_amdgcn_exp2f(sp1[d - 4]); OPAQUE(sp1[d - 4]); }
        GAP();
      });
      pp = pack2(sp0, sp1, 1.f); OPAQUE(pp);
      NOPS(1);
      mfa<62>(ones, pp);
      sfor<8>([&](auto D) { mfa<7 * D + 6>(vf[D], pp); });
      bool bad = false;
      sfor<7>([&](auto Q) { lsum[Q] = acc_rd1<56 + Q>(); bad |= !(lsum[Q] < 1.0995116e12f); });
      if (__builtin_expect(nsafe == 16u || __builtin_amdgcn_ballot_w64(bad) == 0ull, 1)) break;
      nsafe = 16u;
      waitv<0>();
      sfor<32>([&](auto P) { constexpr int p = P; if constexpr ((p & 3) == 0) { setm0(ring + p * 1024u); NOPS(0); }
        if constexpr ((p >> 3) & 1) dma<1, 1024 * (p & 3)>(vv[p & 1], sV, kvs(p >> 4) + ((p >> 2) & 1) * 4096u);
        else dma<1, 1024 * (p & 3)>(vk[p & 3], sK, kvs(p >> 4) + ((p >> 2) & 1) * 4096u); });
    }
  }
  STAMP(4);
  {
    const unsigned l = lane_id(), g4 = l >> 4, q15 = l & 15u, q4 = (l >> 2) & 3u, p4 = l & 3u, drow = (l >> 2) & 7u;
    srd_t sO = __builtin_amdgcn_make_buffer_rsrc((char*)Wo - 4096, (short)0, (int)(W_BYTES + 4096u), 0x00020000);
    srd_t sP = __builtin_amdgcn_make_buffer_rsrc((char*)Pws + F_BYTES, (short)0, (int)P_BYTES, 0x00020000);
    unsigned vo[4];
#pragma unroll
    for (int i = 0; i < 4; ++i) vo[i] = (hg * 512u + 8u * i + drow) * 1280u + (w & 1u) * 640u + 64u * (l >> 5) + 16u * (p4 ^ (2u * (drow >> 2))) + 4096u - 1024u * i;
    const unsigned slot0 = ring + 28672u;
    const unsigned rot8 = (rot * 13u) >> 4, sg0 = early ? 0u : 8u;
    auto cchunk = [&](unsigned sg) { return (unsigned)(0x1023443210ull >> (4u * sg)) & 7u; };
    auto sbase = [&](unsigned sg) { return ((0x7e0u >> sg) & 1u) * 327680u + cchunk(sg) * 128u; };
    auto allO = [&](unsigned u) { setm0(slot0 + (u % 3u) * 4096u); NOPS(0); sfor<4>([&](auto P) { dma<0, 1024 * P>(vo[P], sO, sbase(sg0) + ((u + rot8) & 7u) * 40960u); }); };
    waitv<0>();
    wait_ge(fl + 16u + (l & 1u) * 4u, 11u);
    allO(0); allO(1);
    sfor<7>([&](auto Q) {
      constexpr int q = Q;
      const float inv = __builtin_amdgcn_rcpf(lsum[q]);
      sfor<4>([&](auto K) { constexpr int k = K; *(LAS bf16x8*)(size_t)(ring + l * 16u + (k * 7 + q) * 1024u) = pack2(acc_rd<14 * k + q>(), acc_rd<14 * k + 7 + q>(), inv); });
    });
    unsigned ba = slot0 + 1024u * (g4 >> 1) + 64u * (4u * (g4 & 1u) + q4) + 16u * ((p4 >> 1) ^ (2u * (g4 & 1u))) + 8u * (p4 & 1u), bb = ldsb + l * 16u;
    OPAQUE(ba); OPAQUE(bb);
    bf16x8 af[2][4], bf[2][7];
    if (early) { lds_st(fl + w * 4u, 1u); wait_ge(fl + (l & 1u) * 4u, 1u); }
    else { DBGX(STAMP(5);) LGKM0(); BAR(); DBGX(STAMP(6);) }
    allO(2);
    waitv<8>();
#pragma unroll
    for (int n = 0; n < 4; ++n) { const unsigned a = (ba ^ (32u * (n & 1))) + 512u * (n >> 1); af[0][n] = lds_tr2(a, a + 2048u); }
    LGKM0(); asm volatile("" ::: "memory");
    allO(3);
    unsigned k1 = (1u + rot8) & 7u, o3 = ((4u + rot8) & 7u) * 40960u, sl = 4096u;
    unsigned aa = ba + 4096u, aa1 = aa ^ 32u;
    OPAQUE(aa); OPAQUE(aa1);
    unsigned bs = bb; bool pfw = false;
    auto firstB = [&]() { const unsigned ab = bs + rot8 * 7168u + (rot8 & 4u) * 3072u;
#pragma unroll
      for (int q = 0; q < 7; ++q) bf[0][q] = lds_b128(ab + 1024u * q); };
    f32x4 oa[28];
    sfor<28>([&](auto T) { f32x4 u; asm volatile("" : "=v"(u)); oa[T] = u; });
    auto unpack = [&](auto R, auto&& before) {
      const unsigned ll = lane_id(), one = (ll >> 4) == ((ll >> 2) & 3u) ? (ll & 1u ? 0x3f800000u : 0x00003f80u) : 0u, lo = (ll & 2u) ? 0u : one, hi = (ll & 2u) ? one : 0u;
      bf16x8 sel[2] = {__builtin_bit_cast(bf16x8, u32x4{lo, hi, 0u, 0u}), __builtin_bit_cast(bf16x8, u32x4{0u, 0u, lo, hi})};
      OPAQUE(sel[0]); OPAQUE(sel[1]); NOPS(1);
      sfor<14>([&](auto T) { before(T); GAP(); mfv0a<R + T>(oa[2 * T], sel[0]); mfv0a<R + T>(oa[2 * T + 1], sel[1]); GAP(); });
    };
    auto packed = [&](auto T) { return pack2(oa[2 * T], oa[2 * T + 1], 1.f); };
    auto gput = [&](unsigned idx, u64 val) {
      asm volatile("s_mov_b64 exec, 1\n\tglobal_store_dwordx2 %0, %1, %2 sc1\n\ts_mov_b64 exec, -1" ::"v"(idx * 8u), "v"(val), "s"(gfl) : "memory"); };
    auto pairU = [&](unsigned i, unsigned sc, unsigned sn, auto ZERO, auto AFTER, auto THROT) {
      {
        sfor<2>([&](auto E) {
          constexpr int e = E;
          unsigned ab = 0, ab2 = 0, so = 0; bf16x4 lo;
          sfor<28>([&](auto T) {
            constexpr int t = T, q = t / 4, n = t % 4;
            if constexpr (ZERO && e == 0) mfv00t(oa[t], af[e][n], bf[e][q]); else mfv(oa[t], af[e][n], bf[e][q]);
            GAP();
            if constexpr (t == 1) { aa1 = aa ^ 32u; OPAQUE(aa1); }
            if constexpr (t == 0) {
              if constexpr (AFTER) asm volatile("s_cmp_lt_u32 %0, %1\n\ts_cbranch_scc1 1f\n\ts_waitcnt vmcnt(8)\n1:\n\ts_waitcnt vmcnt(22)" ::"s"(i), "i"(2 - e) : "scc", "memory"); else waitv<8>();
            }
            if constexpr (t >= 1 && t <= 8) { constexpr int j = t - 1; const unsigned a = ((j & 2) ? aa1 : aa) + 512u * (j >> 2);
              if constexpr ((j & 1) == 0) lo = lds_tr(a); else af[e ^ 1][j >> 1] = cat(lo, lds_tr(a + 2048u)); }
            if constexpr (t == 9) { ab2 = (k1 & 4u) * 3072u; SPIN(ab2); }
            if constexpr (t == 10) { ab = k1 * 7168u + ab2; SPIN(ab); }
            if constexpr (t == 11) { ab += bs; OPAQUE(ab); setm0(slot0 + sl); }
            if constexpr (t == 12) { so = (i >> 1) * sn; SPIN(so); }
            if constexpr (t == 13) { so += o3 + sc; SPIN(so); LGKM0(); asm volatile("" ::: "memory"); }
            if constexpr ((t == 14 || t == 17 || t == 20 || t == 23) && HO < 63 && THROT) waitv<HO - 1>();
            if constexpr (t == 14 || t == 17 || t == 20 || t == 23) dma<0, 1024 * ((t - 14) / 3)>(vo[(t - 14) / 3], sO, so);
            if constexpr (t >= 15 && t <= 24 && (t - 14) % 3 != 0) { constexpr int qq = (t - 15) - (t - 14) / 3; bf[e ^ 1][qq] = lds_b128(ab + 1024u * qq); }
            if constexpr (t == 25) { sl = sl == 8192u ? 0u : sl + 4096u; SPIN(sl); }
            if constexpr (t == 26) { aa = ba + sl; OPAQUE(aa); k1 = (k1 + 1u) & 7u; SPIN(k1); }
            if constexpr (t == 27) { o3 = o3 == 286720u ? 0u : o3 + 40960u; SPIN(o3); }
            GAP();
          });
        });
      }
    };
    if (early) {
      firstB(); LGKM0();
      pfw = true;
#pragma unroll 1
      for (unsigned sg = 0; sg < 5u; ++sg) {
        const unsigned sc = sbase(sg), sn = sbase(sg + 1u) - sc;
        pairU(0u, sc, sn, std::true_type{}, std::false_type{}, std::true_type{});
#pragma unroll 1
        for (unsigned i = 1; i < 4; ++i) pairU(i, sc, sn, std::false_type{}, std::false_type{}, std::true_type{});
        NOPS(7); GAP();
        if (pfw && sg == 2u) {
          constexpr unsigned NBX = 128 / NHX, PER = 2560u / NBX / 2u;
          unsigned dummy = 0;
#pragma unroll
          for (unsigned k = 0; k < (PER + 63u) / 64u; ++k) {
            const unsigned li = k * 64u + lane_id();
            asm volatile("buffer_load_dword %0, %1, %2, 0 offen" : "+v"(dummy) : "v"(li < PER ? (hg * 512u + 256u) * 1280u + (((b % NBX) * 2u + w) * PER + li) * 128u + 4096u : NONE), "s"(sO) : "memory");
          }
          waitv<0>(); asm volatile("" :: "v"(dummy));
        }
        sfor<4>([&](auto C) { if (sg == C) sfor<14>([&](auto T) { acc_wr<14 * C + T>(__builtin_bit_cast(f32x4, packed(T))); }); });
      }
      lds_st(fl + w * 4u, 2u); wait_ge(fl + (lane_id() & 1u) * 4u, 2u);
      const unsigned a = bb + w * REG;
      sfor<28>([&](auto T) { acc_lds_wr<T, T * 1024>(a); });
      DBGX(STAMP(5);)
      asm volatile("s_waitcnt lgkmcnt(0)" ::: "memory"); BAR();
      DBGX(STAMP(6);)
    }
    unsigned w2 = w; SPIN(w2);
    if (w2 >= 2u) {
      const unsigned a = bb + (w & 1u) * REG;
      sfor<28>([&](auto T) { acc_lds_rd<28 + T, T * 1024>(a); });
      asm volatile("s_waitcnt lgkmcnt(0)" ::: "memory");
      unpack(IC<28>{}, [](auto) {});
    }
    bs = bb + 2u * REG; OPAQUE(bs);
    firstB();
    LGKM0();
    auto lastseg = [&](unsigned sg, auto AFTER, auto NEXT, auto J) {
      const unsigned sc = sbase(sg), sn = sbase(sg + 1u) - sc;
#pragma unroll 1
      for (unsigned i = 0; i < 4; ++i) {
        pairU(i, sc, sn, std::false_type{}, AFTER, std::false_type{});
      }
      NOPS(7); GAP();
      const unsigned ch = (w & 1u) * 5u + cchunk(sg), qs = (hg + ch) & 7u, po = ((b * 10u + ch) * 8u + hg) * P_UNIT + lane_id() * 16u;
      u32x4 keep[2] = {};
      auto put = [&](auto T) {
        const u32x4 v = __builtin_bit_cast(u32x4, packed(T));
        const bool own = qs == (unsigned)(T / 2);
        keep[T & 1] = own ? v : keep[T & 1];
        __builtin_amdgcn_raw_buffer_store_b128(v, sP, po + (own ? NONE : T * 1024u), 0, 16);
      };
      if constexpr (NEXT >= 0) unpack(NEXT, put); else sfor<14>(put);
      acc_wr<56 + 2 * J>(__builtin_bit_cast(f32x4, keep[0])); acc_wr<57 + 2 * J>(__builtin_bit_cast(f32x4, keep[1]));
      GAP();
    };
    lastseg(early ? 5u : 8u, std::false_type{}, IC<42>{}, IC<0>{});
    lastseg(early ? 6u : 9u, std::true_type{}, IC<28>{}, IC<1>{});
    if (early) lastseg(7u, std::true_type{}, IC<-1>{}, IC<2>{});
    waitv<0>();
    DBGX(STAMP(7);)
    BAR();
    const unsigned ll = lane_id();
    if (w == 0u) gput(hg, ep + 1ull);
    srd_t sOut = __builtin_amdgcn_make_buffer_rsrc(Out, (short)0, (int)X_BYTES, 0x00020000);
    u32x4 v[3][2][8];
    unsigned oo[3], pi[3];
    sfor<3>([&](auto J) {
      constexpr int j = J;
      const unsigned ch = (w & 1u) * 5u + cchunk((early ? 5u : 8u) + j), q = (hg + ch) & 7u;
      const bool ok = q < 7u && (j < 2 || early);
      pi[j] = ok ? (b * 10u + ch) * 8u * P_UNIT + q * 2048u + ll * 16u : NONE;
      oo[j] = ok ? (b * 112u + q * 16u + (ll & 15u)) * 1280u + ch * 128u + (ll >> 4) * 16u : NONE;
      v[j][0][0] = __builtin_bit_cast(u32x4, acc_rd<56 + 2 * j>()); v[j][1][0] = __builtin_bit_cast(u32x4, acc_rd<57 + 2 * j>());
    });
    unsigned rdy = 0, sp = 0;
#pragma unroll
    for (int k = 1; k < 8; ++k) {
      const unsigned src = (hg + k) & 7u;
      while (!((rdy >> src) & 1u) && ++sp < SPIN_CAP) {
        rdy = (unsigned)__builtin_amdgcn_ballot_w64(__hip_atomic_load(gfl + (ll & 7u), __ATOMIC_RELAXED, __HIP_MEMORY_SCOPE_AGENT) == ep + 1ull);
        if (!((rdy >> src) & 1u)) __builtin_amdgcn_s_sleep(2);
      }
      asm volatile("" ::: "memory");
#pragma unroll
      for (int j = 0; j < 3; ++j) { v[j][0][k] = __builtin_amdgcn_raw_buffer_load_b128(sP, pi[j] + src * P_UNIT, 0, 16); v[j][1][k] = __builtin_amdgcn_raw_buffer_load_b128(sP, pi[j] + src * P_UNIT + 1024u, 0, 16); }
    }
    if (hg == 0u && w == 0u) gput(32u, ep + 1ull);
#pragma unroll
    for (int j = 0; j < 3; ++j) {
      if (j < 2 || early) {
#pragma unroll
        for (int r = 0; r < 2; ++r) {
          u32x4 o;
#pragma unroll
          for (int e = 0; e < 4; ++e) {
            float lo = __uint_as_float(v[j][r][0][e] << 16), hi = __uint_as_float(v[j][r][0][e] & 0xffff0000u);
#pragma unroll
            for (int k = 1; k < 8; ++k) { lo += __uint_as_float(v[j][r][k][e] << 16); hi += __uint_as_float(v[j][r][k][e] & 0xffff0000u); }
            o[e] = cvtpk(lo, hi);
          }
          __builtin_amdgcn_raw_buffer_store_b128(regroup(o), sOut, oo[j] + r * 64u, 0, 0);
        }
      }
    }
  }
  asm volatile(".Lgqa_code_end:");
}

extern "C" void kernel_launch(void* const* d_in, const int* in_sizes, int n_in, void* d_out, int out_size, void* d_ws, size_t ws_size,
                              hipStream_t stream) {
  (void)in_sizes; (void)n_in; (void)out_size; (void)ws_size;
  (void)hipFuncSetAttribute((const void*)fused_k, hipFuncAttributeMaxDynamicSharedMemorySize, (int)LDS_ALL);
  fused_k<<<256, 256, LDS_ALL, stream>>>(d_in[0], d_in[1], d_in[2], d_in[3], d_in[4], d_ws, d_out);
}
```
